```python
import torch

def _softmax(x, axis=-1):
    return torch.softmax(x, dim=axis)

def _gelu(x):
    return torch.nn.functional.gelu(x, approximate='tanh')

def _silu(x):
    return torch.nn.functional.silu(x)

def _sigmoid(x):
    return torch.sigmoid(x)

def _layernorm(x, axis=-1, eps=1e-05):
    m = x.mean(dim=axis, keepdim=True)
    v = x.var(dim=axis, keepdim=True, unbiased=False)
    return (x - m) / torch.sqrt(v + eps)

def _scatter_add(tgt, idx, src, axis=0):
    return tgt.clone().index_add_(axis, idx, src)

def _segment_sum(src, seg, n):
    out = torch.zeros((n,) + src.shape[1:], dtype=src.dtype)
    return out.index_add_(0, seg, src)

def _pad(x, pw):
    flat = tuple((v for pair in reversed(pw) for v in pair))
    return torch.nn.functional.pad(x, flat)
kernel_name = 'adaLN_cond__ssm_conv_s4d__dense_activation_silu__prompt'

def setup_inputs():

    _w = lambda a: (lambda t: t.to(torch.bfloat16) if t.is_floating_point() else t)(torch.as_tensor(a))

    def _A_setup():
        Bsz, L, D, N = (1, 2048, 576, 128)
        return {'u': _w((lambda *s: torch.randn(*s, dtype=torch.bfloat16) * 0.05)(Bsz, L, D)), 'log_A_real': _w((lambda *s: torch.randn(*s, dtype=torch.bfloat16) * 3.2)(D, N) * 0.5), 'A_imag': _w((lambda *s: torch.randn(*s, dtype=torch.bfloat16) * 0.05)(D, N) * 3.14), 'C_real': _w((lambda *s: torch.randn(*s, dtype=torch.bfloat16) * 0.2)(D, N) / N ** 0.5), 'C_imag': _w((lambda *s: torch.randn(*s, dtype=torch.bfloat16) * 0.2)(D, N) / N ** 0.5), 'log_dt': _w((lambda *s: torch.randn(*s, dtype=torch.bfloat16) * 0.05)(D) * 0.5 - 4.0), 'D_skip': _w((lambda *s: torch.randn(*s, dtype=torch.bfloat16) * 0.1)(D))}
    _A_in = _A_setup()

    def _B_setup():
        B, S, D, F = (1, 2048, 576, 4608)
        return {'x': _w((lambda *s: torch.randn(*s, dtype=torch.bfloat16) * 0.05)(B, S, D)), 'w1': _w((lambda *s: torch.randn(*s, dtype=torch.bfloat16) * 3.2)(D, F) * D ** (-0.5)), 'w2': _w((lambda *s: torch.randn(*s, dtype=torch.bfloat16) * 0.4)(F, D) * F ** (-0.5))}
    _B_in = _B_setup()
    return {'A_u': _A_in['u'], 'A_log_A_real': _A_in['log_A_real'], 'A_A_imag': _A_in['A_imag'], 'A_C_real': _A_in['C_real'], 'A_C_imag': _A_in['C_imag'], 'A_log_dt': _A_in['log_dt'], 'A_D_skip': _A_in['D_skip'], 'B_x': _B_in['x'], 'B_w1': _B_in['w1'], 'B_w2': _B_in['w2'], 'c': _w((lambda *s: torch.randn(*s, dtype=torch.bfloat16) * 0.2)(1, 576) * 8.0), 'w_mod': _w((lambda *s: torch.randn(*s, dtype=torch.bfloat16) * 0.2)(576, 6 * 576) * 576 ** (-0.5) * 8.0)}

def reference(A_u, A_log_A_real, A_A_imag, A_C_real, A_C_imag, A_log_dt, A_D_skip, B_x, B_w1, B_w2, c, w_mod):

    def _A_ref(u, log_A_real, A_imag, C_real, C_imag, log_dt, D_skip):
        L = u.shape[1]
        dt = torch.exp(log_dt)
        A = -torch.exp(log_A_real) + 1j * A_imag
        C = (C_real + 1j * C_imag) * dt[:, None]
        pos = (lambda x, dt: x.to(dt))(torch.arange(L), log_dt.dtype)
        P = torch.exp((A * dt[:, None])[:, :, None] * pos[None, None, :])
        K = 2.0 * torch.einsum('dn,dnl->dl', C, P).real
        L2 = 2 * L
        u_t = (lambda x, *d: x.permute(*d))(u, 0, 2, 1)
        Kf = (lambda x, *a, **k: torch.fft.rfft(x.float(), *a, **k))(K, L2)
        Uf = (lambda x, *a, **k: torch.fft.rfft(x.float(), *a, **k))(u_t, L2)
        y = (lambda x, *a, **k: torch.fft.irfft(x, *a, **k).to(torch.bfloat16))(Uf * Kf[None, :, :], L2)[:, :, :L]
        y = (lambda x, *d: x.permute(*d))(y, 0, 2, 1)
        return y + u * D_skip[None, None, :]

    def _B_ref(x, w1, w2):
        h = torch.einsum('bsd,df->bsf', x, w1)
        h = _silu(h)
        out = torch.einsum('bsf,fd->bsd', h, w2)
        return out
    x = A_u
    mod = (_silu(c) @ w_mod).reshape(c.shape[0], 6, 1, -1)
    sa, ca, ga, sb, cb, gb = (mod[:, 0].clone(), mod[:, 1].clone(), mod[:, 2].clone(), mod[:, 3].clone(), mod[:, 4].clone(), mod[:, 5].clone())
    h = _layernorm(x, axis=-1) * (1 + ca) + sa
    x = x + ga * _A_ref(h, A_log_A_real, A_A_imag, A_C_real, A_C_imag, A_log_dt, A_D_skip)
    h = _layernorm(x, axis=-1) * (1 + cb) + sb
    x = x + gb * _B_ref(h, B_w1, B_w2)
    return x
```

```cpp
#include <hip/hip_runtime.h>
#include <hip/hip_bf16.h>
#include <stdint.h>

typedef unsigned short u16;
constexpr int L = 2048, D = 576, NS = 128, F = 4608;

__device__ __forceinline__ float bf2f(u16 v) { return __uint_as_float(((unsigned)v) << 16); }
__device__ __forceinline__ u16 f2bf(float f) {
    unsigned u = __float_as_uint(f);
    u += 0x7fffu + ((u >> 16) & 1u);
    return (u16)(u >> 16);
}
__device__ __forceinline__ float rbf(float f) { return bf2f(f2bf(f)); }

__global__ void k_mod(const u16* c, const u16* wmod, float* mod) {
    int j = blockIdx.x * blockDim.x + threadIdx.x;
    if (j >= 6 * D) return;
    float acc = 0.f;
    for (int k = 0; k < D; ++k) {
        float cv = bf2f(c[k]);
        float s = rbf(cv / (1.f + __expf(-cv)));
        acc += s * bf2f(wmod[(size_t)k * 6 * D + j]);
    }
    mod[j] = rbf(acc);
}

__global__ void k_ln(const u16* x, const float* sc, const float* sh, u16* h) {
    int t = blockIdx.x, lane = threadIdx.x;
    float v[9];
    float s = 0.f;
    for (int i = 0; i < 9; ++i) { v[i] = bf2f(x[(size_t)t * D + lane + 64 * i]); s += v[i]; }
    for (int m = 32; m >= 1; m >>= 1) s += __shfl_xor(s, m);
    float mean = s / D;
    float q = 0.f;
    for (int i = 0; i < 9; ++i) { float d = v[i] - mean; q += d * d; }
    for (int m = 32; m >= 1; m >>= 1) q += __shfl_xor(q, m);
    float var = rbf(q / D);
    float mb = rbf(mean);
    float sd = rbf(sqrtf(rbf(var + 1e-5f)));
    for (int i = 0; i < 9; ++i) {
        int d = lane + 64 * i;
        float n = rbf(rbf(v[i] - mb) / sd);
        float hh = rbf(rbf(n * rbf(1.f + sc[d])) + sh[d]);
        h[(size_t)t * D + d] = f2bf(hh);
    }
}

__global__ void k_kgen(const u16* logA, const u16* Aim, const u16* Cr, const u16* Ci, const u16* logdt, float* K) {
    int d = blockIdx.y;
    int l = blockIdx.x * blockDim.x + threadIdx.x;
    float dt = rbf(expf(bf2f(logdt[d])));
    float pos = rbf((float)l);
    float acc = 0.f;
    for (int n = 0; n < NS; ++n) {
        float ar = -rbf(expf(bf2f(logA[d * NS + n])));
        float ai = bf2f(Aim[d * NS + n]);
        float cr = bf2f(Cr[d * NS + n]) * dt, ci = bf2f(Ci[d * NS + n]) * dt;
        float zr = ar * dt, zi = ai * dt;
        float e = expf(zr * pos);
        float sn, cs;
        sincosf(zi * pos, &sn, &cs);
        acc += e * (cr * cs - ci * sn);
    }
    K[(size_t)d * L + l] = 2.f * acc;
}

__global__ void k_conv(const u16* x, const u16* h, const float* K, const u16* Dskip, const float* ga, u16* x1) {
    int d = blockIdx.y;
    __shared__ float sk[L];
    __shared__ float su[L];
    for (int i = threadIdx.x; i < L; i += blockDim.x) { sk[i] = K[(size_t)d * L + i]; su[i] = bf2f(h[(size_t)i * D + d]); }
    __syncthreads();
    int t = blockIdx.x * blockDim.x + threadIdx.x;
    float acc = 0.f;
    for (int j = 0; j <= t; ++j) acc += sk[j] * su[t - j];
    float y = rbf(acc);
    float z = rbf(y + rbf(su[t] * bf2f(Dskip[d])));
    float r = rbf(bf2f(x[(size_t)t * D + d]) + rbf(ga[d] * z));
    x1[(size_t)t * D + d] = f2bf(r);
}

template <int MODE>
__global__ void k_gemm(const u16* A, const u16* B, u16* C, int M, int N, int K, const u16* x1, const float* gb) {
    __shared__ float sa[16][17], sb[16][17];
    int tx = threadIdx.x, ty = threadIdx.y;
    int row = blockIdx.y * 16 + ty, col = blockIdx.x * 16 + tx;
    float acc = 0.f;
    for (int k0 = 0; k0 < K; k0 += 16) {
        sa[ty][tx] = bf2f(A[(size_t)row * K + k0 + tx]);
        sb[ty][tx] = bf2f(B[(size_t)(k0 + ty) * N + col]);
        __syncthreads();
        for (int k = 0; k < 16; ++k) acc += sa[ty][k] * sb[k][tx];
        __syncthreads();
    }
    if (MODE == 0) {
        float v = rbf(acc);
        float s = v / (1.f + __expf(-v));
        C[(size_t)row * N + col] = f2bf(s);
    } else {
        float v = rbf(acc);
        float r = bf2f(x1[(size_t)row * N + col]) + rbf(gb[col] * v);
        C[(size_t)row * N + col] = f2bf(r);
    }
}

extern "C" void kernel_launch(void* const* d_in, const int* in_sizes, int n_in,
                              void* d_out, int out_size, void* d_ws, size_t ws_size,
                              hipStream_t stream) {
    const u16* u = (const u16*)d_in[0];
    const u16* logA = (const u16*)d_in[1];
    const u16* Aim = (const u16*)d_in[2];
    const u16* Cr = (const u16*)d_in[3];
    const u16* Ci = (const u16*)d_in[4];
    const u16* logdt = (const u16*)d_in[5];
    const u16* Dskip = (const u16*)d_in[6];
    const u16* w1 = (const u16*)d_in[8];
    const u16* w2 = (const u16*)d_in[9];
    const u16* c = (const u16*)d_in[10];
    const u16* wmod = (const u16*)d_in[11];
    char* ws = (char*)d_ws;
    float* mod = (float*)ws;
    u16* h = (u16*)(ws + (1 << 16));
    float* K = (float*)(ws + (4 << 20));
    u16* x1 = (u16*)(ws + (12 << 20));
    u16* h2 = (u16*)(ws + (16 << 20));
    u16* H = (u16*)(ws + (20 << 20));
    k_mod<<<(6 * D + 63) / 64, 64, 0, stream>>>(c, wmod, mod);
    k_ln<<<L, 64, 0, stream>>>(u, mod + D, mod, h);
    k_kgen<<<dim3(L / 256, D), 256, 0, stream>>>(logA, Aim, Cr, Ci, logdt, K);
    k_conv<<<dim3(L / 256, D), 256, 0, stream>>>(u, h, K, Dskip, mod + 2 * D, x1);
    k_ln<<<L, 64, 0, stream>>>(x1, mod + 4 * D, mod + 3 * D, h2);
    k_gemm<0><<<dim3(F / 16, L / 16), dim3(16, 16), 0, stream>>>(h2, w1, H, L, F, D, nullptr, nullptr);
    k_gemm<1><<<dim3(D / 16, L / 16), dim3(16, 16), 0, stream>>>(H, w2, (u16*)d_out, L, D, F, x1, mod + 5 * D);
}
```

```cpp
#include <hip/hip_runtime.h>
#include <stdint.h>

typedef unsigned short u16;
typedef unsigned int u32;
typedef float f32x2 __attribute__((ext_vector_type(2)));
typedef float f32x4 __attribute__((ext_vector_type(4)));
typedef float f32x16 __attribute__((ext_vector_type(16)));
typedef _Float16 f16x2 __attribute__((ext_vector_type(2)));
typedef _Float16 f16x8 __attribute__((ext_vector_type(8)));
typedef __bf16 bf16x2 __attribute__((ext_vector_type(2)));
typedef __bf16 bf16x8 __attribute__((ext_vector_type(8)));
typedef short s16x4 __attribute__((ext_vector_type(4)));
typedef u32 u32x2 __attribute__((ext_vector_type(2)));
typedef u32 u32x4 __attribute__((ext_vector_type(4)));
#define LAS __attribute__((address_space(3)))
#define DI __device__ __forceinline__

constexpr int SEQ = 2048, DM = 576, NS = 128, FF = 4608;

constexpr u32 WS_MOD   = 0;
constexpr u32 WS_STAT  = 16384;
constexpr u32 WS_KSC   = 32768;
constexpr u32 WS_FLAG  = 36864;
constexpr u32 WS_SB    = 65536;
constexpr u32 WS_XT    = 1u << 20;
constexpr u32 WS_KR    = WS_XT + 2359296;
constexpr u32 WS_X1F   = WS_KR + 2359296;
constexpr u32 WS_W1F   = WS_X1F + 2359296;
constexpr u32 WS_W2F   = WS_W1F + 5308416;
constexpr u32 WS_HF    = WS_W2F + 5308416;
constexpr u32 WS_PART  = WS_HF + 18874368;
constexpr u32 WS_END   = WS_PART + 18874368;

DI float bflo(u32 u) { return __uint_as_float(u << 16); }
DI float bfhi(u32 u) { return __uint_as_float(u & 0xffff0000u); }
DI float bf2f(u16 v) { return __uint_as_float(((u32)v) << 16); }
DI u32 pkbf(float a, float b) { f32x2 v = {a, b}; return __builtin_bit_cast(u32, __builtin_convertvector(v, bf16x2)); }
DI u32 pkhf(float a, float b) { f32x2 v = {a, b}; return __builtin_bit_cast(u32, __builtin_convertvector(v, f16x2)); }
DI float rbf(float a) { return bflo(pkbf(a, a)); }
template <int CTRL> DI float dppf(float v) {
    return __builtin_bit_cast(float, __builtin_amdgcn_update_dpp(0, __builtin_bit_cast(int, v), CTRL, 0xf, 0xf, true));
}
DI s16x4 trread(const char* smem, u32 off) {
    return __builtin_amdgcn_ds_read_tr16_b64_v4i16((LAS s16x4*)(smem + off));
}
DI u32 xswz(u32 row) { return ((row & 3) << 2) | ((row >> 2) & 3); }
DI float pow2i(int e) { e = e < -126 ? -126 : (e > 127 ? 127 : e); return __uint_as_float((u32)(e + 127) << 23); }
DI int expo(float x) { return (int)((__float_as_uint(x) >> 23) & 255u) - 127; }

DI u32 imgrow(u32 row) { return row * 1184u + (row >> 3) * 128u; }

constexpr int NB_LN = 64, NB_GEMV = 108, NB_KGEN = 576, NB_W2 = 144;
constexpr int NB_PREP = NB_LN + NB_GEMV + NB_KGEN + NB_W2;
constexpr int PREP_LDS = 38400;

__global__ __launch_bounds__(256) void k_prep(const u16* __restrict__ x, const u16* __restrict__ logA,
                                              const u16* __restrict__ Aim, const u16* __restrict__ Cre,
                                              const u16* __restrict__ Cim, const u16* __restrict__ logdt,
                                              const u16* __restrict__ w2, const u16* __restrict__ cvec,
                                              const u16* __restrict__ wmod, char* __restrict__ ws) {
    extern __shared__ __attribute__((aligned(16))) char smem[];
    const int tid = threadIdx.x, lane = tid & 63;
    const int wv = __builtin_amdgcn_readfirstlane(tid >> 6);
    int b = blockIdx.x;
    if (b < NB_LN) {
        if (b == 0) {
            u32x4 z = {0u, 0u, 0u, 0u};
            *(u32x4*)(ws + WS_FLAG + tid * 16) = z;
            *(u32x4*)(ws + WS_FLAG + 4096 + tid * 16) = z;
        }
        const int t0 = b * 32, tok = tid >> 3, sub = tid & 7;
        u32x4 v[9];
#pragma unroll
        for (int i = 0; i < 9; ++i) v[i] = *(const u32x4*)(x + (size_t)(t0 + tok) * DM + (sub + 8 * i) * 8);
        float s = 0.f;
#pragma unroll
        for (int i = 0; i < 9; ++i) {
            *(u32x4*)(smem + imgrow(tok) + (sub + 8 * i) * 16) = v[i];
#pragma unroll
            for (int k = 0; k < 4; ++k) s += bflo(v[i][k]) + bfhi(v[i][k]);
        }
        s += dppf<0xB1>(s); s += dppf<0x4E>(s); s += dppf<0x141>(s);
        const float mean = s * (1.f / DM);
        float q = 0.f;
#pragma unroll
        for (int i = 0; i < 9; ++i)
#pragma unroll
            for (int k = 0; k < 4; ++k) {
                float a = bflo(v[i][k]) - mean, c = bfhi(v[i][k]) - mean;
                q = fmaf(a, a, q); q = fmaf(c, c, q);
            }
        q += dppf<0xB1>(q); q += dppf<0x4E>(q); q += dppf<0x141>(q);
        if (sub == 0) {
            float var = rbf(q * (1.f / DM));
            float sd = rbf(sqrtf(rbf(var + 1e-5f)));
            f32x2 st = {rbf(mean), 1.f / sd};
            *(f32x2*)(ws + WS_STAT + (t0 + tok) * 8) = st;
        }
        __syncthreads();
        const int g = lane >> 4, qq = (lane & 15) >> 2, p = lane & 3;
        u16* xT = (u16*)(ws + WS_XT);
#pragma unroll
        for (int i = 0; i < 9; ++i) {
            int cb = wv * 9 + i;
            s16x4 lo = trread(smem, imgrow(8 * g + qq) + cb * 32 + p * 8);
            s16x4 hi = trread(smem, imgrow(8 * g + 4 + qq) + cb * 32 + p * 8);
            u32x2 l2 = __builtin_bit_cast(u32x2, lo), h2 = __builtin_bit_cast(u32x2, hi);
            u32x4 o = {l2[0], l2[1], h2[0], h2[1]};
            *(u32x4*)(xT + (size_t)(cb * 16 + (lane & 15)) * SEQ + t0 + 8 * g) = o;
        }
        return;
    }
    b -= NB_LN;
    if (b < NB_GEMV) {
        const int seg = tid & 3, rg = tid >> 2;
        u32x4 v[9]; u16 cv[9];
#pragma unroll
        for (int i = 0; i < 9; ++i) {
            int row = rg + 64 * i;
            v[i] = *(const u32x4*)(wmod + (size_t)row * (6 * DM) + b * 32 + seg * 8);
            cv[i] = cvec[row];
        }
        float acc[8];
#pragma unroll
        for (int k = 0; k < 8; ++k) acc[k] = 0.f;
#pragma unroll
        for (int i = 0; i < 9; ++i) {
            float c = bf2f(cv[i]);
            float sl = rbf(c / (1.f + __expf(-c)));
#pragma unroll
            for (int k = 0; k < 4; ++k) {
                acc[2 * k] = fmaf(sl, bflo(v[i][k]), acc[2 * k]);
                acc[2 * k + 1] = fmaf(sl, bfhi(v[i][k]), acc[2 * k + 1]);
            }
        }
        float* red = (float*)smem;
        *(f32x4*)(red + rg * 32 + seg * 8) = (f32x4){acc[0], acc[1], acc[2], acc[3]};
        *(f32x4*)(red + rg * 32 + seg * 8 + 4) = (f32x4){acc[4], acc[5], acc[6], acc[7]};
        __syncthreads();
        const int col = tid & 31, part = tid >> 5;
        float s = 0.f;
#pragma unroll
        for (int i = 0; i < 8; ++i) s += red[(part * 8 + i) * 32 + col];
        __syncthreads();
        red[part * 32 + col] = s;
        __syncthreads();
        if (tid < 32) {
            float t = 0.f;
#pragma unroll
            for (int i = 0; i < 8; ++i) t += red[i * 32 + tid];
            ((float*)(ws + WS_MOD))[b * 32 + tid] = rbf(t);
        }
        return;
    }
    b -= NB_GEMV;
    if (b < NB_KGEN) {
        const int d = b, hn = wv & 1, grp = wv >> 1;
        const int n = hn * 64 + lane, n2 = n ^ 64;
        const float la = bf2f(logA[d * NS + n]), ai = bf2f(Aim[d * NS + n]);
        const float cr = bf2f(Cre[d * NS + n]), ci = bf2f(Cim[d * NS + n]);
        const float cr2 = bf2f(Cre[d * NS + n2]), ci2 = bf2f(Cim[d * NS + n2]);
        const float dt = rbf(__expf(bf2f(logdt[d])));
        const float zr = -rbf(__expf(la)) * dt, zi = ai * dt;
        float bs = fabsf(cr) + fabsf(ci) + fabsf(cr2) + fabsf(ci2);
#pragma unroll
        for (int m = 32; m >= 1; m >>= 1) bs += __shfl_xor(bs, m);
        const float bound = 2.f * dt * bs * 1.0001f;
        const int se = 14 - expo(bound);
        const float scale = pow2i(se > 100 ? 100 : se);
        const float zr2 = zr * 1.44269504088896340736f;
        auto cexp = [&](float p, float& re, float& im) {
            float mag = __builtin_amdgcn_exp2f(zr2 * p);
            float ang = zi * p;
            float t = ang * 0.15915494309189535f;
            float rr = __builtin_rintf(t);
            float fr = fmaf(ang, 0.15915494309189535f, -rr);
            fr = fmaf(ang, 6.4206382e-9f, fr);
            re = mag * __builtin_amdgcn_cosf(fr);
            im = mag * __builtin_amdgcn_sinf(fr);
        };
        const float p1 = grp ? 4.f : 1.f;
        float f1r, f1i, c1r, c1i, s0r, s0i, s1r, s1i;
        cexp(p1, f1r, f1i);
        cexp(16.f * p1, c1r, c1i);
        cexp(grp ? 512.f : 0.f, s0r, s0i);
        cexp(256.f * p1, s1r, s1i);
        const float er = 2.f * dt * scale * cr, ei = 2.f * dt * scale * ci;
        float ar = er * s0r - ei * s0i, aim = er * s0i + ei * s0r;
        float br = ar * s1r - aim * s1i, bim = ar * s1i + aim * s1r;
        float fr_ = 1.f, fi_ = 0.f;
        char* tab = smem + wv * 8704;
        u32 keep[16];
#pragma unroll
        for (int k = 0; k < 16; ++k) {
            *(u32*)(tab + k * 272 + lane * 4) = pkhf(fr_, -fi_);
            *(u32*)(tab + (16 + k) * 272 + lane * 4) = pkhf(ar, aim);
            keep[k] = pkhf(br, bim);
            float t;
            t = fr_ * f1r - fi_ * f1i; fi_ = fr_ * f1i + fi_ * f1r; fr_ = t;
            t = ar * c1r - aim * c1i; aim = ar * c1i + aim * c1r; ar = t;
            t = br * c1r - bim * c1i; bim = br * c1i + bim * c1r; br = t;
        }
        const int r16 = lane & 15, g = lane >> 4;
        f16x8 fb[4];
        f32x4 acc0 = {0.f, 0.f, 0.f, 0.f}, acc1 = {0.f, 0.f, 0.f, 0.f};
        __builtin_amdgcn_fence(__ATOMIC_RELEASE, "wavefront");
        __builtin_amdgcn_wave_barrier();
#pragma unroll
        for (int ks = 0; ks < 4; ++ks) {
            fb[ks] = *(const f16x8*)(tab + r16 * 272 + ks * 64 + g * 16);
            f16x8 fa = *(const f16x8*)(tab + (16 + r16) * 272 + ks * 64 + g * 16);
            acc0 = __builtin_amdgcn_mfma_f32_16x16x32_f16(fa, fb[ks], acc0, 0, 0, 0);
        }
        __builtin_amdgcn_fence(__ATOMIC_RELEASE, "wavefront");
        __builtin_amdgcn_wave_barrier();
#pragma unroll
        for (int k = 0; k < 16; ++k) *(u32*)(tab + (16 + k) * 272 + lane * 4) = keep[k];
        __builtin_amdgcn_fence(__ATOMIC_RELEASE, "wavefront");
        __builtin_amdgcn_wave_barrier();
#pragma unroll
        for (int ks = 0; ks < 4; ++ks) {
            f16x8 fa = *(const f16x8*)(tab + (16 + r16) * 272 + ks * 64 + g * 16);
            acc1 = __builtin_amdgcn_mfma_f32_16x16x32_f16(fa, fb[ks], acc1, 0, 0, 0);
        }
        __syncthreads();
        float* T = (float*)smem;
#pragma unroll
        for (int i = 0; i < 4; ++i) {
            T[hn * 1024 + grp * 512 + (4 * g + i) * 16 + r16] = acc0[i];
            T[hn * 1024 + grp * 512 + 256 + (4 * g + i) * 16 + r16] = acc1[i];
        }
        __syncthreads();
        float kv[8];
#pragma unroll
        for (int j = 0; j < 8; ++j) {
            int lag = 2047 - (tid * 8 + j);
            int p = (int)rbf((float)lag);
            int idx = p < 512 ? p : 512 + ((p - 512) >> 2);
            kv[j] = T[idx] + T[1024 + idx];
        }
        u32x4 o = {pkhf(kv[0], kv[1]), pkhf(kv[2], kv[3]), pkhf(kv[4], kv[5]), pkhf(kv[6], kv[7])};
        *(u32x4*)(ws + WS_KR + (size_t)d * 4096 + tid * 16) = o;
        if (tid == 0) ((float*)(ws + WS_KSC))[d] = 1.f / scale;
        return;
    }
    b -= NB_KGEN;
    {
        const u16* src = w2 + (size_t)b * 32 * DM;
        u32x4 v[9];
#pragma unroll
        for (int i = 0; i < 9; ++i) v[i] = *(const u32x4*)(src + (tid + 256 * i) * 8);
#pragma unroll
        for (int i = 0; i < 9; ++i) {
            int id = tid + 256 * i, row = id / 72, ch = id - row * 72;
            *(u32x4*)(smem + imgrow(row) + ch * 16) = v[i];
        }
        __syncthreads();
        const int kq = b / 36, kidx = b - kq * 36;
        const int g = lane >> 4, qq = (lane & 15) >> 2, p = lane & 3;
        const int r0 = 16 * (g >> 1) + 4 * (g & 1) + qq;
#pragma unroll
        for (int i = 0; i < 9; ++i) {
            int cb = wv * 9 + i;
            s16x4 lo = trread(smem, imgrow(r0) + cb * 32 + p * 8);
            s16x4 hi = trread(smem, imgrow(r0 + 8) + cb * 32 + p * 8);
            u32x2 l2 = __builtin_bit_cast(u32x2, lo), h2 = __builtin_bit_cast(u32x2, hi);
            u32x4 o = {l2[0], l2[1], h2[0], h2[1]};
            *(u32x4*)(ws + WS_W2F + (size_t)((kq * 4 + wv) * 324 + kidx * 9 + i) * 1024 + lane * 16) = o;
        }
    }
}

constexpr int NB_CONV = 576, NB_W1 = 144;
constexpr int CONV_LDS = 36864 + 2048;
constexpr int UCP = 4640, KB_OFF = 4 * UCP;

__global__ __launch_bounds__(256) void k_conv(const u16* __restrict__ dskip, const u16* __restrict__ w1,
                                              char* __restrict__ ws) {
    extern __shared__ __attribute__((aligned(16))) char smem[];
    const int tid = threadIdx.x, lane = tid & 63;
    const int wv = __builtin_amdgcn_readfirstlane(tid >> 6);
    const float* mod = (const float*)(ws + WS_MOD);
    int b = blockIdx.x;
    if (b < NB_CONV) {
        const int d = b;
        const u16* xr = (const u16*)(ws + WS_XT) + (size_t)d * SEQ;
        const u32x4 kv = *(const u32x4*)(ws + WS_KR + (size_t)d * 4096 + tid * 16);
        const u32x4 x0 = *(const u32x4*)(xr + tid * 8);
        const int tp = tid > 0 ? tid * 8 - 4 : 0;
        const u32x2 xm = *(const u32x2*)(xr + tp);
        f32x4 st[6];
#pragma unroll
        for (int i = 0; i < 2; ++i) st[i] = *(const f32x4*)(ws + WS_STAT + (tp + 2 * i) * 8);
#pragma unroll
        for (int i = 0; i < 4; ++i) st[2 + i] = *(const f32x4*)(ws + WS_STAT + (tid * 8 + 2 * i) * 8);
        const float sa = mod[d], ca = mod[DM + d], ga = mod[2 * DM + d];
        const float dsk = bf2f(dskip[d]);
        const float kinv = ((const float*)(ws + WS_KSC))[d];
        const float c1 = rbf(1.f + ca);
        const int he = expo(64.f * fabsf(c1) + 2.f * fabsf(sa)) - 14;
        const float hs = pow2i(-he), oscale = pow2i(he) * kinv;
        *(u32x4*)(smem + KB_OFF + tid * 16) = kv;
        {
            u32x4 z = {0u, 0u, 0u, 0u};
            if (tid < 96) *(u32x4*)(smem + (tid / 24) * UCP + (tid % 24) * 16) = z;
            else if (tid < 104) *(u32x4*)(smem + KB_OFF + 4096 + (tid - 96) * 16) = z;
        }
        u32 xw[6] = {xm[0], xm[1], x0[0], x0[1], x0[2], x0[3]};
        u32 P[6], hb[4];
#pragma unroll
        for (int k = 0; k < 6; ++k) {
            u32 t = pkbf(bflo(xw[k]) - st[k][0], bfhi(xw[k]) - st[k][2]);
            t = pkbf(bflo(t) * st[k][1], bfhi(t) * st[k][3]);
            t = pkbf(bflo(t) * c1, bfhi(t) * c1);
            t = pkbf(bflo(t) + sa, bfhi(t) + sa);
            if (k >= 2) hb[k - 2] = t;
            P[k] = pkhf(bflo(t) * hs, bfhi(t) * hs);
        }
        if (tid == 0) { P[0] = 0u; P[1] = 0u; }
        u32 Q[5];
#pragma unroll
        for (int k = 0; k < 5; ++k) Q[k] = __builtin_amdgcn_alignbit(P[k + 1], P[k], 16);
        {
            char* ub = smem + 384 + tid * 16;
            *(u32x4*)(ub) = (u32x4){Q[0], Q[1], Q[2], Q[3]};
            *(u32x4*)(ub + UCP) = (u32x4){P[1], P[2], P[3], P[4]};
            *(u32x4*)(ub + 2 * UCP) = (u32x4){Q[1], Q[2], Q[3], Q[4]};
            *(u32x4*)(ub + 3 * UCP) = (u32x4){P[2], P[3], P[4], P[5]};
        }
        __syncthreads();
        const int c = lane & 15, g = lane >> 4;
        f32x4 acc[8];
#pragma unroll
        for (int j = 0; j < 8; ++j) acc[j] = (f32x4){0.f, 0.f, 0.f, 0.f};
#pragma unroll
        for (int rr = 0; rr < 2; ++rr) {
            const int r = 2 * wv + rr;
            const char* kb = smem + KB_OFF + 2 * (2076 - 4 * c + 8 * g - 32 * r);
            const char* ub = smem + (c & 3) * UCP + 2 * (8 * g + 64 * (c >> 2) + 32 * (1 - r) + 192);
            f16x8 kf[8], uf[8];
#pragma unroll
            for (int i = 0; i < 8; ++i) {
                u32x2 a = *(const u32x2*)(kb - i * 512), a2 = *(const u32x2*)(kb - i * 512 + 8);
                kf[i] = __builtin_bit_cast(f16x8, (u32x4){a[0], a[1], a2[0], a2[1]});
                uf[i] = *(const f16x8*)(ub + i * 512);
            }
#pragma unroll
            for (int i = 0; i < 8; ++i)
#pragma unroll
                for (int dl = 0; dl + i < 8; ++dl)
                    acc[i + dl] = __builtin_amdgcn_mfma_f32_16x16x32_f16(uf[dl], kf[i], acc[i + dl], 0, 0, 0);
        }
        __syncthreads();
#pragma unroll
        for (int j = 0; j < 8; ++j) *(f32x4*)(smem + wv * 8192 + j * 1024 + lane * 16) = acc[j];
        __syncthreads();
        f32x4 y0 = *(const f32x4*)(smem + tid * 32), y1 = *(const f32x4*)(smem + tid * 32 + 16);
#pragma unroll
        for (int w = 1; w < 4; ++w) {
            y0 += *(const f32x4*)(smem + w * 8192 + tid * 32);
            y1 += *(const f32x4*)(smem + w * 8192 + tid * 32 + 16);
        }
        float y[8] = {y0[0], y0[1], y0[2], y0[3], y1[0], y1[1], y1[2], y1[3]};
        u32x4 o;
#pragma unroll
        for (int k = 0; k < 4; ++k) {
            u32 yb = pkbf(y[2 * k] * oscale, y[2 * k + 1] * oscale);
            u32 sk = pkbf(bflo(hb[k]) * dsk, bfhi(hb[k]) * dsk);
            u32 z = pkbf(bflo(yb) + bflo(sk), bfhi(yb) + bfhi(sk));
            u32 gz = pkbf(bflo(z) * ga, bfhi(z) * ga);
            o[k] = pkbf(bflo(x0[k]) + bflo(gz), bfhi(x0[k]) + bfhi(gz));
        }
        *(u32x4*)(ws + WS_X1F + (size_t)(tid >> 4) * 147456 + d * 256 + ((((u32)tid & 15u) ^ xswz(d)) << 4)) = o;
        return;
    }
    b -= NB_CONV;
    {
        const int q = b / 9, c = b - q * 9, f0 = b * 32;
        const int seg = tid & 3, rg = tid >> 2;
        u32x4 v[9]; float sb[9], cb[9];
#pragma unroll
        for (int i = 0; i < 9; ++i) {
            int row = rg + 64 * i;
            v[i] = *(const u32x4*)(w1 + (size_t)row * FF + f0 + seg * 8);
            sb[i] = mod[3 * DM + row]; cb[i] = mod[4 * DM + row];
        }
        float S[8], B[8];
#pragma unroll
        for (int k = 0; k < 8; ++k) { S[k] = 0.f; B[k] = 0.f; }
#pragma unroll
        for (int i = 0; i < 9; ++i) {
            const float c1 = rbf(1.f + cb[i]);
            u32x4 o;
#pragma unroll
            for (int k = 0; k < 4; ++k) {
                float lo = bflo(v[i][k]), hi = bfhi(v[i][k]);
                o[k] = pkbf(lo * c1, hi * c1);
                S[2 * k] += bflo(o[k]); S[2 * k + 1] += bfhi(o[k]);
                B[2 * k] = fmaf(sb[i], lo, B[2 * k]); B[2 * k + 1] = fmaf(sb[i], hi, B[2 * k + 1]);
            }
            *(u32x4*)(smem + (tid + 256 * i) * 16) = o;
        }
#pragma unroll
        for (int k = 0; k < 8; ++k) {
#pragma unroll
            for (int m = 4; m <= 32; m <<= 1) { S[k] += __shfl_xor(S[k], m); B[k] += __shfl_xor(B[k], m); }
        }
        float* red = (float*)(smem + 36864);
        if (lane < 4) {
#pragma unroll
            for (int k = 0; k < 8; ++k) { red[wv * 64 + lane * 8 + k] = S[k]; red[wv * 64 + 32 + lane * 8 + k] = B[k]; }
        }
        __syncthreads();
        if (tid < 64) {
            float t = red[tid] + red[64 + tid] + red[128 + tid] + red[192 + tid];
            ((float*)(ws + WS_SB + q * 3072))[(tid >> 5) * 288 + c * 32 + (tid & 31)] = t;
        }
        const int h = lane >> 5, blk = (lane >> 4) & 1, qq = (lane & 15) >> 2, p = lane & 3;
#pragma unroll
        for (int i = 0; i < 9; ++i) {
            const int s = wv * 9 + i, kh = s & 1, il = s >> 1;
            const int zz = il % 6;
            const int pos = (il / 6) * 12 + (zz < 4 ? 4 * kh + zz : 4 + 2 * kh + zz);
            const u32 a = (16 * s + 8 * h + qq) * 64 + ((blk ^ kh) * 32) + p * 8;
            s16x4 lo = trread(smem, a), hi = trread(smem, a + 256);
            u32x2 l2 = __builtin_bit_cast(u32x2, lo), h2 = __builtin_bit_cast(u32x2, hi);
            u32x4 o = {l2[0], l2[1], h2[0], h2[1]};
            *(u32x4*)(ws + WS_W1F + (size_t)(q * 324 + c * 36 + pos) * 1024 + lane * 16) = o;
        }
    }
}
constexpr u32 USZ = 12288, L_EX = 11 * USZ, L_SB = L_EX + 16384, L_ST = L_SB + 3072;
constexpr int MLP_LDS = L_ST + 2048;
constexpr int NUNIT = 54;
constexpr int V0 = 21;

template <int N> struct IC { static constexpr int value = N; };
template <int A, int B, class F> DI void static_for(F&& f) {
    if constexpr (A < B) { f(IC<A>{}); static_for<A + 1, B>(f); }
}
constexpr int n_hload(int u) {
    return u == 24 ? 8 : u == 26 ? 4 : u == 30 ? 4 : u == 31 ? 2 : u == 34 ? 2 : u == 35 ? 4 : u == 40 ? 6 : u == 41 ? 6 : 0;
}
constexpr bool is_poll(int u) { return u == 21 || u == 27 || u == 31 || u == 37; }
constexpr bool is_hstore(int u) { return u >= 4 && u <= 28 && (u % 3) == 1; }
constexpr bool is_pstore(int u) { return u >= 46; }
constexpr int n_extra(int u) { return u < V0 ? 0 : n_hload(u) + (is_poll(u) ? 1 : 0) + (is_hstore(u) ? 1 : 0) + (is_pstore(u) ? 1 : 0); }
constexpr int n_dma(int u, int cls) { return (u + 11 >= NUNIT) ? 0 : 1 + ((((u + 11) & 1) == cls) ? 1 : 0); }
constexpr int ops(int a, int b) {
    int m = 1 << 20;
    for (int cls = 0; cls < 2; ++cls) { int t = 0; for (int u = a; u <= b; ++u) t += n_dma(u, cls) + n_extra(u); m = t < m ? t : m; }
    return m;
}
constexpr int imin(int a, int b) { return a < b ? a : b; }
constexpr int need_extra(int V) {
    return V == 24 ? 21
         : V == 27 ? 24
         : V == 30 ? 27
         : V == 31 ? 28
         : V == 33 ? 26
         : V == 34 ? 31
         : V == 36 ? 30
         : V == 39 ? 34
         : V == 40 ? 37
         : V == 42 ? 35
         : V == 45 ? 41
         : -1;
}
constexpr int wait_n(int V) {
    int n = 63;
    if (V + 1 < NUNIT) n = imin(n, (V - 10 < V0 ? 0 : n_hload(V - 10) + (is_poll(V - 10) ? 1 : 0)) + ops(V - 9, V - 1));
    if (V == 21) n = imin(n, 12);
    if (need_extra(V) >= 0) n = imin(n, ops(need_extra(V) + 1, V - 1));
    return n;
}
#define WAITBAR(N) do { __builtin_amdgcn_sched_barrier(0); \
    asm volatile("s_waitcnt vmcnt(%0) lgkmcnt(0)\n\ts_barrier" :: "n"(N) : "memory"); \
    __builtin_amdgcn_sched_barrier(0); } while (0)

__global__ __launch_bounds__(512, 2) void k_mlp(char* __restrict__ ws) {
    extern __shared__ __attribute__((aligned(16))) char smem[];
    const int tid = threadIdx.x, lane = tid & 63;
    const int w = __builtin_amdgcn_readfirstlane(tid >> 6), tg = w & 3, kh = w >> 2;
    const int b = blockIdx.x, xcd = b & 7, rr = b >> 3;
    const int kq = xcd >> 1, it = 8 * (xcd & 1) + (rr >> 2), s = rr & 3, q = 4 * kq + s;
    const auto rs = __builtin_amdgcn_make_buffer_rsrc(ws, 0, WS_END, 0x00020000);
    const u32x4 srd = {(u32)(uintptr_t)ws, (u32)((uintptr_t)ws >> 32) & 0xffffu, WS_END, 0x00020000u};
    const u32 voff = lane * 16;
    auto dma = [&](u32 src, u32 dst) {
        __builtin_amdgcn_raw_ptr_buffer_load_lds(rs, (LAS void*)(smem + dst), 16, voff, src, 0, 0);
    };
    if (w < 3) dma(WS_SB + q * 3072 + w * 1024, L_SB + w * 1024);
    {
        const u32 xs = WS_X1F + it * 147456 + w * 1024;
#pragma unroll
        for (int k = 0; k < 18; ++k) dma(xs + k * 8192, w * 1024 + k * 8192);
    }
    const u32 posP = w * 1024, posS = 8192 + (w & 3) * 1024;
    u32 bP = WS_W1F + q * 331776 + posP, bS = WS_W1F + q * 331776 + posS, strd = USZ;
    auto issue_unit = [&](u32 slot, int odd) {
        dma(bP, slot + posP);
        if (kh == odd) dma(bS, slot + posS);
        bP += strd; bS += strd;
    };
    const u32 w2b = WS_W2F + (kq * 4 + s) * 331776;
    const u32 hfb = WS_HF + (it * 4 + kq) * 36 * 8192 + tg * 2048;
    auto kap = [&](int e2) -> u32 {
        if (e2 < 3) return 9 * s + 2 * e2;
        const int e3 = e2 - 3;
        return 9 * ((s + 1 + e3 % 3) & 3) + 2 * (e3 / 3);
    };
    auto set_stage_a = [&](int n) {
        const u32 k0 = kap(2 * n), k1 = kap(2 * n + 1);
        bP = w2b + ((((w >> 1) & 1 ? k1 : k0) + (w >> 2)) * 9 + (w & 1)) * 1024;
        bS = w2b + (((w & 1 ? k1 : k0) + ((w >> 1) & 1)) * 9 + 2) * 1024;
        strd = 3072;
    };
    auto kapb = [&](int khx, int kk) -> u32 { return 9 * ((s + 2 * khx + kk / 3) & 3) + 6 + kk % 3; };
    auto set_phase_b = [&]() {
        bP = w2b + kapb(w >> 2, w & 3) * 9216;
        bS = w2b + kapb((w >> 1) & 1, 4 + (w & 1)) * 9216;
        strd = 1024;
    };

    bf16x8 Xf[18];
    float sum = 0.f, sq = 0.f;
    {
        const int h = lane >> 5, blk = (lane >> 4) & 1, qq = (lane & 15) >> 2, p = lane & 3;
        const u32 ch = 4 * tg + 2 * blk + (p >> 1);
        const u32 r0 = 8 * h + qq, r1 = r0 + 4;
        const u32 a0 = (16 * kh + r0) * 256 + ((ch ^ xswz(r0)) << 4) + 8 * (p & 1);
        const u32 a1 = (16 * kh + r1) * 256 + ((ch ^ xswz(r1)) << 4) + 8 * (p & 1);
        const bf16x2 ones = __builtin_bit_cast(bf16x2, 0x3f803f80u);
        const u32 lbase = (u32)(uintptr_t)(LAS char*)smem;
        static_for<0, 6>([&](auto A) {
            constexpr int a = A.value;
            WAITBAR(a == 0 ? 15 : 12);
            if constexpr (a > 0) { issue_unit((2 * a - 2) * USZ, 0); issue_unit((2 * a - 1) * USZ, 1); }
            u32x2 lo[3], hi[3];
            asm volatile("ds_read_b64_tr_b16 %0, %6\n\tds_read_b64_tr_b16 %1, %7\n\t"
                         "ds_read_b64_tr_b16 %2, %6 offset:8192\n\tds_read_b64_tr_b16 %3, %7 offset:8192\n\t"
                         "ds_read_b64_tr_b16 %4, %6 offset:16384\n\tds_read_b64_tr_b16 %5, %7 offset:16384\n\t"
                         "s_waitcnt lgkmcnt(0)"
                         : "=&v"(lo[0]), "=&v"(hi[0]), "=&v"(lo[1]), "=&v"(hi[1]), "=&v"(lo[2]), "=&v"(hi[2])
                         : "v"(lbase + a0 + a * 24576), "v"(lbase + a1 + a * 24576) : "memory");
#pragma unroll
            for (int z = 0; z < 3; ++z) {
                u32x4 v = {lo[z][0], lo[z][1], hi[z][0], hi[z][1]};
                Xf[3 * a + z] = __builtin_bit_cast(bf16x8, v);
#pragma unroll
                for (int k = 0; k < 4; ++k) {
                    const u32 ek = v[k];
                    const bf16x2 e = __builtin_bit_cast(bf16x2, ek);
                    sum = __builtin_amdgcn_fdot2_f32_bf16(e, ones, sum, false);
                    sq = __builtin_amdgcn_fdot2_f32_bf16(e, e, sq, false);
                }
            }
            asm volatile("" : "+v"(sum), "+v"(sq));
        });
    }
    sum += __shfl_xor(sum, 32); sq += __shfl_xor(sq, 32);
    if (lane < 32) *(f32x2*)(smem + L_ST + w * 256 + lane * 8) = (f32x2){sum, sq};
    const u32 rdA = voff + kh * 4096, rdB = voff + 8192 + kh * 2048;
    bf16x8 Wf[3][6];
    auto read_unit = [&](bf16x8 (&dst)[6], u32 slot) {
#pragma unroll
        for (int i = 0; i < 4; ++i) dst[i] = *(const bf16x8*)(smem + slot + rdA + i * 1024);
#pragma unroll
        for (int i = 0; i < 2; ++i) dst[4 + i] = *(const bf16x8*)(smem + slot + rdB + i * 1024);
    };
    WAITBAR(13);
    issue_unit(10 * USZ, 0);
    read_unit(Wf[0], 0);
    float mneg, rinv;
    {
        f32x2 o = *(const f32x2*)(smem + L_ST + (w ^ 4) * 256 + (lane & 31) * 8);
        const float mean = (sum + o[0]) * (1.f / DM);
        float var = fmaf(-mean, mean, (sq + o[1]) * (1.f / DM));
        var = rbf(fmaxf(var, 0.f));
        const float sd = rbf(sqrtf(rbf(var + 1e-5f)));
        mneg = -rbf(mean); rinv = 1.f / sd;
    }

    f32x16 acc;
    float keep[8];
#pragma unroll
    for (int i = 0; i < 8; ++i) keep[i] = 0.f;
    const u32 exW = L_EX + w * 2048 + voff, exR = L_EX + (w ^ 4) * 2048 + voff;
    const u32 sbR = L_SB + (16 * kh + 4 * (lane >> 5)) * 4;
    const u32 hsoff = ((lane >> 4) & 1) * 1024 + ((2 * kh + (lane >> 5)) * 16 + (lane & 15)) * 16;
    u32* const flagp = (u32*)(ws + WS_FLAG + (it * 4 + kq) * 64);
    u32 hp[4];

    auto epi_half = [&](int c, int half) {
        f32x4 o = *(const f32x4*)(smem + exR + half * 1024);
        f32x4 S = *(const f32x4*)(smem + sbR + c * 128 + half * 32);
        f32x4 B = *(const f32x4*)(smem + sbR + 1152 + c * 128 + half * 32);
        float r[4];
#pragma unroll
        for (int j = 0; j < 4; ++j) r[j] = fmaf(rinv, fmaf(mneg, S[j], keep[4 * half + j] + o[j]), B[j]);
#pragma unroll
        for (int k = 0; k < 2; ++k) {
            const u32 t = pkbf(r[2 * k], r[2 * k + 1]);
            const float x0 = bflo(t), x1 = bfhi(t);
            const float e0 = __builtin_amdgcn_exp2f(x0 * -1.44269504088896340736f);
            const float e1 = __builtin_amdgcn_exp2f(x1 * -1.44269504088896340736f);
            hp[2 * half + k] = pkbf(x0 * __builtin_amdgcn_rcpf(1.f + e0), x1 * __builtin_amdgcn_rcpf(1.f + e1));
        }
    };
    auto h_store = [&](int c) {
        __builtin_amdgcn_raw_buffer_store_b128((u32x4){hp[0], hp[1], hp[2], hp[3]}, rs, hsoff, hfb + (9 * s + c) * 8192, 16);
    };
    auto publish = [&](u32 level) {
        if (tid == 0) __hip_atomic_store(flagp + s, level, __ATOMIC_RELAXED, __HIP_MEMORY_SCOPE_AGENT);
    };
    auto g1_mfma = [&](auto VV) {
        constexpr int v = VV.value;
        if constexpr (v == 0) {
#pragma unroll
            for (int i = 0; i < 16; ++i) acc[i] = 0.f;
        }
#pragma unroll
        for (int i = 0; i < 6; ++i) acc = __builtin_amdgcn_mfma_f32_32x32x16_bf16(Wf[v][i], Xf[6 * v + i], acc, 0, 0, 0);
        if constexpr (v == 2) {
            *(f32x4*)(smem + exW) = (f32x4){acc[8], acc[9], acc[10], acc[11]};
            *(f32x4*)(smem + exW + 1024) = (f32x4){acc[12], acc[13], acc[14], acc[15]};
#pragma unroll
            for (int i = 0; i < 8; ++i) keep[i] = acc[i];
        }
    };

    u32 cur = 0;
#pragma nounroll
    for (int c = 0; c < 7; ++c) {
        static_for<0, 3>([&](auto VV) {
            constexpr int v = VV.value;
            WAITBAR(v == 0 ? 12 : 13);
            if constexpr (v == 1) {
                if (c == 5) set_stage_a(0);
                if (c == 6) set_stage_a(1);
            }
            if constexpr (v == 0) { if (c >= 4) publish(c - 3); }
            issue_unit(cur, (c + v + 1) & 1);
            cur = cur == 10 * USZ ? 0 : cur + USZ;
            read_unit(Wf[(v + 1) % 3], cur);
            if constexpr (v < 2) {
                if (c > 0) { epi_half(c - 1, v); if constexpr (v == 1) h_store(c - 1); }
            }
            g1_mfma(VV);
        });
    }

    u32x4 HA[6][2][2], HB[6][2], pollv;
    f32x4 acc2[9][2];
#pragma unroll
    for (int i = 0; i < 9; ++i) { acc2[i][0] = (f32x4){0.f, 0.f, 0.f, 0.f}; acc2[i][1] = (f32x4){0.f, 0.f, 0.f, 0.f}; }
    auto hload = [&](u32x4& d0, u32x4& d1, u32 kidx) {
        const u32 so = hfb + kidx * 8192;
        asm volatile("buffer_load_dwordx4 %0, %2, %3, %4 offen sc1\n\tbuffer_load_dwordx4 %1, %5, %3, %4 offen sc1"
                     : "=&v"(d0), "=&v"(d1) : "v"(voff + kh * 1024), "s"(srd), "s"(so), "v"(voff + (kh ^ 1) * 1024) : "memory");
    };
    const u32 fso = WS_FLAG + (it * 4 + kq) * 64;
    const u32 vzero = 0;
    auto poll = [&]() {
        asm volatile("buffer_load_dwordx4 %0, %1, %2, %3 offen sc1" : "=v"(pollv) : "v"(vzero), "s"(srd), "s"(fso) : "memory");
    };
    auto check = [&](u32 level) {
        asm volatile("" : "+v"(pollv));
#pragma nounroll
        for (u32 spins = 0; spins < (1u << 20); ++spins) {
            const u32 f0 = __builtin_amdgcn_readfirstlane(pollv[0]), f1 = __builtin_amdgcn_readfirstlane(pollv[1]);
            const u32 f2 = __builtin_amdgcn_readfirstlane(pollv[2]), f3 = __builtin_amdgcn_readfirstlane(pollv[3]);
            const bool ok = (f0 >= level || s == 0) && (f1 >= level || s == 1) && (f2 >= level || s == 2) && (f3 >= level || s == 3);
            if (ok) break;
            __builtin_amdgcn_s_sleep(1);
            asm volatile("buffer_load_dwordx4 %0, %1, %2, %3 offen sc1\n\ts_waitcnt vmcnt(0)"
                         : "=v"(pollv) : "v"(vzero), "s"(srd), "s"(fso) : "memory");
        }
    };
    auto ready_a = [&](int n) {
        asm volatile("" : "+v"(HA[n][0][0]), "+v"(HA[n][0][1]), "+v"(HA[n][1][0]), "+v"(HA[n][1][1]));
    };
    const u32 psb = WS_PART + ((kq * 16 + it) * 4 + s) * 73728 + tg * 2048 + kh * 1024;

    static_for<V0, NUNIT + 1>([&](auto VV) {
        constexpr int V = VV.value;
        WAITBAR(wait_n(V));
        if constexpr (V == 21) publish(4);
        if constexpr (V == 24) { publish(6); check(2); }
        if constexpr (V == 30) check(4);
        if constexpr (V == 31) publish(9);
        if constexpr (V == 34) check(6);
        if constexpr (V == 40) check(9);
        constexpr int T = V + 11;
        if constexpr (T < NUNIT) {
            if constexpr (T < 45 && (T - 27) % 3 == 0) set_stage_a((T - 27) / 3);
            if constexpr (T == 45) set_phase_b();
            issue_unit((V % 11) * USZ, T & 1);
        }
        if constexpr (is_poll(V)) poll();
        if constexpr (V == 24) {
            hload(HA[0][0][0], HA[0][0][1], kap(0) + kh); hload(HA[0][1][0], HA[0][1][1], kap(1) + kh);
            hload(HA[1][0][0], HA[1][0][1], kap(2) + kh); hload(HA[1][1][0], HA[1][1][1], kap(3) + kh);
        }
        if constexpr (V == 26) { hload(HA[2][0][0], HA[2][0][1], kap(4) + kh); hload(HA[2][1][0], HA[2][1][1], kap(5) + kh); }
        if constexpr (V == 30) { hload(HA[3][0][0], HA[3][0][1], kap(6) + kh); hload(HA[3][1][0], HA[3][1][1], kap(7) + kh); }
        if constexpr (V == 31) { hload(HA[4][0][0], HA[4][0][1], kap(8) + kh); }
        if constexpr (V == 34) { hload(HA[4][1][0], HA[4][1][1], kap(9) + kh); }
        if constexpr (V == 35) { hload(HA[5][0][0], HA[5][0][1], kap(10) + kh); hload(HA[5][1][0], HA[5][1][1], kap(11) + kh); }
        if constexpr (V == 40 || V == 41) {
#pragma unroll
            for (int k = 0; k < 3; ++k) hload(HB[3 * (V - 40) + k][0], HB[3 * (V - 40) + k][1], kapb(kh, 3 * (V - 40) + k));
        }
        if constexpr (V + 1 < NUNIT) read_unit(Wf[(V + 1) % 3], ((V + 1) % 11) * USZ);
        if constexpr (V <= 28 && V % 3 < 2) {
            epi_half(V / 3 - 1, V % 3);
            if constexpr (V % 3 == 1) h_store(V / 3 - 1);
        }
        if constexpr (V < 27) g1_mfma(IC<V % 3>{});
        else if constexpr (V < 45) {
            constexpr int n = (V - 27) / 3, v = (V - 27) % 3;
            if constexpr (v == 0) ready_a(n);
#pragma unroll
            for (int kk = 0; kk < 2; ++kk)
#pragma unroll
                for (int so = 0; so < 3; ++so) {
                    const bf16x8 a = Wf[V % 3][so < 2 ? 2 * kk + so : 4 + kk];
#pragma unroll
                    for (int tt = 0; tt < 2; ++tt)
                        acc2[3 * v + so][tt] = __builtin_amdgcn_mfma_f32_16x16x32_bf16(
                            a, __builtin_bit_cast(bf16x8, HA[n][kk][tt]), acc2[3 * v + so][tt], 0, 0, 0);
                }
        } else {
            constexpr int sg = V - 45;
            if constexpr (sg > 0) {
                f32x4 o = *(const f32x4*)(smem + exR + ((sg - 1) & 1) * 1024);
                o += acc2[sg - 1][0];
                __builtin_amdgcn_raw_buffer_store_b128(__builtin_bit_cast(u32x4, o), rs, voff, psb + (sg - 1) * 8192, 0);
            }
            if constexpr (sg < 9) {
                if constexpr (sg == 0) {
                    asm volatile("" : "+v"(HB[0][0]), "+v"(HB[0][1]), "+v"(HB[1][0]), "+v"(HB[1][1]), "+v"(HB[2][0]), "+v"(HB[2][1]));
                    asm volatile("" : "+v"(HB[3][0]), "+v"(HB[3][1]), "+v"(HB[4][0]), "+v"(HB[4][1]), "+v"(HB[5][0]), "+v"(HB[5][1]));
                }
#pragma unroll
                for (int tt = 1; tt >= 0; --tt)
#pragma unroll
                    for (int kk = 0; kk < 6; ++kk)
                        acc2[sg][tt] = __builtin_amdgcn_mfma_f32_16x16x32_bf16(
                            Wf[V % 3][kk], __builtin_bit_cast(bf16x8, HB[kk][tt]), acc2[sg][tt], 0, 0, 0);
                *(f32x4*)(smem + exW + (sg & 1) * 1024) = acc2[sg][1];
            }
        }
    });
}

__global__ __launch_bounds__(256) void k_fin(const char* __restrict__ ws, u16* __restrict__ out) {
    const int lane = threadIdx.x & 63;
    const int fr = blockIdx.x * 4 + (threadIdx.x >> 6);
    const int tt = fr & 1, tg = (fr >> 1) & 3, r = fr >> 3, tile = r % 9, r2 = r / 9, nq = r2 & 3, it = r2 >> 2;
    f32x4 a = *(const f32x4*)(ws + WS_PART + (size_t)fr * 1024 + lane * 16);
#pragma unroll
    for (int k = 1; k < 4; ++k) a += *(const f32x4*)(ws + WS_PART + (size_t)k * 4718592 + (size_t)fr * 1024 + lane * 16);
    const int tl = tg * 32 + tt * 16 + (lane & 15), d0 = nq * 144 + tile * 16 + (lane >> 4) * 4;
    const f32x4 gb = *(const f32x4*)(ws + WS_MOD + (5 * DM + d0) * 4);
    float xr[4];
#pragma unroll
    for (int j = 0; j < 4; ++j) {
        const u32 d = d0 + j;
        xr[j] = bf2f(*(const u16*)(ws + WS_X1F + (size_t)it * 147456 + d * 256 + ((((u32)tl >> 3) ^ xswz(d)) << 4) + (tl & 7) * 2));
    }
    u32x2 o;
#pragma unroll
    for (int k = 0; k < 2; ++k) {
        const u32 t = pkbf(a[2 * k], a[2 * k + 1]);
        const u32 gz = pkbf(bflo(t) * gb[2 * k], bfhi(t) * gb[2 * k + 1]);
        o[k] = pkbf(xr[2 * k] + bflo(gz), xr[2 * k + 1] + bfhi(gz));
    }
    *(u32x2*)(out + (size_t)(it * 128 + tl) * DM + d0) = o;
}

extern "C" void kernel_launch(void* const* d_in, const int* in_sizes, int n_in, void* d_out, int out_size,
                              void* d_ws, size_t ws_size, hipStream_t stream) {
    const u16* x = (const u16*)d_in[0];
    char* ws = (char*)d_ws;
    (void)hipFuncSetAttribute((const void*)k_mlp, hipFuncAttributeMaxDynamicSharedMemorySize, MLP_LDS);
    k_prep<<<NB_PREP, 256, PREP_LDS, stream>>>(x, (const u16*)d_in[1], (const u16*)d_in[2], (const u16*)d_in[3],
                                               (const u16*)d_in[4], (const u16*)d_in[5], (const u16*)d_in[9],
                                               (const u16*)d_in[10], (const u16*)d_in[11], ws);
    k_conv<<<NB_CONV + NB_W1, 256, CONV_LDS, stream>>>((const u16*)d_in[6], (const u16*)d_in[8], ws);
    k_mlp<<<256, 512, MLP_LDS, stream>>>(ws);
    k_fin<<<4608 / 4, 256, 0, stream>>>(ws, (u16*)d_out);
}
```

```cpp
#include <hip/hip_runtime.h>
#include <stdint.h>

typedef unsigned short u16;
typedef unsigned int u32;
typedef float f32x2 __attribute__((ext_vector_type(2)));
typedef float f32x4 __attribute__((ext_vector_type(4)));
typedef float f32x16 __attribute__((ext_vector_type(16)));
typedef _Float16 f16x2 __attribute__((ext_vector_type(2)));
typedef _Float16 f16x8 __attribute__((ext_vector_type(8)));
typedef __bf16 bf16x2 __attribute__((ext_vector_type(2)));
typedef __bf16 bf16x8 __attribute__((ext_vector_type(8)));
typedef short s16x4 __attribute__((ext_vector_type(4)));
typedef u32 u32x2 __attribute__((ext_vector_type(2)));
typedef u32 u32x4 __attribute__((ext_vector_type(4)));
#define LAS __attribute__((address_space(3)))
#define DI __device__ __forceinline__
#define RTSTAMP(k)

constexpr int SEQ = 2048, DM = 576, NS = 128, FF = 4608;

constexpr u32 WS_MOD   = 0;
constexpr u32 WS_STAT  = 16384;
constexpr u32 WS_KSC   = 32768;
constexpr u32 WS_FLAG  = 36864;
constexpr u32 WS_EX    = 65536;
constexpr u32 WS_XT    = 1u << 20;
constexpr u32 WS_KR    = WS_XT + 2359296;
constexpr u32 WS_X1F   = WS_KR + 2359296;
constexpr u32 WS_WF    = WS_X1F + 2359296;
constexpr u32 WS_HF    = WS_WF + 10616832;
constexpr u32 WS_PART  = WS_HF + 18874368;
constexpr u32 WS_PH    = WS_PART + 6291456;
constexpr u32 WS_END   = WS_PH + 2097152;

DI float bflo(u32 u) { return __uint_as_float(u << 16); }
DI float bfhi(u32 u) { return __uint_as_float(u & 0xffff0000u); }
DI float bf2f(u16 v) { return __uint_as_float(((u32)v) << 16); }
DI u32 pkbf(float a, float b) { f32x2 v = {a, b}; return __builtin_bit_cast(u32, __builtin_convertvector(v, bf16x2)); }
DI u32 pkhf(float a, float b) { f32x2 v = {a, b}; return __builtin_bit_cast(u32, __builtin_convertvector(v, f16x2)); }
DI float rbf(float a) { return bflo(pkbf(a, a)); }
template <int CTRL> DI float dppf(float v) {
    return __builtin_bit_cast(float, __builtin_amdgcn_update_dpp(0, __builtin_bit_cast(int, v), CTRL, 0xf, 0xf, true));
}
DI s16x4 trread(const char* smem, u32 off) {
    return __builtin_amdgcn_ds_read_tr16_b64_v4i16((LAS s16x4*)(smem + off));
}
DI u32 xswz(u32 row) { return ((row & 3) << 2) | ((row >> 2) & 3); }
DI float pow2i(int e) { e = e < -126 ? -126 : (e > 127 ? 127 : e); return __uint_as_float((u32)(e + 127) << 23); }
DI int expo(float x) { return (int)((__float_as_uint(x) >> 23) & 255u) - 127; }

DI u32 imgrow(u32 row) { return row * 1184u + (row >> 3) * 128u; }

constexpr int NB_LN = 64, NB_GEMV = 108, NB_KGEN = 576, NB_W2 = 144;
constexpr int NB_PREP = NB_LN + NB_GEMV + NB_KGEN + NB_W2;
constexpr int PREP_LDS = 38400;

__global__ __launch_bounds__(256) void k_prep(const u16* __restrict__ x, const u16* __restrict__ logA,
                                              const u16* __restrict__ Aim, const u16* __restrict__ Cre,
                                              const u16* __restrict__ Cim, const u16* __restrict__ logdt,
                                              const u16* __restrict__ w2, const u16* __restrict__ cvec,
                                              const u16* __restrict__ wmod, char* __restrict__ ws) {
    extern __shared__ __attribute__((aligned(16))) char smem[];
    RTSTAMP(0);
    const int tid = threadIdx.x, lane = tid & 63;
    const int wv = __builtin_amdgcn_readfirstlane(tid >> 6);
    int b = blockIdx.x;
    if (b < NB_LN) {
        if (b == 0) {
            u32x4 z = {0u, 0u, 0u, 0u};
            *(u32x4*)(ws + WS_FLAG + tid * 16) = z;
            *(u32x4*)(ws + WS_FLAG + 4096 + tid * 16) = z;
        }
        const int t0 = b * 32, tok = tid >> 3, sub = tid & 7;
        u32x4 v[9];
#pragma unroll
        for (int i = 0; i < 9; ++i) v[i] = *(const u32x4*)(x + (size_t)(t0 + tok) * DM + (sub + 8 * i) * 8);
        float s = 0.f;
#pragma unroll
        for (int i = 0; i < 9; ++i) {
            *(u32x4*)(smem + imgrow(tok) + (sub + 8 * i) * 16) = v[i];
#pragma unroll
            for (int k = 0; k < 4; ++k) s += bflo(v[i][k]) + bfhi(v[i][k]);
        }
        s += dppf<0xB1>(s); s += dppf<0x4E>(s); s += dppf<0x141>(s);
        const float mean = s * (1.f / DM);
        float q = 0.f;
#pragma unroll
        for (int i = 0; i < 9; ++i)
#pragma unroll
            for (int k = 0; k < 4; ++k) {
                float a = bflo(v[i][k]) - mean, c = bfhi(v[i][k]) - mean;
                q = fmaf(a, a, q); q = fmaf(c, c, q);
            }
        q += dppf<0xB1>(q); q += dppf<0x4E>(q); q += dppf<0x141>(q);
        if (sub == 0) {
            float var = rbf(q * (1.f / DM));
            float sd = rbf(sqrtf(rbf(var + 1e-5f)));
            f32x2 st = {rbf(mean), 1.f / sd};
            *(f32x2*)(ws + WS_STAT + (t0 + tok) * 8) = st;
        }
        __syncthreads();
        const int g = lane >> 4, qq = (lane & 15) >> 2, p = lane & 3;
        u16* xT = (u16*)(ws + WS_XT);
#pragma unroll
        for (int i = 0; i < 9; ++i) {
            int cb = wv * 9 + i;
            s16x4 lo = trread(smem, imgrow(8 * g + qq) + cb * 32 + p * 8);
            s16x4 hi = trread(smem, imgrow(8 * g + 4 + qq) + cb * 32 + p * 8);
            u32x2 l2 = __builtin_bit_cast(u32x2, lo), h2 = __builtin_bit_cast(u32x2, hi);
            u32x4 o = {l2[0], l2[1], h2[0], h2[1]};
            *(u32x4*)(xT + (size_t)(cb * 16 + (lane & 15)) * SEQ + t0 + 8 * g) = o;
        }
        return;
    }
    b -= NB_LN;
    if (b < NB_GEMV) {
        const int seg = tid & 3, rg = tid >> 2;
        u32x4 v[9]; u16 cv[9];
#pragma unroll
        for (int i = 0; i < 9; ++i) {
            int row = rg + 64 * i;
            v[i] = *(const u32x4*)(wmod + (size_t)row * (6 * DM) + b * 32 + seg * 8);
            cv[i] = cvec[row];
        }
        float acc[8];
#pragma unroll
        for (int k = 0; k < 8; ++k) acc[k] = 0.f;
#pragma unroll
        for (int i = 0; i < 9; ++i) {
            float c = bf2f(cv[i]);
            float sl = rbf(c / (1.f + __expf(-c)));
#pragma unroll
            for (int k = 0; k < 4; ++k) {
                acc[2 * k] = fmaf(sl, bflo(v[i][k]), acc[2 * k]);
                acc[2 * k + 1] = fmaf(sl, bfhi(v[i][k]), acc[2 * k + 1]);
            }
        }
        float* red = (float*)smem;
        *(f32x4*)(red + rg * 32 + seg * 8) = (f32x4){acc[0], acc[1], acc[2], acc[3]};
        *(f32x4*)(red + rg * 32 + seg * 8 + 4) = (f32x4){acc[4], acc[5], acc[6], acc[7]};
        __syncthreads();
        const int col = tid & 31, part = tid >> 5;
        float s = 0.f;
#pragma unroll
        for (int i = 0; i < 8; ++i) s += red[(part * 8 + i) * 32 + col];
        __syncthreads();
        red[part * 32 + col] = s;
        __syncthreads();
        if (tid < 32) {
            float t = 0.f;
#pragma unroll
            for (int i = 0; i < 8; ++i) t += red[i * 32 + tid];
            ((float*)(ws + WS_MOD))[b * 32 + tid] = rbf(t);
        }
        return;
    }
    b -= NB_GEMV;
    if (b < NB_KGEN) {
        const int d = b, hn = wv & 1, grp = wv >> 1;
        const int n = hn * 64 + lane, n2 = n ^ 64;
        const float la = bf2f(logA[d * NS + n]), ai = bf2f(Aim[d * NS + n]);
        const float cr = bf2f(Cre[d * NS + n]), ci = bf2f(Cim[d * NS + n]);
        const float cr2 = bf2f(Cre[d * NS + n2]), ci2 = bf2f(Cim[d * NS + n2]);
        const float dt = rbf(__expf(bf2f(logdt[d])));
        const float zr = -rbf(__expf(la)) * dt, zi = ai * dt;
        float bs = fabsf(cr) + fabsf(ci) + fabsf(cr2) + fabsf(ci2);
#pragma unroll
        for (int m = 32; m >= 1; m >>= 1) bs += __shfl_xor(bs, m);
        const float bound = 2.f * dt * bs * 1.0001f;
        const int se = 14 - expo(bound);
        const float scale = pow2i(se > 100 ? 100 : se);
        const float zr2 = zr * 1.44269504088896340736f;
        auto cexp = [&](float p, float& re, float& im) {
            float mag = __builtin_amdgcn_exp2f(zr2 * p);
            float ang = zi * p;
            float t = ang * 0.15915494309189535f;
            float rr = __builtin_rintf(t);
            float fr = fmaf(ang, 0.15915494309189535f, -rr);
            fr = fmaf(ang, 6.4206382e-9f, fr);
            re = mag * __builtin_amdgcn_cosf(fr);
            im = mag * __builtin_amdgcn_sinf(fr);
        };
        const float p1 = grp ? 4.f : 1.f;
        float f1r, f1i, c1r, c1i, s0r, s0i, s1r, s1i;
        cexp(p1, f1r, f1i);
        cexp(16.f * p1, c1r, c1i);
        cexp(grp ? 512.f : 0.f, s0r, s0i);
        cexp(256.f * p1, s1r, s1i);
        const float er = 2.f * dt * scale * cr, ei = 2.f * dt * scale * ci;
        float ar = er * s0r - ei * s0i, aim = er * s0i + ei * s0r;
        float br = ar * s1r - aim * s1i, bim = ar * s1i + aim * s1r;
        float fr_ = 1.f, fi_ = 0.f;
        char* tab = smem + wv * 8704;
        u32 keep[16];
#pragma unroll
        for (int k = 0; k < 16; ++k) {
            *(u32*)(tab + k * 272 + lane * 4) = pkhf(fr_, -fi_);
            *(u32*)(tab + (16 + k) * 272 + lane * 4) = pkhf(ar, aim);
            keep[k] = pkhf(br, bim);
            float t;
            t = fr_ * f1r - fi_ * f1i; fi_ = fr_ * f1i + fi_ * f1r; fr_ = t;
            t = ar * c1r - aim * c1i; aim = ar * c1i + aim * c1r; ar = t;
            t = br * c1r - bim * c1i; bim = br * c1i + bim * c1r; br = t;
        }
        const int r16 = lane & 15, g = lane >> 4;
        f16x8 fb[4];
        f32x4 acc0 = {0.f, 0.f, 0.f, 0.f}, acc1 = {0.f, 0.f, 0.f, 0.f};
        __builtin_amdgcn_fence(__ATOMIC_RELEASE, "wavefront");
        __builtin_amdgcn_wave_barrier();
#pragma unroll
        for (int ks = 0; ks < 4; ++ks) {
            fb[ks] = *(const f16x8*)(tab + r16 * 272 + ks * 64 + g * 16);
            f16x8 fa = *(const f16x8*)(tab + (16 + r16) * 272 + ks * 64 + g * 16);
            acc0 = __builtin_amdgcn_mfma_f32_16x16x32_f16(fa, fb[ks], acc0, 0, 0, 0);
        }
        __builtin_amdgcn_fence(__ATOMIC_RELEASE, "wavefront");
        __builtin_amdgcn_wave_barrier();
#pragma unroll
        for (int k = 0; k < 16; ++k) *(u32*)(tab + (16 + k) * 272 + lane * 4) = keep[k];
        __builtin_amdgcn_fence(__ATOMIC_RELEASE, "wavefront");
        __builtin_amdgcn_wave_barrier();
#pragma unroll
        for (int ks = 0; ks < 4; ++ks) {
            f16x8 fa = *(const f16x8*)(tab + (16 + r16) * 272 + ks * 64 + g * 16);
            acc1 = __builtin_amdgcn_mfma_f32_16x16x32_f16(fa, fb[ks], acc1, 0, 0, 0);
        }
        __syncthreads();
        float* T = (float*)smem;
#pragma unroll
        for (int i = 0; i < 4; ++i) {
            T[hn * 1024 + grp * 512 + (4 * g + i) * 16 + r16] = acc0[i];
            T[hn * 1024 + grp * 512 + 256 + (4 * g + i) * 16 + r16] = acc1[i];
        }
        __syncthreads();
        float kv[8];
#pragma unroll
        for (int j = 0; j < 8; ++j) {
            int lag = 2047 - (tid * 8 + j);
            int p = (int)rbf((float)lag);
            int idx = p < 512 ? p : 512 + ((p - 512) >> 2);
            kv[j] = T[idx] + T[1024 + idx];
        }
        u32x4 o = {pkhf(kv[0], kv[1]), pkhf(kv[2], kv[3]), pkhf(kv[4], kv[5]), pkhf(kv[6], kv[7])};
        *(u32x4*)(ws + WS_KR + (size_t)d * 4096 + tid * 16) = o;
        if (tid == 0) ((float*)(ws + WS_KSC))[d] = 1.f / scale;
        return;
    }
    b -= NB_KGEN;
    {
        const u16* src = w2 + (size_t)b * 32 * DM;
        u32x4 v[9];
#pragma unroll
        for (int i = 0; i < 9; ++i) v[i] = *(const u32x4*)(src + (tid + 256 * i) * 8);
#pragma unroll
        for (int i = 0; i < 9; ++i) {
            int id = tid + 256 * i, row = id / 72, ch = id - row * 72;
            *(u32x4*)(smem + imgrow(row) + ch * 16) = v[i];
        }
        __syncthreads();
        const int kq = b / 36, kk = b - kq * 36, sp = kk / 9, c = kk - sp * 9;
        const int par = wv & 1, rel = (sp - wv) & 3;
        const int e = rel == 0 ? (c < 8 ? c : 20) : (c < 4 ? 8 : 9) + c * 3 + (rel - 1);
        const int h = lane >> 5, blk = (lane >> 4) & 1, qq = (lane & 15) >> 2, p = lane & 3;
#pragma unroll
        for (int i = 0; i < 9; ++i) {
            const int sg = (i + 1) >> 1, s2r = i == 0 ? 0 : (i - 1) & 1;
            const int T = 9 * (wv >> 1) + (sg == 0 ? 4 : 5 * par + ((kq + sg) & 3));
            const int m = sg == 4 ? 252 + 2 * e + s2r
                        : e < 20 ? 7 * e + i
                        : sg == 0 ? 172 + (e - 20) : (sg == 1 ? 140 : 188 + (sg - 2) * 32) + (e - 20) * 2 + s2r;
            const int r0 = 16 * (s2r ^ par) + 4 * h + qq;
            const u32 col = T * 64 + blk * 32 + p * 8;
            s16x4 lo = trread(smem, imgrow(r0) + col);
            s16x4 hi = trread(smem, imgrow(r0 + 8) + col);
            u32x2 l2 = __builtin_bit_cast(u32x2, lo), h2 = __builtin_bit_cast(u32x2, hi);
            u32x4 o = {l2[0], l2[1], h2[0], h2[1]};
            *(u32x4*)(ws + WS_WF + (size_t)((kq * 4 + wv) * 648 + 324 + m) * 1024 + lane * 16) = o;
        }
    }
}

constexpr int NB_CONV = 576, NB_W1 = 144;
constexpr int CONV_LDS = 36864 + 2048;
constexpr int UCP = 4640, KB_OFF = 4 * UCP;

__global__ __launch_bounds__(256) void k_conv(const u16* __restrict__ dskip, const u16* __restrict__ w1,
                                              char* __restrict__ ws) {
    extern __shared__ __attribute__((aligned(16))) char smem[];
    const int tid = threadIdx.x, lane = tid & 63;
    const int wv = __builtin_amdgcn_readfirstlane(tid >> 6);
    RTSTAMP(1);
    const float* mod = (const float*)(ws + WS_MOD);
    int b = blockIdx.x;
    if (b < NB_CONV) {
        const int d = b;
        const u16* xr = (const u16*)(ws + WS_XT) + (size_t)d * SEQ;
        const u32x4 kv = *(const u32x4*)(ws + WS_KR + (size_t)d * 4096 + tid * 16);
        const u32x4 x0 = *(const u32x4*)(xr + tid * 8);
        const int tp = tid > 0 ? tid * 8 - 4 : 0;
        const u32x2 xm = *(const u32x2*)(xr + tp);
        f32x4 st[6];
#pragma unroll
        for (int i = 0; i < 2; ++i) st[i] = *(const f32x4*)(ws + WS_STAT + (tp + 2 * i) * 8);
#pragma unroll
        for (int i = 0; i < 4; ++i) st[2 + i] = *(const f32x4*)(ws + WS_STAT + (tid * 8 + 2 * i) * 8);
        const float sa = mod[d], ca = mod[DM + d], ga = mod[2 * DM + d];
        const float dsk = bf2f(dskip[d]);
        const float kinv = ((const float*)(ws + WS_KSC))[d];
        const float c1 = rbf(1.f + ca);
        const int he = expo(64.f * fabsf(c1) + 2.f * fabsf(sa)) - 14;
        const float hs = pow2i(-he), oscale = pow2i(he) * kinv;
        *(u32x4*)(smem + KB_OFF + tid * 16) = kv;
        {
            u32x4 z = {0u, 0u, 0u, 0u};
            if (tid < 96) *(u32x4*)(smem + (tid / 24) * UCP + (tid % 24) * 16) = z;
            else if (tid < 104) *(u32x4*)(smem + KB_OFF + 4096 + (tid - 96) * 16) = z;
        }
        u32 xw[6] = {xm[0], xm[1], x0[0], x0[1], x0[2], x0[3]};
        u32 P[6], hb[4];
#pragma unroll
        for (int k = 0; k < 6; ++k) {
            u32 t = pkbf(bflo(xw[k]) - st[k][0], bfhi(xw[k]) - st[k][2]);
            t = pkbf(bflo(t) * st[k][1], bfhi(t) * st[k][3]);
            t = pkbf(bflo(t) * c1, bfhi(t) * c1);
            t = pkbf(bflo(t) + sa, bfhi(t) + sa);
            if (k >= 2) hb[k - 2] = t;
            P[k] = pkhf(bflo(t) * hs, bfhi(t) * hs);
        }
        if (tid == 0) { P[0] = 0u; P[1] = 0u; }
        u32 Q[5];
#pragma unroll
        for (int k = 0; k < 5; ++k) Q[k] = __builtin_amdgcn_alignbit(P[k + 1], P[k], 16);
        {
            char* ub = smem + 384 + tid * 16;
            *(u32x4*)(ub) = (u32x4){Q[0], Q[1], Q[2], Q[3]};
            *(u32x4*)(ub + UCP) = (u32x4){P[1], P[2], P[3], P[4]};
            *(u32x4*)(ub + 2 * UCP) = (u32x4){Q[1], Q[2], Q[3], Q[4]};
            *(u32x4*)(ub + 3 * UCP) = (u32x4){P[2], P[3], P[4], P[5]};
        }
        __syncthreads();
        const int c = lane & 15, g = lane >> 4;
        f32x4 acc[8];
#pragma unroll
        for (int j = 0; j < 8; ++j) acc[j] = (f32x4){0.f, 0.f, 0.f, 0.f};
#pragma unroll
        for (int rr = 0; rr < 2; ++rr) {
            const int r = 2 * wv + rr;
            const char* kb = smem + KB_OFF + 2 * (2076 - 4 * c + 8 * g - 32 * r);
            const char* ub = smem + (c & 3) * UCP + 2 * (8 * g + 64 * (c >> 2) + 32 * (1 - r) + 192);
            f16x8 kf[8], uf[8];
#pragma unroll
            for (int i = 0; i < 8; ++i) {
                u32x2 a = *(const u32x2*)(kb - i * 512), a2 = *(const u32x2*)(kb - i * 512 + 8);
                kf[i] = __builtin_bit_cast(f16x8, (u32x4){a[0], a[1], a2[0], a2[1]});
                uf[i] = *(const f16x8*)(ub + i * 512);
            }
#pragma unroll
            for (int i = 0; i < 8; ++i)
#pragma unroll
                for (int dl = 0; dl + i < 8; ++dl)
                    acc[i + dl] = __builtin_amdgcn_mfma_f32_16x16x32_f16(uf[dl], kf[i], acc[i + dl], 0, 0, 0);
        }
        __syncthreads();
#pragma unroll
        for (int j = 0; j < 8; ++j) *(f32x4*)(smem + wv * 8192 + j * 1024 + lane * 16) = acc[j];
        __syncthreads();
        f32x4 y0 = *(const f32x4*)(smem + tid * 32), y1 = *(const f32x4*)(smem + tid * 32 + 16);
#pragma unroll
        for (int w = 1; w < 4; ++w) {
            y0 += *(const f32x4*)(smem + w * 8192 + tid * 32);
            y1 += *(const f32x4*)(smem + w * 8192 + tid * 32 + 16);
        }
        float y[8] = {y0[0], y0[1], y0[2], y0[3], y1[0], y1[1], y1[2], y1[3]};
        u32x4 o;
#pragma unroll
        for (int k = 0; k < 4; ++k) {
            u32 yb = pkbf(y[2 * k] * oscale, y[2 * k + 1] * oscale);
            u32 sk = pkbf(bflo(hb[k]) * dsk, bfhi(hb[k]) * dsk);
            u32 z = pkbf(bflo(yb) + bflo(sk), bfhi(yb) + bfhi(sk));
            u32 gz = pkbf(bflo(z) * ga, bfhi(z) * ga);
            o[k] = pkbf(bflo(x0[k]) + bflo(gz), bfhi(x0[k]) + bfhi(gz));
        }
        *(u32x4*)(ws + WS_X1F + (size_t)(tid >> 4) * 147456 + ((d / 48) * 12 + ((tid >> 2) & 3) * 3 + (d >> 4) % 3) * 1024 + (d & 15) * 64 + (tid & 3) * 16) = o;
        return;
    }
    b -= NB_CONV;
    {
        const int q = b / 9, c = b - q * 9, f0 = b * 32;
        const int seg = tid & 3, rg = tid >> 2;
        u32x4 v[9]; float sb[9], cb[9];
#pragma unroll
        for (int i = 0; i < 9; ++i) {
            int row = rg + 64 * i;
            v[i] = *(const u32x4*)(w1 + (size_t)row * FF + f0 + seg * 8);
            sb[i] = mod[3 * DM + row]; cb[i] = mod[4 * DM + row];
        }
        float S[8], B[8];
#pragma unroll
        for (int k = 0; k < 8; ++k) { S[k] = 0.f; B[k] = 0.f; }
#pragma unroll
        for (int i = 0; i < 9; ++i) {
            const float c1 = rbf(1.f + cb[i]);
            u32x4 o;
#pragma unroll
            for (int k = 0; k < 4; ++k) {
                float lo = bflo(v[i][k]), hi = bfhi(v[i][k]);
                o[k] = pkbf(lo * c1, hi * c1);
                S[2 * k] += bflo(o[k]); S[2 * k + 1] += bfhi(o[k]);
                B[2 * k] = fmaf(sb[i], lo, B[2 * k]); B[2 * k + 1] = fmaf(sb[i], hi, B[2 * k + 1]);
            }
            *(u32x4*)(smem + (tid + 256 * i) * 16) = o;
        }
#pragma unroll
        for (int k = 0; k < 8; ++k) {
#pragma unroll
            for (int m = 4; m <= 32; m <<= 1) { S[k] += __shfl_xor(S[k], m); B[k] += __shfl_xor(B[k], m); }
        }
        float* red = (float*)(smem + 36864);
        if (lane < 4) {
#pragma unroll
            for (int k = 0; k < 8; ++k) { red[wv * 64 + lane * 8 + k] = S[k]; red[wv * 64 + 32 + lane * 8 + k] = B[k]; }
        }
        __syncthreads();
        if (tid < 64) {
            u32x4 o = {0u, 0u, 0u, 0u};
            if (tid < 32) {
                const float S_ = -(red[tid] + red[64 + tid] + red[128 + tid] + red[192 + tid]);
                const float B_ = red[32 + tid] + red[96 + tid] + red[160 + tid] + red[224 + tid];
                const float s1 = rbf(S_), s2 = rbf(S_ - s1), s3 = S_ - s1 - s2;
                const float b1 = rbf(B_), b2 = rbf(B_ - b1), b3 = B_ - b1 - b2;
                o[0] = pkbf(s1, s2); o[1] = pkbf(s3, b1); o[2] = pkbf(b2, b3);
            }
            *(u32x4*)(ws + WS_EX + q * 10240 + c * 1024 + (tid ^ ((q & 1) << 4)) * 16) = o;
        }
        if (c == 0 && tid >= 64 && tid < 128) {
            const int j = tid - 64, s = q & 3, kq = q >> 2;
            const int d = j < 32 ? (9 * (s >> 1) + 5 * (s & 1) + kq) * 32 + j : (9 * (s >> 1) + 4) * 32 + (j - 32);
            ((float*)(ws + WS_EX + q * 10240 + 9216))[j] = mod[5 * DM + d];
        }
        const int h = lane >> 5, blk = (lane >> 4) & 1, qq = (lane & 15) >> 2, p = lane & 3;
#pragma unroll
        for (int i = 0; i < 9; ++i) {
            const int s = wv * 9 + i;
            const u32 a = (16 * s + 8 * h + qq) * 64 + (blk ^ (q & 1)) * 32 + p * 8;
            s16x4 lo = trread(smem, a), hi = trread(smem, a + 256);
            u32x2 l2 = __builtin_bit_cast(u32x2, lo), h2 = __builtin_bit_cast(u32x2, hi);
            u32x4 o = {l2[0], l2[1], h2[0], h2[1]};
            *(u32x4*)(ws + WS_WF + (size_t)(q * 648 + c * 36 + s) * 1024 + lane * 16) = o;
        }
    }
}
constexpr u32 L_EXF = 147456;
constexpr int MLP_LDS = L_EXF + 12288;
constexpr int GE = 648;

#if defined(__HIP_DEVICE_COMPILE__)
#pragma clang attribute push(__attribute__((target("no-packed-fp32-ops"))), apply_to = function)
#endif
template <int N> struct IC { static constexpr int value = N; };
template <int A, int B, class F> DI void static_for(F&& f) {
    if constexpr (B - A == 1) f(IC<A>{});
    else if constexpr (A < B) { static_for<A, (A + B) / 2>(f); static_for<(A + B) / 2, B>(f); }
}
constexpr int imin(int a, int b) { return a < b ? a : b; }
constexpr int XW = 4;
constexpr int poll_of(int G) { return G == 226 ? 206 : G == 298 ? 278 : G == 368 ? 350 : G == 436 ? 418 : G == 622 ? 598 : -1; }
constexpr bool s_poll(int G) { return G == 206 || G == 278 || G == 350 || G == 418 || G == 598; }
constexpr int HS0 = 22, HS1 = 35;
constexpr int s_hstore(int G) { return G >= 36 && G < 324 ? (G % 36 == HS0 ? 0 : G % 36 == HS1 ? 1 : -1) : G == 338 ? 0 : G == 346 ? 1 : -1; }
constexpr int s_pstore(int G) {
    return G == 499 ? 2 : G == 503 ? 3 : G == 515 ? 0 : G == 519 ? 1 : G == 547 ? 4 : G == 551 ? 5 : G == 579 ? 6 : G == 581 ? 7 : -1;
}
constexpr bool s_res(int G) { return G == 586 || G == 588 || G == 590; }
constexpr int s_pub(int G) { return G % 36 == 24 && G >= 132 && G <= 348 ? G / 36 - 2 : G == 380 ? 9 : 0; }
constexpr int s_pub2(int G) { return G == 564 ? 1 : G == 600 ? 2 : G == 620 ? 3 : 0; }
constexpr int m_slot(int m) { return m < 140 ? (m % 7 + 1) / 2 : m < 172 ? 1 : m < 188 ? 0 : m < 252 ? 2 + (m - 188) / 32 : 4; }
constexpr int m_ent(int m) { return m < 140 ? m / 7 : m < 172 ? 20 + (m - 140) / 2 : m < 188 ? 20 + (m - 172) : m < 252 ? 20 + ((m - 188) % 32) / 2 : (m - 252) / 2; }
constexpr int m_half(int m) { return m < 140 ? (m % 7 == 0 ? 0 : (m % 7 - 1) & 1) : (m >= 172 && m < 188) ? 0 : m & 1; }
constexpr bool m_first(int m) { return m == 0 || m == 1 || m == 3 || m == 5 || m == 252; }
constexpr bool ent_first(int m) { return m < 140 ? m % 7 == 0 : (m < 172 && (m & 1) == 0); }
constexpr bool ent_own(int e) { return e < 8 || e == 20; }
struct Sched {
    int dn[GE];
    int dgap[162];
    int hl[GE];
    int pre[GE + 1];
    int hgap[36];
    constexpr Sched() : dn(), dgap(), hl(), pre(), hgap() {
        int nd = 3 * XW, nh = 0;
        for (int n = 0; n < 162; ++n) dgap[n] = -1;
        for (int e = 0; e < 36; ++e) hgap[e] = -1;
        for (int G = 0; G < GE; ++G) {
            dn[G] = -1;
            if (nd < 162 && (G < 36 ? G % 2 == 1 : G % 4 == 1)) { dn[G] = nd; dgap[nd] = G; ++nd; }
            const int lim = G > 436 ? 54 : G > 368 ? 36 : G > 298 ? 24 : G > 226 ? 12 : 0;
            const bool slot = G < 437 ? G % 4 == 3 : G % 2 == 0;
            int h = -1;
            if (nh < lim && slot) { const int k = nh / 2; h = 2 * (k < 12 ? 8 + k : 9 + k) + (nh & 1); ++nh; }
            hl[G] = h;
            if (h >= 0) hgap[h / 2] = G;
            pre[G + 1] = pre[G] + (dn[G] >= 0) + (h >= 0) + s_poll(G) + (s_hstore(G) >= 0) + (s_pstore(G) >= 0) + s_res(G) + (s_pub(G) > 0);
        }
    }
};
constexpr Sched SCH{};
constexpr int younger(int a, int b) { return imin(SCH.pre[b] - SCH.pre[a + 1], 63); }
constexpr bool s_ring(int G) { return G % 36 == 24 && G + 12 < GE; }
constexpr bool s_bar(int G) { return s_ring(G) || s_pub(G) > 0 || s_pub2(G) > 0; }
constexpr int bar_wait(int G) {
    int n = 63;
    if (s_ring(G)) n = younger(SCH.dgap[9 * (G / 36 + 2) - 1], G);
    if (s_pub(G) > 0) n = imin(n, younger(G == 380 ? 346 : 36 * s_pub(G) + HS1, G));
    if (s_pub2(G) > 0) n = imin(n, younger(G == 564 ? 519 : G == 600 ? 551 : 581, G));
    return n;
}

#define SB() __builtin_amdgcn_sched_barrier(0)
#define STAMP(i) do {} while (0)
#define TW(code) do { code; } while (0)
#define MFMA "v_mfma_f32_32x32x16_bf16 "

__global__ __launch_bounds__(256, 1) void k_mlp(char* __restrict__ ws, u16* __restrict__ out) {
    extern __shared__ __attribute__((aligned(16))) char smem[];
    const int tid = threadIdx.x, lane = tid & 63;
    const int w = __builtin_amdgcn_readfirstlane(tid >> 6);
    const int b = blockIdx.x, xcd = b & 7, rr = b >> 3;
    const int kq = xcd >> 1, it = 8 * (xcd & 1) + (rr >> 2), s = rr & 3, q = 4 * kq + s, par = s & 1;
    const auto rs = __builtin_amdgcn_make_buffer_rsrc(ws, 0, WS_END, 0x00020000);
    const u32x4 srd = {(u32)(uintptr_t)ws, (u32)((uintptr_t)ws >> 32) & 0xffffu, WS_END, 0x00020000u};
    const u32 voff = lane * 16;
    const u32 lbase = (u32)(uintptr_t)(LAS char*)smem;
    const u32 wl = lbase + w * 3072;
    auto set_m0 = [&](u32 v) { asm volatile("s_mov_b32 m0, %0" :: "s"(v) : "memory"); };
    auto dma = [&](auto O, u32 so) {
        const u32 vo = voff; const u32x4 sr = srd;
        asm volatile("buffer_load_dwordx4 %0, %1, %2 offen offset:%3 lds" :: "v"(vo), "s"(sr), "s"(so), "n"(O.value * 1024) : "memory");
    };
    auto dma3 = [&](u32 dst, u32 so) {
        asm volatile("s_mov_b32 m0, %3\n\ts_nop 0\n\tbuffer_load_dwordx4 %0, %1, %2 offen lds\n\t"
                     "buffer_load_dwordx4 %0, %1, %2 offen offset:1024 lds\n\tbuffer_load_dwordx4 %0, %1, %2 offen offset:2048 lds"
                     :: "v"(voff), "s"(srd), "s"(so), "s"(dst) : "memory");
    };
    const u32 xsb = WS_X1F + it * 147456;
    const u32 wfb = WS_WF + q * 663552 + w * 3072;
    auto issue_group = [&](auto I) {
        constexpr int i = I.value;
        if constexpr (i < 12) dma3(wl + i * 12288, xsb + w * 3072 + i * 12288);
        else if constexpr (i == 12) dma3(wl + L_EXF, WS_EX + q * 10240 + w * 3072);
        else if constexpr (i < 13 + XW) dma3(wl + (i - 13) * 12288, wfb + (i - 13) * 12288);
    };
    static_for<0, 4>(issue_group);
    const u32 hfb = WS_HF + (it * 4 + kq) * 36 * 8192 + w * 2048;

    u32x4 Xa[36];
    float sum = 0.f, sq = 0.f;
    {
        const u32 a0 = wl + (8 * (lane >> 5) + ((lane & 15) >> 2)) * 64 + ((lane >> 4) & 1) * 32 + (lane & 3) * 8;
        const bf16x2 ones = __builtin_bit_cast(bf16x2, 0x3f803f80u);
        u32x2 t[2][6];
        auto xread = [&](auto A) {
            constexpr int a = A.value;
            u32x2 (&d)[6] = t[a & 1];
            const u32 ad = a0 + a * 12288;
            asm volatile("s_waitcnt vmcnt(%7)\n\t"
                         "ds_read_b64_tr_b16 %0, %6\n\tds_read_b64_tr_b16 %1, %6 offset:256\n\t"
                         "ds_read_b64_tr_b16 %2, %6 offset:1024\n\tds_read_b64_tr_b16 %3, %6 offset:1280\n\t"
                         "ds_read_b64_tr_b16 %4, %6 offset:2048\n\tds_read_b64_tr_b16 %5, %6 offset:2304"
                         : "=&v"(d[0]), "=&v"(d[1]), "=&v"(d[2]), "=&v"(d[3]), "=&v"(d[4]), "=&v"(d[5])
                         : "v"(ad), "n"(3 * (imin(4 + 2 * a, 13 + XW) - a - 1)) : "memory");
        };
        xread(IC<0>{});
        STAMP(1);
        static_for<0, 12>([&](auto A) {
            constexpr int a = A.value;
            u32x2 (&d)[6] = t[a & 1];
            asm volatile("s_waitcnt lgkmcnt(0)" : "+v"(d[0]), "+v"(d[1]), "+v"(d[2]), "+v"(d[3]), "+v"(d[4]), "+v"(d[5]) :: "memory");
            issue_group(IC<4 + 2 * a>{}); issue_group(IC<5 + 2 * a>{});
            if constexpr (a < 11) xread(IC<a + 1>{});
#pragma unroll
            for (int z = 0; z < 3; ++z) {
                u32x4 v = {d[2 * z][0], d[2 * z][1], d[2 * z + 1][0], d[2 * z + 1][1]};
#pragma unroll
                for (int k = 0; k < 4; ++k) {
                    const u32 ek = v[k];
                    const bf16x2 e = __builtin_bit_cast(bf16x2, ek);
                    sum = __builtin_amdgcn_fdot2_f32_bf16(e, ones, sum, false);
                    sq = __builtin_amdgcn_fdot2_f32_bf16(e, e, sq, false);
                }
                Xa[3 * a + z] = v;
                asm volatile("" : "+a"(Xa[3 * a + z]));
            }
            asm volatile("" : "+v"(sum), "+v"(sq));
            SB();
        });
    }
    u32x4 Wf[18], Ef;
    const u32 rd0 = lbase + voff;
    const u32 rb[3] = {rd0, rd0 + 49152, rd0 + 98304};
    auto rdw = [&](auto I, u32 base, auto O) {
        u32x4& d = Wf[I.value];
        asm volatile("ds_read_b128 %0, %1 offset:%2" : "=v"(d) : "v"(base), "n"(O.value) : "memory");
    };
    auto rde = [&](auto C) {
        u32x4& d = Ef; const u32 base = rb[2];
        asm volatile("ds_read_b128 %0, %1 offset:%2" : "=v"(d) : "v"(base), "n"(L_EXF - 98304 + C.value * 1024) : "memory");
    };
    float ysc;
    u32x4 Xe;
    {
        sum += __builtin_bit_cast(float, __builtin_amdgcn_ds_bpermute((lane ^ 32) * 4, __builtin_bit_cast(int, sum)));
        sq += __builtin_bit_cast(float, __builtin_amdgcn_ds_bpermute((lane ^ 32) * 4, __builtin_bit_cast(int, sq)));
        asm volatile("" : "+v"(sum), "+v"(sq));
        SB();
        asm volatile("s_waitcnt vmcnt(%0)\n\ts_barrier" :: "n"(3 * (XW - 3)) : "memory");
        STAMP(2);
        static_for<0, 12>([&](auto I) { rdw(I, rd0, IC<I.value * 1024>{}); });
        rde(IC<0>{});
        SB();
        const float mean = sum * (1.f / DM);
        float var = fmaf(-mean, mean, sq * (1.f / DM));
        var = rbf(fmaxf(var, 0.f));
        const float sd = rbf(__builtin_amdgcn_sqrtf(rbf(var + 1e-5f)));
        ysc = -1.44269504088896340736f * __builtin_amdgcn_rcpf(sd);
        const bool lo = lane < 32;
        Xe = (u32x4){lo ? pkbf(mean, mean) : 0u, lo ? pkbf(mean, sd) : 0u, lo ? pkbf(sd, sd) : 0u, 0u};
        asm volatile("" : "+v"(Xe), "+v"(ysc));
        SB();
    }

    f32x16 acc[2], acc2[5];
    float yv[16], ev[16];
    u32x4 H[36][2], pollv;
    u32* const flagp = (u32*)(ws + WS_FLAG + (it * 4 + kq) * 64);
    auto publish = [&](u32 level) {
        if (tid == 0) __hip_atomic_store(flagp + s, level, __ATOMIC_RELAXED, __HIP_MEMORY_SCOPE_AGENT);
    };
    auto act = [&](auto C, auto HS) {
        constexpr int t = HS.value / 2, e = C.value < 8 ? C.value : 20;
        if constexpr (HS.value % 2 == 0) {
            if constexpr (t < 16) { yv[t] = acc[C.value & 1][t] * ysc; asm volatile("" : "+v"(yv[t])); }
            if constexpr (t >= 2) { yv[t - 2] = yv[t - 2] * ev[t - 2]; asm volatile("" : "+v"(yv[t - 2])); }
            if constexpr (t < 16) { ev[t] = __builtin_amdgcn_exp2f(yv[t]); asm volatile("" : "+v"(ev[t])); }
        } else {
            if constexpr (t >= 1 && t < 17) { ev[t - 1] = 1.f + ev[t - 1]; asm volatile("" : "+v"(ev[t - 1])); }
            if constexpr (t >= 3 && (t & 1)) {
                u32 pk = pkbf(yv[t - 3], yv[t - 2]);
                asm volatile("" : "+v"(pk));
                H[e][(t - 3) / 8][((t - 3) / 2) & 3] = pk;
            }
            if constexpr (t >= 1 && t < 17) { ev[t - 1] = __builtin_amdgcn_rcpf(ev[t - 1]); asm volatile("" : "+v"(ev[t - 1])); }
        }
    };
    const u32 vh0 = voff + par * 1024, vh1 = voff + (par ^ 1) * 1024;
    const u32 hown = hfb + 9 * s * 8192;
    const u32 vpf = (lane & 15) * 128 + (lane >> 4 & 1) * 8192;
    u32 pfd = 0;
    auto prefetch = [&](u32 vo, u32 so) {
        asm volatile("buffer_load_dword %0, %1, %2, %3 offen sc1" : "+v"(pfd) : "v"(vo), "s"(srd), "s"(so) : "memory");
    };
    const u32 hpeer[3] = {hfb + 9 * ((s + 1) & 3) * 8192, hfb + 9 * ((s + 2) & 3) * 8192, hfb + 9 * ((s + 3) & 3) * 8192};
    auto hload = [&](auto E, auto HF) {
        constexpr int e = E.value, hf = HF.value, k = e < 20 ? e - 8 : e - 9;
        const u32 so = hpeer[k % 3] + (k / 3) * 8192, vo = hf ? vh1 : vh0; const u32x4 sr = srd;
        u32x4& d = H[e][hf];
        asm volatile("buffer_load_dwordx4 %0, %1, %2, %3 offen sc1" : "=a"(d) : "v"(vo), "s"(sr), "s"(so) : "memory");
    };
    const u32 vzero = 0;
    const u32 fso1 = WS_FLAG + (it * 4 + kq) * 64;
    auto poll = [&]() {
        asm volatile("buffer_load_dwordx4 %0, %1, %2, %3 offen sc1" : "=v"(pollv) : "v"(vzero), "s"(srd), "s"(fso1) : "memory");
    };
    auto check = [&](u32 level) {
        asm volatile("" : "+v"(pollv));
#pragma nounroll
        for (u32 spins = 0; spins < (1u << 20); ++spins) {
            const bool bad = pollv[0] < level || pollv[1] < level || pollv[2] < level || pollv[3] < level;
            if (__builtin_amdgcn_ballot_w64(bad) == 0ull) break;
            __builtin_amdgcn_s_sleep(1);
            asm volatile("buffer_load_dwordx4 %0, %1, %2, %3 offen sc1\n\ts_waitcnt vmcnt(0)"
                         : "=v"(pollv) : "v"(vzero), "s"(srd), "s"(fso1) : "memory");
        }
    };
    const u32 pgb = WS_PART + (it * 4 + s) * 98304 + w * 2048;
    const u32 phb = WS_PH + (it * 2 + (s >> 1)) * 65536 + w * 16384;
    const u32 fso2 = WS_FLAG + 4096 + (it * 2 + (s >> 1)) * 64;
    const int Town = 9 * (s >> 1) + 5 * par + kq, Thalf = 9 * (s >> 1) + 4;
    auto xfrag = [&](int ks) -> u32 { return xsb + ((ks / 3) * 12 + 3 * w + ks % 3) * 1024; };
    u32 fv, ok1 = 0;
    const u32 vfl = (lane & 7) * 4;
    u32x4 P[6];
    u32 Q[8];
    const u32 vq = voff + ((kq & 1) * 2 + par) * 4;
    const u32 pin = pgb + kq * 24576, qin = phb + (kq >> 1) * 8192;
    auto publish2 = [&](u32 level) {
        if (tid == 0) __hip_atomic_store((u32*)(ws + fso2) + 2 * kq + par, level, __ATOMIC_RELAXED, __HIP_MEMORY_SCOPE_AGENT);
    };
    auto poll2 = [&]() {
        asm volatile("buffer_load_dword %0, %1, %2, %3 offen sc1" : "=v"(fv) : "v"(vfl), "s"(srd), "s"(fso2) : "memory");
    };
    auto test2 = [&](u32 level) -> u32 {
        asm volatile("" : "+v"(fv));
        return __builtin_amdgcn_readfirstlane(__builtin_amdgcn_ballot_w64(fv < level) == 0ull ? 1u : 0u);
    };
#define PLOAD2(C, r, cond, skip) \
    asm volatile("s_cmp_eq_u32 %5, " #skip "\n\ts_cbranch_scc1 1f\n\t" \
                 "buffer_load_dwordx4 %0, %2, %3, %4 offen sc1\n\tbuffer_load_dwordx4 %1, %2, %3, %4 offen offset:1024 sc1\n1:" \
                 : C(P[2 * r]), C(P[2 * r + 1]) : "v"(voff), "s"(srd), "s"(pin + r * 8192), "s"(cond) : "memory", "scc")
#define QLOAD4(C, r, cond, skip) \
    asm volatile("s_cmp_eq_u32 %7, " #skip "\n\ts_cbranch_scc1 1f\n\t" \
                 "buffer_load_dword %0, %4, %5, %6 offen sc1\n\tbuffer_load_dword %1, %4, %5, %6 offen offset:1024 sc1\n\t" \
                 "buffer_load_dword %2, %4, %5, %6 offen offset:2048 sc1\n\tbuffer_load_dword %3, %4, %5, %6 offen offset:3072 sc1\n1:" \
                 : C(Q[4 * r]), C(Q[4 * r + 1]), C(Q[4 * r + 2]), C(Q[4 * r + 3]) \
                 : "v"(vq), "s"(srd), "s"(qin + r * 4096), "s"(cond) : "memory", "scc")
    auto early = [&](int i) {
        if (i == 0) PLOAD2("=&v", 0, ok1, 0);
        if (i == 1) QLOAD4("=&v", 0, ok1, 0);
        if (i == 2) QLOAD4("=&v", 1, ok1, 0);
    };

    static_for<0, GE>([&](auto GG) {
        constexpr int G = GG.value, c = G / 36, g = G % 36, m = G - 324;
        if constexpr (g == 0 && G > 0) STAMP(2 + G / 36);
        if constexpr (s_bar(G)) {
            if constexpr (G % 6 == 0) TW(asm volatile("s_waitcnt vmcnt(%0) lgkmcnt(6)\n\ts_barrier" :: "n"(bar_wait(G)) : "memory"));
            else TW(asm volatile("s_waitcnt vmcnt(%0)\n\ts_barrier" :: "n"(bar_wait(G)) : "memory"));
        } else if constexpr (G % 6 == 0) asm volatile("s_waitcnt lgkmcnt(%0)" :: "n"(G == 0 ? 7 : G + 6 < GE ? 6 : 0) : "memory");
        u32x4& wf = Wf[G % 18];
        if constexpr (G < 324) {
            f32x16& ac = acc[c & 1]; u32x4& xa = Xa[g];
            if constexpr (g == 0) asm volatile(MFMA "%0, %1, %2, 0" : "=&v"(ac) : "v"(wf), "a"(xa));
            else asm volatile(MFMA "%0, %1, %2, %0" : "+v"(ac) : "v"(wf), "a"(xa));
            if constexpr (g == 12) {
                u32x4& ef = Ef; u32x4& xe = Xe;
                SB();
                if constexpr (c > 0) act(IC<c - 1>{}, IC<11>{});
                SB();
                asm volatile(MFMA "%0, %1, %2, %0" : "+v"(ac) : "v"(ef), "v"(xe));
            }
        } else {
            constexpr int sl = m_slot(m), e = m_ent(m), hf = m_half(m);
            if constexpr (ent_first(m) && !ent_own(e))
                TW(asm volatile("s_waitcnt vmcnt(%0)" :: "n"(younger(SCH.hgap[e], G)) : "memory"));
            f32x16& ac = acc2[sl]; u32x4& hh = H[e][hf];
            if constexpr (m_first(m)) asm volatile(MFMA "%0, %1, %2, 0" : "=&v"(ac) : "v"(wf), "v"(hh));
            else if constexpr (ent_own(e)) asm volatile(MFMA "%0, %1, %2, %0" : "+v"(ac) : "v"(wf), "v"(hh));
            else asm volatile(MFMA "%0, %1, %2, %0" : "+v"(ac) : "v"(wf), "a"(hh));
        }
        SB();
        if constexpr (G + 12 < GE) {
            constexpr int lo = ((G + 12) % 144) * 1024;
            rdw(IC<(G + 12) % 18>{}, rb[lo / 49152], IC<lo % 49152>{});
        }
        if constexpr (g == 14 && c < 8) rde(IC<c + 1>{});
        if constexpr (G + 1 < GE) {
            if constexpr (SCH.dn[G + 1] >= 0 && SCH.dn[G + 1] % 3 == 0) set_m0(wl + ((SCH.dn[G + 1] / 3) % 12) * 12288);
        }
        if constexpr (SCH.dn[G] >= 0) dma(IC<SCH.dn[G] % 3>{}, wfb + (SCH.dn[G] / 3) * 12288);
        if constexpr (G >= 36 && G < 324 && g >= 1) act(IC<c - 1>{}, IC<(g < 12 ? g - 1 : g)>{});
        if constexpr (G >= 326 && G < 344) { act(IC<8>{}, IC<2 * (G - 326)>{}); act(IC<8>{}, IC<2 * (G - 326) + 1>{}); }
        if constexpr (s_hstore(G) >= 0) {
            constexpr int hf = s_hstore(G), cc = c - 1;
            __builtin_amdgcn_raw_buffer_store_b128(H[cc < 8 ? cc : 20][hf], rs, hf ? vh1 : vh0, hown + cc * 8192, 16);
        }
        if constexpr (s_pub(G) > 0) {
            if constexpr (G == 380) prefetch(vpf, hown + 7 * 8192); else prefetch(vpf & 2047u, hown + (s_pub(G) - 1) * 8192);
            publish(s_pub(G));
        }
        if constexpr (s_pub2(G) > 0) publish2(s_pub2(G));
        if constexpr (poll_of(G) >= 0) {
            TW(asm volatile("s_waitcnt vmcnt(%0)" :: "n"(younger(poll_of(G), G)) : "memory"));
            if constexpr (G < 500) { constexpr int G_ = G; { constexpr int G = G_ + 1; TW(check(G_ == 226 ? 2 : G_ == 298 ? 4 : G_ == 368 ? 6 : 9)); } }
            else ok1 = test2(1);
        }
        if constexpr (s_poll(G)) { if constexpr (G < 500) poll(); else poll2(); }
        if constexpr (SCH.hl[G] >= 0) hload(IC<SCH.hl[G] / 2>{}, IC<SCH.hl[G] % 2>{});
        if constexpr (G == 623) early(0);
        if constexpr (G == 626) early(1);
        if constexpr (G == 630) early(2);
        if constexpr (s_pstore(G) >= 0) {
            constexpr int sg = s_pstore(G) / 2, pt = s_pstore(G) % 2;
            const f32x16& a = acc2[sg];
            const u32x4 v = {pkbf(a[8 * pt], a[8 * pt + 1]), pkbf(a[8 * pt + 2], a[8 * pt + 3]), pkbf(a[8 * pt + 4], a[8 * pt + 5]), pkbf(a[8 * pt + 6], a[8 * pt + 7])};
            if constexpr (sg == 0) __builtin_amdgcn_raw_buffer_store_b128(v, rs, voff, phb + pt * 8192 + (2 * kq + par) * 1024, 16);
            else __builtin_amdgcn_raw_buffer_store_b128(v, rs, voff + pt * 1024, pgb + ((kq + sg) & 3) * 24576 + (sg - 1) * 8192, 16);
        }
        if constexpr (s_res(G)) {
            constexpr int i = (G - 586) / 2;
            set_m0(wl + 73728 + i * 1024);
            const u32 so = xfrag(i < 2 ? 2 * Town + i : 2 * Thalf + (kq >> 1));
            asm volatile("s_nop 0" ::: "memory");
            dma(IC<0>{}, so);
        }
        SB();
    });
    STAMP(20);

    {
        u32x2 xr[5];
        f32x4 gb[4];
        f32x2 gh;
        const u32 ra = wl + 73728 + (4 * (lane >> 5) + ((lane & 15) >> 2)) * 64 + ((lane >> 4) & 1) * 32 + (lane & 3) * 8;
        const u32 ga = lbase + L_EXF + 9216 + (lane >> 5) * 16;
        poll2();
        asm volatile("ds_read_b64_tr_b16 %0, %5\n\tds_read_b64_tr_b16 %1, %5 offset:512\n\tds_read_b64_tr_b16 %2, %5 offset:1024\n\t"
                     "ds_read_b64_tr_b16 %3, %5 offset:1536\n\tds_read_b64_tr_b16 %4, %6 offset:2048"
                     : "=&v"(xr[0]), "=&v"(xr[1]), "=&v"(xr[2]), "=&v"(xr[3]), "=&v"(xr[4]) : "v"(ra), "v"(ra + (kq & 1) * 512) : "memory");
        asm volatile("ds_read_b128 %0, %5\n\tds_read_b128 %1, %5 offset:32\n\tds_read_b128 %2, %5 offset:64\n\tds_read_b128 %3, %5 offset:96\n\t"
                     "ds_read_b64 %4, %6 offset:128\n\ts_waitcnt vmcnt(0) lgkmcnt(0)"
                     : "=&v"(gb[0]), "=&v"(gb[1]), "=&v"(gb[2]), "=&v"(gb[3]), "=&v"(gh)
                     : "v"(ga), "v"(ga + kq * 32 + par * 8) : "memory");
        asm volatile("" : "+v"(xr[0]), "+v"(xr[1]), "+v"(xr[2]), "+v"(xr[3]), "+v"(xr[4]), "+v"(fv));
        asm volatile("" :: "v"(pfd));
#pragma nounroll
        for (u32 spins = 0; spins < (1u << 20); ++spins) {
            if (__builtin_amdgcn_ballot_w64(fv < 3u) == 0ull) break;
            __builtin_amdgcn_s_sleep(1);
            asm volatile("buffer_load_dword %0, %1, %2, %3 offen sc1\n\ts_waitcnt vmcnt(0)" : "=v"(fv) : "v"(vfl), "s"(srd), "s"(fso2) : "memory");
        }
        STAMP(21);
        PLOAD2("=&v", 2, ok1, 2);
        PLOAD2("=&v", 1, ok1, 2);
        PLOAD2("+v", 0, ok1, 1);
        QLOAD4("+v", 0, ok1, 1);
        QLOAD4("+v", 1, ok1, 1);
        asm volatile("s_waitcnt vmcnt(0)" : "+v"(P[0]), "+v"(P[1]), "+v"(P[2]), "+v"(P[3]), "+v"(P[4]), "+v"(P[5]) :: "memory");
        asm volatile("" : "+v"(Q[0]), "+v"(Q[1]), "+v"(Q[2]), "+v"(Q[3]), "+v"(Q[4]), "+v"(Q[5]), "+v"(Q[6]), "+v"(Q[7]));
        STAMP(22);
        u16* orow = out + (size_t)(it * 128 + w * 32 + (lane & 31)) * DM + 4 * (lane >> 5);
        auto fin2 = [&](float a0, float a1, float g0, float g1, u32 xk) -> u32 {
            const u32 t = pkbf(a0 * -0.69314718055994530942f, a1 * -0.69314718055994530942f);
            const u32 gz = pkbf(bflo(t) * g0, bfhi(t) * g1);
            return pkbf(bflo(xk) + bflo(gz), bfhi(xk) + bfhi(gz));
        };
#pragma unroll
        for (int k = 0; k < 4; ++k) {
            u32x2 o;
#pragma unroll
            for (int j = 0; j < 2; ++j) {
                const int d = 2 * k + j;
                const u32 p0 = P[d >> 2][d & 3], p1 = P[2 + (d >> 2)][d & 3], p2 = P[4 + (d >> 2)][d & 3], xk = xr[k][j];
                o[j] = fin2(acc2[4][2 * d] + bflo(p0) + bflo(p1) + bflo(p2), acc2[4][2 * d + 1] + bfhi(p0) + bfhi(p1) + bfhi(p2),
                            gb[k][2 * j], gb[k][2 * j + 1], xk);
            }
            *(u32x2*)(orow + Town * 32 + 8 * k) = o;
        }
        float a0 = 0.f, a1 = 0.f;
#pragma unroll
        for (int r = 0; r < 8; ++r) { a0 += bflo(Q[r]); a1 += bfhi(Q[r]); }
        *(u32*)(orow + Thalf * 32 + 8 * kq + 2 * par) = fin2(a0, a1, gh[0], gh[1], par ? xr[4][1] : xr[4][0]);
    }
}
#if defined(__HIP_DEVICE_COMPILE__)
#pragma clang attribute pop
#endif

extern "C" void kernel_launch(void* const* d_in, const int* in_sizes, int n_in, void* d_out, int out_size,
                              void* d_ws, size_t ws_size, hipStream_t stream) {
    const u16* x = (const u16*)d_in[0];
    char* ws = (char*)d_ws;
    (void)hipFuncSetAttribute((const void*)k_mlp, hipFuncAttributeMaxDynamicSharedMemorySize, MLP_LDS);
    k_prep<<<NB_PREP, 256, PREP_LDS, stream>>>(x, (const u16*)d_in[1], (const u16*)d_in[2], (const u16*)d_in[3],
                                               (const u16*)d_in[4], (const u16*)d_in[5], (const u16*)d_in[9],
                                               (const u16*)d_in[10], (const u16*)d_in[11], ws);
    k_conv<<<NB_CONV + NB_W1, 256, CONV_LDS, stream>>>((const u16*)d_in[6], (const u16*)d_in[8], ws);
    k_mlp<<<256, 256, MLP_LDS, stream>>>(ws, (u16*)d_out);
}
```

```cpp
#include <hip/hip_runtime.h>
#include <stdint.h>

typedef unsigned short u16;
typedef unsigned int u32;
typedef float f32x2 __attribute__((ext_vector_type(2)));
typedef float f32x4 __attribute__((ext_vector_type(4)));
typedef float f32x16 __attribute__((ext_vector_type(16)));
typedef _Float16 f16x2 __attribute__((ext_vector_type(2)));
typedef _Float16 f16x8 __attribute__((ext_vector_type(8)));
typedef __bf16 bf16x2 __attribute__((ext_vector_type(2)));
typedef __bf16 bf16x8 __attribute__((ext_vector_type(8)));
typedef short s16x4 __attribute__((ext_vector_type(4)));
typedef u32 u32x2 __attribute__((ext_vector_type(2)));
typedef u32 u32x4 __attribute__((ext_vector_type(4)));
#define LAS __attribute__((address_space(3)))
#define DI __device__ __forceinline__
#define RTSTAMP(k)
#define MARK(i)

constexpr int SEQ = 2048, DM = 576, NS = 128, FF = 4608;

constexpr u32 WS_MOD   = 0;
constexpr u32 WS_STAT  = 16384;
constexpr u32 WS_KSC   = 32768;
constexpr u32 WS_FLAG  = 36864;
constexpr u32 WS_EX    = 65536;
constexpr u32 WS_XT    = 1u << 20;
constexpr u32 WS_KR    = WS_XT + 2359296;
constexpr u32 WS_X1F   = WS_KR + 2359296;
constexpr u32 WS_WF    = WS_X1F + 2359296;
constexpr u32 WS_HF    = WS_WF + 10616832;
constexpr u32 WS_PART  = WS_HF + 18874368;
constexpr u32 WS_PH    = WS_PART + 6291456;
constexpr u32 WS_END   = WS_PH + 2097152;

DI float bflo(u32 u) { return __builtin_bit_cast(float, u << 16); }
DI float bfhi(u32 u) { return __builtin_bit_cast(float, u & 0xffff0000u); }
DI float bf2f(u16 v) { return __builtin_bit_cast(float, ((u32)v) << 16); }
DI u32 pkbf(float a, float b) { f32x2 v = {a, b}; return __builtin_bit_cast(u32, __builtin_convertvector(v, bf16x2)); }
DI u32 pkhf(float a, float b) { f32x2 v = {a, b}; return __builtin_bit_cast(u32, __builtin_convertvector(v, f16x2)); }
DI float rbf(float a) { return bflo(pkbf(a, a)); }
template <int CTRL, int RM = 0xf> DI float dppf(float v) {
    return __builtin_bit_cast(float, __builtin_amdgcn_update_dpp(0, __builtin_bit_cast(int, v), CTRL, RM, 0xf, true));
}
DI u32x4 trread2(const char* smem, u32 off0, u32 off1) {
    const u32x2 l = __builtin_bit_cast(u32x2, __builtin_amdgcn_ds_read_tr16_b64_v4i16((LAS s16x4*)(smem + off0)));
    const u32x2 h = __builtin_bit_cast(u32x2, __builtin_amdgcn_ds_read_tr16_b64_v4i16((LAS s16x4*)(smem + off1)));
    return (u32x4){l[0], l[1], h[0], h[1]};
}
DI void st16(char* ws, u32 off, u32x4 v) {
    __builtin_amdgcn_raw_buffer_store_b128(v, __builtin_amdgcn_make_buffer_rsrc(ws, 0, WS_END, 0x00020000), off, 0, 16);
}
DI void st4(char* ws, u32 off, u32 v) {
    __builtin_amdgcn_raw_buffer_store_b32(v, __builtin_amdgcn_make_buffer_rsrc(ws, 0, WS_END, 0x00020000), off, 0, 16);
}
DI float pow2i(int e) { e = e < -126 ? -126 : (e > 127 ? 127 : e); return __builtin_bit_cast(float, (u32)(e + 127) << 23); }
DI int expo(float x) { return (int)((__builtin_bit_cast(u32, x) >> 23) & 255u) - 127; }
DI u32 imgrow(u32 row) { return row * 1184u + (row >> 3) * 128u; }
DI f32x2 cmul(f32x2 z, f32x2 w, f32x2 wp) {
    const f32x2 zx = {z[0], z[0]}, zy = {z[1], z[1]};
    return __builtin_elementwise_fma(zy, wp, zx * w);
}
#define CODE_PREFETCH(sym) do { u32 lo_, hi_, len_; \
    asm volatile("s_getpc_b64 s[20:21]\n\ts_add_u32 s20, s20, " #sym "@rel32@lo+4\n\ts_addc_u32 s21, s21, " #sym "@rel32@hi+12\n\t" \
                 "s_mov_b32 %0, s20\n\ts_mov_b32 %1, s21\n\ts_mov_b32 %2, " #sym "_end-" #sym : "=s"(lo_), "=s"(hi_), "=s"(len_) :: "scc", "s20", "s21"); \
    const char* p_ = (const char*)(((unsigned long long)hi_ << 32) | lo_); \
    if (threadIdx.x * 128u < len_) { u32 t_; asm volatile("global_load_dword %0, %1, off\n\ts_waitcnt vmcnt(0)" : "=v"(t_) : "v"(p_ + threadIdx.x * 128u) : "memory"); } \
    } while (0)

#define DELAY1 64
#define DELAY2 40
constexpr int NB_PF = 16, NB_LN = 64, NB_GEMV = 108, NB_KGEN = 576, NB_W2 = 144;
constexpr int NB_PREP = NB_PF + NB_LN + NB_GEMV + NB_KGEN + NB_W2;
constexpr int PREP_LDS = 38400;

extern "C" __global__ __launch_bounds__(256, 4) void k_prep(const u16* __restrict__ x, const u16* __restrict__ logA,
                                              const u16* __restrict__ Aim, const u16* __restrict__ Cre,
                                              const u16* __restrict__ Cim, const u16* __restrict__ logdt,
                                              const u16* __restrict__ w2, const u16* __restrict__ cvec,
                                              const u16* __restrict__ wmod, char* __restrict__ ws) {
    extern __shared__ __attribute__((aligned(16))) char smem[];
    RTSTAMP(0);
    const int tid = threadIdx.x, lane = tid & 63;
    const int wv = __builtin_amdgcn_readfirstlane(tid >> 6);
    int b = blockIdx.x;
    if (b < NB_PF) {
        if (b < 8) CODE_PREFETCH(k_prep); else CODE_PREFETCH(k_conv);
        if (b < 2) st16(ws, WS_FLAG + b * 4096 + tid * 16, (u32x4){0u, 0u, 0u, 0u});
    } else if ((b -= NB_PF) < NB_LN) {
        const int t0 = b * 32, tok = tid >> 3, sub = tid & 7;
        u32x4 v[9];
#pragma unroll
        for (int i = 0; i < 9; ++i) v[i] = *(const u32x4*)(x + (size_t)(t0 + tok) * DM + (sub + 8 * i) * 8);
        MARK(0);
#pragma unroll
        for (int i = 0; i < 9; ++i) *(u32x4*)(smem + imgrow(tok) + (sub + 8 * i) * 16) = v[i];
        __syncthreads();
        const int g = lane >> 4, qq = (lane & 15) >> 2, p = lane & 3;
#pragma unroll
        for (int i = 0; i < 9; ++i) {
            const int cb = wv * 9 + i;
            st16(ws, WS_XT + b * 36864 + cb * 1024 + (lane & 15) * 64 + g * 16,
                 trread2(smem, imgrow(8 * g + qq) + cb * 32 + p * 8, imgrow(8 * g + 4 + qq) + cb * 32 + p * 8));
        }
        MARK(1);
        float s = 0.f;
#pragma unroll
        for (int i = 0; i < 9; ++i)
#pragma unroll
            for (int k = 0; k < 4; ++k) s += bflo(v[i][k]) + bfhi(v[i][k]);
        s += dppf<0xB1>(s); s += dppf<0x4E>(s); s += dppf<0x141>(s);
        const float mean = s * (1.f / DM);
        float q = 0.f;
#pragma unroll
        for (int i = 0; i < 9; ++i)
#pragma unroll
            for (int k = 0; k < 4; ++k) {
                const float a = bflo(v[i][k]) - mean, c = bfhi(v[i][k]) - mean;
                q = fmaf(a, a, q); q = fmaf(c, c, q);
            }
        q += dppf<0xB1>(q); q += dppf<0x4E>(q); q += dppf<0x141>(q);
        if (sub == 0) st4(ws, WS_STAT + (t0 + tok) * 4, pkbf(mean, __builtin_amdgcn_sqrtf(rbf(rbf(q * (1.f / DM)) + 1e-5f))));
    } else if ((b -= NB_LN) < NB_GEMV) {
        const int seg = tid & 3, rg = tid >> 2;
        u32x4 v[9]; u16 cv[9];
#pragma unroll
        for (int i = 0; i < 9; ++i) {
            const int row = rg + 64 * i;
            cv[i] = cvec[row];
            v[i] = *(const u32x4*)(wmod + (size_t)row * (6 * DM) + b * 32 + seg * 8);
        }
        MARK(0);
        float acc[8];
#pragma unroll
        for (int k = 0; k < 8; ++k) acc[k] = 0.f;
#pragma unroll
        for (int i = 0; i < 9; ++i) {
            const float c = bf2f(cv[i]);
            const float sl = rbf(c * __builtin_amdgcn_rcpf(1.f + __builtin_amdgcn_exp2f(c * -1.44269504088896340736f)));
#pragma unroll
            for (int k = 0; k < 4; ++k) {
                acc[2 * k] = fmaf(sl, bflo(v[i][k]), acc[2 * k]);
                acc[2 * k + 1] = fmaf(sl, bfhi(v[i][k]), acc[2 * k + 1]);
            }
        }
#pragma unroll
        for (int k = 0; k < 8; ++k) { acc[k] += dppf<0x114>(acc[k]); acc[k] += dppf<0x118>(acc[k]); }
        float* red = (float*)smem;
        if ((lane & 15) >= 12) {
            *(f32x4*)(red + (tid >> 4) * 32 + seg * 8) = (f32x4){acc[0], acc[1], acc[2], acc[3]};
            *(f32x4*)(red + (tid >> 4) * 32 + seg * 8 + 4) = (f32x4){acc[4], acc[5], acc[6], acc[7]};
        }
        __syncthreads();
        MARK(1);
        if (tid < 32) {
            float t = 0.f;
#pragma unroll
            for (int i = 0; i < 16; ++i) t += red[i * 32 + tid];
            st4(ws, WS_MOD + (b * 32 + tid) * 4, __builtin_bit_cast(u32, rbf(t)));
        }
    } else if ((b -= NB_GEMV) < NB_KGEN) {
        const int d = b, hn = wv & 1, grp = wv >> 1;
        const int n = hn * 64 + lane, n2 = n ^ 64;
        const u16 la_ = logA[d * NS + n], ai_ = Aim[d * NS + n], cr_ = Cre[d * NS + n], ci_ = Cim[d * NS + n];
        const u16 cr2_ = Cre[d * NS + n2], ci2_ = Cim[d * NS + n2], ld_ = logdt[d];
        u32 ta[8];
#pragma unroll
        for (int j = 0; j < 8; j += 2) {
            const float l0 = (float)(2047 - tid * 8 - j);
            const u32 pk = pkbf(l0, l0 - 1.f);
            const float p0 = bflo(pk), p1 = bfhi(pk);
            ta[j] = (u32)fminf(4.f * p0, p0 + 1536.f); ta[j + 1] = (u32)fminf(4.f * p1, p1 + 1536.f);
        }
        asm volatile("" : "+v"(ta[0]), "+v"(ta[1]), "+v"(ta[2]), "+v"(ta[3]), "+v"(ta[4]), "+v"(ta[5]), "+v"(ta[6]), "+v"(ta[7]));
        MARK(0);
        const float cr = bf2f(cr_), ci = bf2f(ci_);
        const float dt = rbf(__builtin_amdgcn_exp2f(bf2f(ld_) * 1.44269504088896340736f));
        const float zr = -rbf(__builtin_amdgcn_exp2f(bf2f(la_) * 1.44269504088896340736f)) * dt, zi = bf2f(ai_) * dt;
        float bs = fabsf(cr) + fabsf(ci) + fabsf(bf2f(cr2_)) + fabsf(bf2f(ci2_));
        bs += dppf<0xB1>(bs); bs += dppf<0x4E>(bs); bs += dppf<0x141>(bs); bs += dppf<0x140>(bs);
        bs += dppf<0x142, 0xa>(bs); bs += dppf<0x143, 0xc>(bs);
        bs = __builtin_bit_cast(float, __builtin_amdgcn_readlane(__builtin_bit_cast(int, bs), 63));
        const int se = 14 - expo(2.f * dt * bs * 1.0001f);
        const float scale = pow2i(se > 100 ? 100 : se);
        const float zr2 = zr * 1.44269504088896340736f;
        auto cexp = [&](float p) -> f32x2 {
            const float mag = __builtin_amdgcn_exp2f(zr2 * p);
            const float ang = zi * p;
            const float rr = __builtin_rintf(ang * 0.15915494309189535f);
            float fr = fmaf(ang, 0.15915494309189535f, -rr);
            fr = fmaf(ang, 6.4206382e-9f, fr);
            return (f32x2){mag * __builtin_amdgcn_cosf(fr), mag * __builtin_amdgcn_sinf(fr)};
        };
        const float p1 = grp ? 4.f : 1.f;
        const f32x2 f1 = cexp(p1), c1 = cexp(16.f * p1), s0 = cexp(grp ? 512.f : 0.f), s1 = cexp(256.f * p1);
        const f32x2 fw = {f1[0], -f1[1]}, fwp = {f1[1], f1[0]};
        const f32x2 cwp = {-c1[1], c1[0]};
        const float es = 2.f * dt * scale;
        f32x2 za = cmul((f32x2){es * cr, es * ci}, s0, (f32x2){-s0[1], s0[0]});
        f32x2 zb = cmul(za, s1, (f32x2){-s1[1], s1[0]});
        f32x2 zf = {1.f, 0.f};
        char* tab = smem + wv * 8704;
        u32 keep[16];
#pragma unroll
        for (int k = 0; k < 16; ++k) {
            *(u32*)(tab + k * 272 + lane * 4) = pkhf(zf[0], zf[1]);
            *(u32*)(tab + (16 + k) * 272 + lane * 4) = pkhf(za[0], za[1]);
            keep[k] = pkhf(zb[0], zb[1]);
            zf = cmul(zf, fw, fwp); za = cmul(za, c1, cwp); zb = cmul(zb, c1, cwp);
        }
        const int r16 = lane & 15, g = lane >> 4;
        f16x8 fb[4];
        f32x4 acc0 = {0.f, 0.f, 0.f, 0.f}, acc1 = {0.f, 0.f, 0.f, 0.f};
        __builtin_amdgcn_fence(__ATOMIC_RELEASE, "wavefront");
        __builtin_amdgcn_wave_barrier();
#pragma unroll
        for (int ks = 0; ks < 4; ++ks) {
            fb[ks] = *(const f16x8*)(tab + r16 * 272 + ks * 64 + g * 16);
            const f16x8 fa = *(const f16x8*)(tab + (16 + r16) * 272 + ks * 64 + g * 16);
            acc0 = __builtin_amdgcn_mfma_f32_16x16x32_f16(fb[ks], fa, acc0, 0, 0, 0);
        }
        __builtin_amdgcn_fence(__ATOMIC_RELEASE, "wavefront");
        __builtin_amdgcn_wave_barrier();
#pragma unroll
        for (int k = 0; k < 16; ++k) *(u32*)(tab + (16 + k) * 272 + lane * 4) = keep[k];
        __builtin_amdgcn_fence(__ATOMIC_RELEASE, "wavefront");
        __builtin_amdgcn_wave_barrier();
#pragma unroll
        for (int ks = 0; ks < 4; ++ks) {
            const f16x8 fa = *(const f16x8*)(tab + (16 + r16) * 272 + ks * 64 + g * 16);
            acc1 = __builtin_amdgcn_mfma_f32_16x16x32_f16(fb[ks], fa, acc1, 0, 0, 0);
        }
        MARK(1);
        __builtin_amdgcn_fence(__ATOMIC_RELEASE, "wavefront");
        __builtin_amdgcn_wave_barrier();
        *(f32x4*)(tab + r16 * 64 + g * 16) = acc0;
        *(f32x4*)(tab + 1024 + r16 * 64 + g * 16) = acc1;
        __syncthreads();
        float kv[8];
#pragma unroll
        for (int j = 0; j < 8; ++j) {
            const u32 o = ta[j] + (ta[j] >= 2048u ? 2u * 8704u - 2048u : 0u);
            kv[j] = *(const float*)(smem + o) + *(const float*)(smem + o + 8704);
        }
        MARK(2);
        st16(ws, WS_KR + d * 4096 + tid * 16, (u32x4){pkhf(kv[0], kv[1]), pkhf(kv[2], kv[3]), pkhf(kv[4], kv[5]), pkhf(kv[6], kv[7])});
        if (tid == 0) st4(ws, WS_KSC + d * 4, __builtin_bit_cast(u32, pow2i(se > 100 ? -100 : -se)));
    } else {
        b -= NB_KGEN;
        const u16* src = w2 + (size_t)b * 32 * DM;
        if (DELAY1 > 0) __builtin_amdgcn_s_sleep(DELAY1);
        const int tok = tid >> 3, sub = tid & 7;
        u32x4 v[9];
#pragma unroll
        for (int i = 0; i < 9; ++i) v[i] = *(const u32x4*)(src + tok * DM + (sub + 8 * i) * 8);
        MARK(0);
#pragma unroll
        for (int i = 0; i < 9; ++i) *(u32x4*)(smem + imgrow(tok) + (sub + 8 * i) * 16) = v[i];
        __syncthreads();
        const int kq = b / 36, kk = b - kq * 36, sp = kk / 9, c = kk - sp * 9;
        const int par = wv & 1, rel = (sp - wv) & 3;
        const int e = rel == 0 ? (c < 8 ? c : 20) : (c < 4 ? 8 : 9) + c * 3 + (rel - 1);
        const int h = lane >> 5, blk = (lane >> 4) & 1, qq = (lane & 15) >> 2, p = lane & 3;
#pragma unroll
        for (int i = 0; i < 9; ++i) {
            const int sg = (i + 1) >> 1, s2r = i == 0 ? 0 : (i - 1) & 1;
            const int T = 9 * (wv >> 1) + (sg == 0 ? 4 : 5 * par + ((kq + sg) & 3));
            const int m = sg == 4 ? 252 + 2 * e + s2r
                        : e < 20 ? 7 * e + i
                        : sg == 0 ? 172 + (e - 20) : (sg == 1 ? 140 : 188 + (sg - 2) * 32) + (e - 20) * 2 + s2r;
            const int r0 = 16 * (s2r ^ par) + 4 * h + qq;
            const u32 col = T * 64 + blk * 32 + p * 8;
            st16(ws, WS_WF + ((kq * 4 + wv) * 648 + 324 + m) * 1024 + lane * 16, trread2(smem, imgrow(r0) + col, imgrow(r0 + 8) + col));
        }
    }
    asm volatile("k_prep_end:");
}

constexpr int NB_CONV = 576, NB_W1 = 144;
constexpr int CONV_LDS = 36864 + 4096;
constexpr int UCP = 4640, KB_OFF = 4 * UCP;

extern "C" __global__ __launch_bounds__(256, 4) void k_conv(const u16* __restrict__ dskip, const u16* __restrict__ w1,
                                              char* __restrict__ ws) {
    extern __shared__ __attribute__((aligned(16))) char smem[];
    const int tid = threadIdx.x, lane = tid & 63;
    const int wv = __builtin_amdgcn_readfirstlane(tid >> 6);
    RTSTAMP(1);
    const float* mod = (const float*)(ws + WS_MOD);
    int b = blockIdx.x;
    if (b < NB_PF) {
        CODE_PREFETCH(k_mlp);
    } else if ((b -= NB_PF) < NB_W1) {
        const int q = b / 9, c = b - q * 9, f0 = b * 32;
        if (DELAY2 > 0) __builtin_amdgcn_s_sleep(DELAY2);
        const int seg = tid & 3, rg = tid >> 2;
        u32x4 v[9]; float sb[9], cb[9];
#pragma unroll
        for (int i = 0; i < 9; ++i) {
            const int row = rg + 64 * i;
            v[i] = *(const u32x4*)(w1 + (size_t)row * FF + f0 + seg * 8);
            sb[i] = mod[3 * DM + row]; cb[i] = mod[4 * DM + row];
        }
        MARK(0);
        float S[8], B[8];
#pragma unroll
        for (int k = 0; k < 8; ++k) { S[k] = 0.f; B[k] = 0.f; }
#pragma unroll
        for (int i = 0; i < 9; ++i) {
            const float c1 = rbf(1.f + cb[i]);
            u32x4 o;
#pragma unroll
            for (int k = 0; k < 4; ++k) {
                const float lo = bflo(v[i][k]), hi = bfhi(v[i][k]);
                o[k] = pkbf(lo * c1, hi * c1);
                S[2 * k] += bflo(o[k]); S[2 * k + 1] += bfhi(o[k]);
                B[2 * k] = fmaf(sb[i], lo, B[2 * k]); B[2 * k + 1] = fmaf(sb[i], hi, B[2 * k + 1]);
            }
            *(u32x4*)(smem + (tid + 256 * i) * 16) = o;
        }
#pragma unroll
        for (int k = 0; k < 8; ++k) {
            S[k] += dppf<0x114>(S[k]); S[k] += dppf<0x118>(S[k]);
            B[k] += dppf<0x114>(B[k]); B[k] += dppf<0x118>(B[k]);
        }
        float* red = (float*)(smem + 36864);
        if ((lane & 15) >= 12) {
            float* r = red + (tid >> 4) * 64 + seg * 8;
            *(f32x4*)(r) = (f32x4){S[0], S[1], S[2], S[3]}; *(f32x4*)(r + 4) = (f32x4){S[4], S[5], S[6], S[7]};
            *(f32x4*)(r + 32) = (f32x4){B[0], B[1], B[2], B[3]}; *(f32x4*)(r + 36) = (f32x4){B[4], B[5], B[6], B[7]};
        }
        __syncthreads();
        MARK(1);
        const int h = lane >> 5, blk = (lane >> 4) & 1, qq = (lane & 15) >> 2, p = lane & 3;
#pragma unroll
        for (int i = 0; i < 9; ++i) {
            const int s = wv * 9 + i;
            const u32 a = (16 * s + 8 * h + qq) * 64 + (blk ^ (q & 1)) * 32 + p * 8;
            st16(ws, WS_WF + (q * 648 + c * 36 + s) * 1024 + lane * 16, trread2(smem, a, a + 256));
        }
        if (wv == 0) {
            float t = 0.f;
#pragma unroll
            for (int i = 0; i < 16; ++i) t += red[i * 64 + lane];
            const float B_ = __builtin_bit_cast(float, __builtin_amdgcn_ds_bpermute((lane ^ 32) * 4, __builtin_bit_cast(int, t)));
            const float S_ = -t;
            const float s1 = rbf(S_), s2 = rbf(S_ - s1), s3 = S_ - s1 - s2;
            const float b1 = rbf(B_), b2 = rbf(B_ - b1), b3 = B_ - b1 - b2;
            const bool lo = lane < 32;
            st16(ws, WS_EX + q * 10240 + c * 1024 + (lane ^ ((q & 1) << 4)) * 16,
                 (u32x4){lo ? pkbf(s1, s2) : 0u, lo ? pkbf(s3, b1) : 0u, lo ? pkbf(b2, b3) : 0u, 0u});
        } else if (wv == 1 && c == 0) {
            const int s = q & 3, kq = q >> 2;
            const int d = lane < 32 ? (9 * (s >> 1) + 5 * (s & 1) + kq) * 32 + lane : (9 * (s >> 1) + 4) * 32 + (lane - 32);
            st4(ws, WS_EX + q * 10240 + 9216 + lane * 4, __builtin_bit_cast(u32, mod[5 * DM + d]));
        }
    } else {
        const int d = b - NB_W1;
        const int tp = tid > 0 ? tid * 8 - 4 : 0;
        const char* xb = ws + WS_XT + d * 64;
        const u32x4 x0 = *(const u32x4*)(xb + (tid >> 2) * 36864 + (tid & 3) * 16);
        const u32x2 xm = *(const u32x2*)(xb + (tp >> 5) * 36864 + (tp & 31) * 2);
        u32x4 st[3];
        st[0] = *(const u32x4*)(ws + WS_STAT + tp * 4);
        st[1] = *(const u32x4*)(ws + WS_STAT + tid * 32);
        st[2] = *(const u32x4*)(ws + WS_STAT + tid * 32 + 16);
        const u32x4 kv = *(const u32x4*)(ws + WS_KR + d * 4096 + tid * 16);
        const float sa = mod[d], ca = mod[DM + d], ga = mod[2 * DM + d];
        const float dsk = bf2f(dskip[d]);
        const float kinv = ((const float*)(ws + WS_KSC))[d];
        {
            const u32x4 z = {0u, 0u, 0u, 0u};
            if (tid < 96) *(u32x4*)(smem + (tid / 24) * UCP + (tid % 24) * 16) = z;
            else if (tid < 104) *(u32x4*)(smem + KB_OFF + 4096 + (tid - 96) * 16) = z;
        }
        MARK(0);
        const float c1 = rbf(1.f + ca);
        const int he = expo(64.f * fabsf(c1) + 2.f * fabsf(sa)) - 14;
        const float hs = pow2i(-he), oscale = pow2i(he) * kinv;
        const u32 xw[6] = {xm[0], xm[1], x0[0], x0[1], x0[2], x0[3]};
        u32 P[6], hb[4];
#pragma unroll
        for (int k = 0; k < 6; ++k) {
            const u32 s0 = st[k >> 1][(k & 1) * 2], s1 = st[k >> 1][(k & 1) * 2 + 1];
            u32 t = pkbf(bflo(xw[k]) - bflo(s0), bfhi(xw[k]) - bflo(s1));
            t = pkbf(bflo(t) * __builtin_amdgcn_rcpf(bfhi(s0)), bfhi(t) * __builtin_amdgcn_rcpf(bfhi(s1)));
            t = pkbf(bflo(t) * c1, bfhi(t) * c1);
            t = pkbf(bflo(t) + sa, bfhi(t) + sa);
            if (k >= 2) hb[k - 2] = t;
            P[k] = pkhf(bflo(t) * hs, bfhi(t) * hs);
        }
        if (tid == 0) { P[0] = 0u; P[1] = 0u; }
        u32 Q[5];
#pragma unroll
        for (int k = 0; k < 5; ++k) Q[k] = __builtin_amdgcn_alignbit(P[k + 1], P[k], 16);
        {
            char* ub = smem + 384 + tid * 16;
            *(u32x4*)(ub) = (u32x4){Q[0], Q[1], Q[2], Q[3]};
            *(u32x4*)(ub + UCP) = (u32x4){P[1], P[2], P[3], P[4]};
            *(u32x4*)(ub + 2 * UCP) = (u32x4){Q[1], Q[2], Q[3], Q[4]};
            *(u32x4*)(ub + 3 * UCP) = (u32x4){P[2], P[3], P[4], P[5]};
            *(u32x4*)(smem + KB_OFF + tid * 16) = kv;
        }
        __syncthreads();
        MARK(1);
        const int c = lane & 15, g = lane >> 4;
        f32x4 acc[8];
#pragma unroll
        for (int j = 0; j < 8; ++j) acc[j] = (f32x4){0.f, 0.f, 0.f, 0.f};
#pragma unroll
        for (int rr = 0; rr < 2; ++rr) {
            const int r = 2 * wv + rr;
            const char* kb = smem + KB_OFF + 2 * (2076 - 4 * c + 8 * g - 32 * r);
            const char* ub = smem + (c & 3) * UCP + 2 * (8 * g + 64 * (c >> 2) + 32 * (1 - r) + 192);
            f16x8 kf[8], uf[8];
#pragma unroll
            for (int i = 0; i < 8; ++i) {
                const u32x2 a = *(const u32x2*)(kb - i * 512), a2 = *(const u32x2*)(kb - i * 512 + 8);
                kf[i] = __builtin_bit_cast(f16x8, (u32x4){a[0], a[1], a2[0], a2[1]});
                uf[i] = *(const f16x8*)(ub + i * 512);
            }
#pragma unroll
            for (int i = 0; i < 8; ++i)
#pragma unroll
                for (int dl = 0; dl + i < 8; ++dl)
                    acc[i + dl] = __builtin_amdgcn_mfma_f32_16x16x32_f16(uf[dl], kf[i], acc[i + dl], 0, 0, 0);
        }
        __syncthreads();
        MARK(2);
#pragma unroll
        for (int j = 0; j < 8; ++j) *(f32x4*)(smem + wv * 8192 + j * 1024 + lane * 16) = acc[j];
        __syncthreads();
        f32x4 y0 = *(const f32x4*)(smem + tid * 32), y1 = *(const f32x4*)(smem + tid * 32 + 16);
#pragma unroll
        for (int w = 1; w < 4; ++w) {
            y0 += *(const f32x4*)(smem + w * 8192 + tid * 32);
            y1 += *(const f32x4*)(smem + w * 8192 + tid * 32 + 16);
        }
        const float y[8] = {y0[0], y0[1], y0[2], y0[3], y1[0], y1[1], y1[2], y1[3]};
        u32x4 o;
#pragma unroll
        for (int k = 0; k < 4; ++k) {
            const u32 yb = pkbf(y[2 * k] * oscale, y[2 * k + 1] * oscale);
            const u32 sk = pkbf(bflo(hb[k]) * dsk, bfhi(hb[k]) * dsk);
            const u32 z = pkbf(bflo(yb) + bflo(sk), bfhi(yb) + bfhi(sk));
            const u32 gz = pkbf(bflo(z) * ga, bfhi(z) * ga);
            o[k] = pkbf(bflo(x0[k]) + bflo(gz), bfhi(x0[k]) + bfhi(gz));
        }
        MARK(3);
        st16(ws, WS_X1F + (tid >> 4) * 147456 + ((d / 48) * 12 + ((tid >> 2) & 3) * 3 + (d >> 4) % 3) * 1024 + (d & 15) * 64 + (tid & 3) * 16, o);
    }
    asm volatile("k_conv_end:");
}
constexpr u32 L_EXF = 147456;
constexpr int MLP_LDS = L_EXF + 12288;
constexpr int GE = 648;

#if defined(__HIP_DEVICE_COMPILE__)
#pragma clang attribute push(__attribute__((target("no-packed-fp32-ops"))), apply_to = function)
#endif
template <int N> struct IC { static constexpr int value = N; };
template <int A, int B, class F> DI void static_for(F&& f) {
    if constexpr (B - A == 1) f(IC<A>{});
    else if constexpr (A < B) { static_for<A, (A + B) / 2>(f); static_for<(A + B) / 2, B>(f); }
}
constexpr int imin(int a, int b) { return a < b ? a : b; }
constexpr int XW = 4;
constexpr int poll_of(int G) { return G == 226 ? 206 : G == 298 ? 278 : G == 368 ? 350 : G == 436 ? 418 : G == 622 ? 598 : -1; }
constexpr bool s_poll(int G) { return G == 206 || G == 278 || G == 350 || G == 418 || G == 598; }
constexpr int HS0 = 22, HS1 = 35;
constexpr int s_hstore(int G) { return G >= 36 && G < 324 ? (G % 36 == HS0 ? 0 : G % 36 == HS1 ? 1 : -1) : G == 338 ? 0 : G == 346 ? 1 : -1; }
constexpr int s_pstore(int G) {
    return G == 499 ? 2 : G == 503 ? 3 : G == 515 ? 0 : G == 519 ? 1 : G == 547 ? 4 : G == 551 ? 5 : G == 579 ? 6 : G == 581 ? 7 : -1;
}
constexpr bool s_res(int G) { return G == 586 || G == 588 || G == 590; }
constexpr int s_pub(int G) { return G % 36 == 24 && G >= 132 && G <= 348 ? G / 36 - 2 : G == 380 ? 9 : 0; }
constexpr int s_pub2(int G) { return G == 564 ? 1 : G == 600 ? 2 : G == 620 ? 3 : 0; }
constexpr int m_slot(int m) { return m < 140 ? (m % 7 + 1) / 2 : m < 172 ? 1 : m < 188 ? 0 : m < 252 ? 2 + (m - 188) / 32 : 4; }
constexpr int m_ent(int m) { return m < 140 ? m / 7 : m < 172 ? 20 + (m - 140) / 2 : m < 188 ? 20 + (m - 172) : m < 252 ? 20 + ((m - 188) % 32) / 2 : (m - 252) / 2; }
constexpr int m_half(int m) { return m < 140 ? (m % 7 == 0 ? 0 : (m % 7 - 1) & 1) : (m >= 172 && m < 188) ? 0 : m & 1; }
constexpr bool m_first(int m) { return m == 0 || m == 1 || m == 3 || m == 5 || m == 252; }
constexpr bool ent_first(int m) { return m < 140 ? m % 7 == 0 : (m < 172 && (m & 1) == 0); }
constexpr bool ent_own(int e) { return e < 8 || e == 20; }
struct Sched {
    int dn[GE];
    int dgap[162];
    int hl[GE];
    int pre[GE + 1];
    int hgap[36];
    constexpr Sched() : dn(), dgap(), hl(), pre(), hgap() {
        int nd = 3 * XW, nh = 0;
        for (int n = 0; n < 162; ++n) dgap[n] = -1;
        for (int e = 0; e < 36; ++e) hgap[e] = -1;
        for (int G = 0; G < GE; ++G) {
            dn[G] = -1;
            if (nd < 162 && (G < 36 ? G % 2 == 1 : G % 4 == 1)) { dn[G] = nd; dgap[nd] = G; ++nd; }
            const int lim = G > 436 ? 54 : G > 368 ? 36 : G > 298 ? 24 : G > 226 ? 12 : 0;
            const bool slot = G < 437 ? G % 4 == 3 : G % 2 == 0;
            int h = -1;
            if (nh < lim && slot) { const int k = nh / 2; h = 2 * (k < 12 ? 8 + k : 9 + k) + (nh & 1); ++nh; }
            hl[G] = h;
            if (h >= 0) hgap[h / 2] = G;
            pre[G + 1] = pre[G] + (dn[G] >= 0) + (h >= 0) + s_poll(G) + (s_hstore(G) >= 0) + (s_pstore(G) >= 0) + s_res(G) + (s_pub(G) > 0);
        }
    }
};
constexpr Sched SCH{};
constexpr int younger(int a, int b) { return imin(SCH.pre[b] - SCH.pre[a + 1], 63); }
constexpr bool s_ring(int G) { return G % 36 == 24 && G + 12 < GE; }
constexpr bool s_bar(int G) { return s_ring(G) || s_pub(G) > 0 || s_pub2(G) > 0; }
constexpr int bar_wait(int G) {
    int n = 63;
    if (s_ring(G)) n = younger(SCH.dgap[9 * (G / 36 + 2) - 1], G);
    if (s_pub(G) > 0) n = imin(n, younger(G == 380 ? 346 : 36 * s_pub(G) + HS1, G));
    if (s_pub2(G) > 0) n = imin(n, younger(G == 564 ? 519 : G == 600 ? 551 : 581, G));
    return n;
}

#define SB() __builtin_amdgcn_sched_barrier(0)
#define STAMP(i) do {} while (0)
#define TW(code) do { code; } while (0)
#define MFMA "v_mfma_f32_32x32x16_bf16 "

extern "C" __global__ __launch_bounds__(256, 1) void k_mlp(char* __restrict__ ws, u16* __restrict__ out) {
    extern __shared__ __attribute__((aligned(16))) char smem[];
    const int tid = threadIdx.x, lane = tid & 63;
    const int w = __builtin_amdgcn_readfirstlane(tid >> 6);
    const int b = blockIdx.x, xcd = b & 7, rr = b >> 3;
    const int kq = xcd >> 1, it = 8 * (xcd & 1) + (rr >> 2), s = rr & 3, q = 4 * kq + s, par = s & 1;
    const auto rs = __builtin_amdgcn_make_buffer_rsrc(ws, 0, WS_END, 0x00020000);
    const u32x4 srd = {(u32)(uintptr_t)ws, (u32)((uintptr_t)ws >> 32) & 0xffffu, WS_END, 0x00020000u};
    const u32 voff = lane * 16;
    const u32 lbase = (u32)(uintptr_t)(LAS char*)smem;
    const u32 wl = lbase + w * 3072;
    auto set_m0 = [&](u32 v) { asm volatile("s_mov_b32 m0, %0" :: "s"(v) : "memory"); };
    auto dma = [&](auto O, u32 so) {
        const u32 vo = voff; const u32x4 sr = srd;
        asm volatile("buffer_load_dwordx4 %0, %1, %2 offen offset:%3 lds" :: "v"(vo), "s"(sr), "s"(so), "n"(O.value * 1024) : "memory");
    };
    auto dma3 = [&](u32 dst, u32 so) {
        asm volatile("s_mov_b32 m0, %3\n\ts_nop 0\n\tbuffer_load_dwordx4 %0, %1, %2 offen lds\n\t"
                     "buffer_load_dwordx4 %0, %1, %2 offen offset:1024 lds\n\tbuffer_load_dwordx4 %0, %1, %2 offen offset:2048 lds"
                     :: "v"(voff), "s"(srd), "s"(so), "s"(dst) : "memory");
    };
    const u32 xsb = WS_X1F + it * 147456;
    const u32 wfb = WS_WF + q * 663552 + w * 3072;
    auto issue_group = [&](auto I) {
        constexpr int i = I.value;
        if constexpr (i < 12) dma3(wl + i * 12288, xsb + w * 3072 + i * 12288);
        else if constexpr (i == 12) dma3(wl + L_EXF, WS_EX + q * 10240 + w * 3072);
        else if constexpr (i < 13 + XW) dma3(wl + (i - 13) * 12288, wfb + (i - 13) * 12288);
    };
    static_for<0, 4>(issue_group);
    const u32 hfb = WS_HF + (it * 4 + kq) * 36 * 8192 + w * 2048;

    u32x4 Xa[36];
    float sum = 0.f, sq = 0.f;
    {
        const u32 a0 = wl + (8 * (lane >> 5) + ((lane & 15) >> 2)) * 64 + ((lane >> 4) & 1) * 32 + (lane & 3) * 8;
        const bf16x2 ones = __builtin_bit_cast(bf16x2, 0x3f803f80u);
        u32x2 t[2][6];
        auto xread = [&](auto A) {
            constexpr int a = A.value;
            u32x2 (&d)[6] = t[a & 1];
            const u32 ad = a0 + a * 12288;
            asm volatile("s_waitcnt vmcnt(%7)\n\t"
                         "ds_read_b64_tr_b16 %0, %6\n\tds_read_b64_tr_b16 %1, %6 offset:256\n\t"
                         "ds_read_b64_tr_b16 %2, %6 offset:1024\n\tds_read_b64_tr_b16 %3, %6 offset:1280\n\t"
                         "ds_read_b64_tr_b16 %4, %6 offset:2048\n\tds_read_b64_tr_b16 %5, %6 offset:2304"
                         : "=&v"(d[0]), "=&v"(d[1]), "=&v"(d[2]), "=&v"(d[3]), "=&v"(d[4]), "=&v"(d[5])
                         : "v"(ad), "n"(3 * (imin(4 + 2 * a, 13 + XW) - a - 1)) : "memory");
        };
        xread(IC<0>{});
        STAMP(1);
        static_for<0, 12>([&](auto A) {
            constexpr int a = A.value;
            u32x2 (&d)[6] = t[a & 1];
            asm volatile("s_waitcnt lgkmcnt(0)" : "+v"(d[0]), "+v"(d[1]), "+v"(d[2]), "+v"(d[3]), "+v"(d[4]), "+v"(d[5]) :: "memory");
            issue_group(IC<4 + 2 * a>{}); issue_group(IC<5 + 2 * a>{});
            if constexpr (a < 11) xread(IC<a + 1>{});
#pragma unroll
            for (int z = 0; z < 3; ++z) {
                u32x4 v = {d[2 * z][0], d[2 * z][1], d[2 * z + 1][0], d[2 * z + 1][1]};
#pragma unroll
                for (int k = 0; k < 4; ++k) {
                    const u32 ek = v[k];
                    const bf16x2 e = __builtin_bit_cast(bf16x2, ek);
                    sum = __builtin_amdgcn_fdot2_f32_bf16(e, ones, sum, false);
                    sq = __builtin_amdgcn_fdot2_f32_bf16(e, e, sq, false);
                }
                Xa[3 * a + z] = v;
                asm volatile("" : "+a"(Xa[3 * a + z]));
            }
            asm volatile("" : "+v"(sum), "+v"(sq));
            SB();
        });
    }
    u32x4 Wf[18], Ef;
    const u32 rd0 = lbase + voff;
    const u32 rb[3] = {rd0, rd0 + 49152, rd0 + 98304};
    auto rdw = [&](auto I, u32 base, auto O) {
        u32x4& d = Wf[I.value];
        asm volatile("ds_read_b128 %0, %1 offset:%2" : "=v"(d) : "v"(base), "n"(O.value) : "memory");
    };
    auto rde = [&](auto C) {
        u32x4& d = Ef; const u32 base = rb[2];
        asm volatile("ds_read_b128 %0, %1 offset:%2" : "=v"(d) : "v"(base), "n"(L_EXF - 98304 + C.value * 1024) : "memory");
    };
    float ysc;
    u32x4 Xe;
    {
        sum += __builtin_bit_cast(float, __builtin_amdgcn_ds_bpermute((lane ^ 32) * 4, __builtin_bit_cast(int, sum)));
        sq += __builtin_bit_cast(float, __builtin_amdgcn_ds_bpermute((lane ^ 32) * 4, __builtin_bit_cast(int, sq)));
        asm volatile("" : "+v"(sum), "+v"(sq));
        SB();
        asm volatile("s_waitcnt vmcnt(%0)\n\ts_barrier" :: "n"(3 * (XW - 3)) : "memory");
        STAMP(2);
        static_for<0, 12>([&](auto I) { rdw(I, rd0, IC<I.value * 1024>{}); });
        rde(IC<0>{});
        SB();
        const float mean = sum * (1.f / DM);
        float var = fmaf(-mean, mean, sq * (1.f / DM));
        var = rbf(fmaxf(var, 0.f));
        const float sd = rbf(__builtin_amdgcn_sqrtf(rbf(var + 1e-5f)));
        ysc = -1.44269504088896340736f * __builtin_amdgcn_rcpf(sd);
        const bool lo = lane < 32;
        Xe = (u32x4){lo ? pkbf(mean, mean) : 0u, lo ? pkbf(mean, sd) : 0u, lo ? pkbf(sd, sd) : 0u, 0u};
        asm volatile("" : "+v"(Xe), "+v"(ysc));
        SB();
    }

    f32x16 acc[2], acc2[5];
    float yv[16], ev[16];
    u32x4 H[36][2], pollv;
    u32* const flagp = (u32*)(ws + WS_FLAG + (it * 4 + kq) * 64);
    auto publish = [&](u32 level) {
        if (tid == 0) __hip_atomic_store(flagp + s, level, __ATOMIC_RELAXED, __HIP_MEMORY_SCOPE_AGENT);
    };
    auto act = [&](auto C, auto HS) {
        constexpr int t = HS.value / 2, e = C.value < 8 ? C.value : 20;
        if constexpr (HS.value % 2 == 0) {
            if constexpr (t < 16) { yv[t] = acc[C.value & 1][t] * ysc; asm volatile("" : "+v"(yv[t])); }
            if constexpr (t >= 2) { yv[t - 2] = yv[t - 2] * ev[t - 2]; asm volatile("" : "+v"(yv[t - 2])); }
            if constexpr (t < 16) { ev[t] = __builtin_amdgcn_exp2f(yv[t]); asm volatile("" : "+v"(ev[t])); }
        } else {
            if constexpr (t >= 1 && t < 17) { ev[t - 1] = 1.f + ev[t - 1]; asm volatile("" : "+v"(ev[t - 1])); }
            if constexpr (t >= 3 && (t & 1)) {
                u32 pk = pkbf(yv[t - 3], yv[t - 2]);
                asm volatile("" : "+v"(pk));
                H[e][(t - 3) / 8][((t - 3) / 2) & 3] = pk;
            }
            if constexpr (t >= 1 && t < 17) { ev[t - 1] = __builtin_amdgcn_rcpf(ev[t - 1]); asm volatile("" : "+v"(ev[t - 1])); }
        }
    };
    const u32 vh0 = voff + par * 1024, vh1 = voff + (par ^ 1) * 1024;
    const u32 hown = hfb + 9 * s * 8192;
    const u32 vpf = (lane & 15) * 128 + (lane >> 4 & 1) * 8192;
    u32 pfd = 0;
    auto prefetch = [&](u32 vo, u32 so) {
        asm volatile("buffer_load_dword %0, %1, %2, %3 offen sc1" : "+v"(pfd) : "v"(vo), "s"(srd), "s"(so) : "memory");
    };
    const u32 hpeer[3] = {hfb + 9 * ((s + 1) & 3) * 8192, hfb + 9 * ((s + 2) & 3) * 8192, hfb + 9 * ((s + 3) & 3) * 8192};
    auto hload = [&](auto E, auto HF) {
        constexpr int e = E.value, hf = HF.value, k = e < 20 ? e - 8 : e - 9;
        const u32 so = hpeer[k % 3] + (k / 3) * 8192, vo = hf ? vh1 : vh0; const u32x4 sr = srd;
        u32x4& d = H[e][hf];
        asm volatile("buffer_load_dwordx4 %0, %1, %2, %3 offen sc1" : "=a"(d) : "v"(vo), "s"(sr), "s"(so) : "memory");
    };
    const u32 vzero = 0;
    const u32 fso1 = WS_FLAG + (it * 4 + kq) * 64;
    auto poll = [&]() {
        asm volatile("buffer_load_dwordx4 %0, %1, %2, %3 offen sc1" : "=v"(pollv) : "v"(vzero), "s"(srd), "s"(fso1) : "memory");
    };
    auto check = [&](u32 level) {
        asm volatile("" : "+v"(pollv));
#pragma nounroll
        for (u32 spins = 0; spins < (1u << 20); ++spins) {
            const bool bad = pollv[0] < level || pollv[1] < level || pollv[2] < level || pollv[3] < level;
            if (__builtin_amdgcn_ballot_w64(bad) == 0ull) break;
            __builtin_amdgcn_s_sleep(1);
            asm volatile("buffer_load_dwordx4 %0, %1, %2, %3 offen sc1\n\ts_waitcnt vmcnt(0)"
                         : "=v"(pollv) : "v"(vzero), "s"(srd), "s"(fso1) : "memory");
        }
    };
    const u32 pgb = WS_PART + (it * 4 + s) * 98304 + w * 2048;
    const u32 phb = WS_PH + (it * 2 + (s >> 1)) * 65536 + w * 16384;
    const u32 fso2 = WS_FLAG + 4096 + (it * 2 + (s >> 1)) * 64;
    const int Town = 9 * (s >> 1) + 5 * par + kq, Thalf = 9 * (s >> 1) + 4;
    auto xfrag = [&](int ks) -> u32 { return xsb + ((ks / 3) * 12 + 3 * w + ks % 3) * 1024; };
    u32 fv, ok1 = 0;
    const u32 vfl = (lane & 7) * 4;
    u32x4 P[6];
    u32 Q[8];
    const u32 vq = voff + ((kq & 1) * 2 + par) * 4;
    const u32 pin = pgb + kq * 24576, qin = phb + (kq >> 1) * 8192;
    auto publish2 = [&](u32 level) {
        if (tid == 0) __hip_atomic_store((u32*)(ws + fso2) + 2 * kq + par, level, __ATOMIC_RELAXED, __HIP_MEMORY_SCOPE_AGENT);
    };
    auto poll2 = [&]() {
        asm volatile("buffer_load_dword %0, %1, %2, %3 offen sc1" : "=v"(fv) : "v"(vfl), "s"(srd), "s"(fso2) : "memory");
    };
    auto test2 = [&](u32 level) -> u32 {
        asm volatile("" : "+v"(fv));
        return __builtin_amdgcn_readfirstlane(__builtin_amdgcn_ballot_w64(fv < level) == 0ull ? 1u : 0u);
    };
#define PLOAD2(C, r, cond, skip) \
    asm volatile("s_cmp_eq_u32 %5, " #skip "\n\ts_cbranch_scc1 1f\n\t" \
                 "buffer_load_dwordx4 %0, %2, %3, %4 offen sc1\n\tbuffer_load_dwordx4 %1, %2, %3, %4 offen offset:1024 sc1\n1:" \
                 : C(P[2 * r]), C(P[2 * r + 1]) : "v"(voff), "s"(srd), "s"(pin + r * 8192), "s"(cond) : "memory", "scc")
#define QLOAD4(C, r, cond, skip) \
    asm volatile("s_cmp_eq_u32 %7, " #skip "\n\ts_cbranch_scc1 1f\n\t" \
                 "buffer_load_dword %0, %4, %5, %6 offen sc1\n\tbuffer_load_dword %1, %4, %5, %6 offen offset:1024 sc1\n\t" \
                 "buffer_load_dword %2, %4, %5, %6 offen offset:2048 sc1\n\tbuffer_load_dword %3, %4, %5, %6 offen offset:3072 sc1\n1:" \
                 : C(Q[4 * r]), C(Q[4 * r + 1]), C(Q[4 * r + 2]), C(Q[4 * r + 3]) \
                 : "v"(vq), "s"(srd), "s"(qin + r * 4096), "s"(cond) : "memory", "scc")
    auto early = [&](int i) {
        if (i == 0) PLOAD2("=&v", 0, ok1, 0);
        if (i == 1) QLOAD4("=&v", 0, ok1, 0);
        if (i == 2) QLOAD4("=&v", 1, ok1, 0);
    };

    static_for<0, GE>([&](auto GG) {
        constexpr int G = GG.value, c = G / 36, g = G % 36, m = G - 324;
        if constexpr (g == 0 && G > 0) STAMP(2 + G / 36);
        if constexpr (s_bar(G)) {
            if constexpr (G % 6 == 0) TW(asm volatile("s_waitcnt vmcnt(%0) lgkmcnt(6)\n\ts_barrier" :: "n"(bar_wait(G)) : "memory"));
            else TW(asm volatile("s_waitcnt vmcnt(%0)\n\ts_barrier" :: "n"(bar_wait(G)) : "memory"));
        } else if constexpr (G % 6 == 0) asm volatile("s_waitcnt lgkmcnt(%0)" :: "n"(G == 0 ? 7 : G + 6 < GE ? 6 : 0) : "memory");
        u32x4& wf = Wf[G % 18];
        if constexpr (G < 324) {
            f32x16& ac = acc[c & 1]; u32x4& xa = Xa[g];
            if constexpr (g == 0) asm volatile(MFMA "%0, %1, %2, 0" : "=&v"(ac) : "v"(wf), "a"(xa));
            else asm volatile(MFMA "%0, %1, %2, %0" : "+v"(ac) : "v"(wf), "a"(xa));
            if constexpr (g == 12) {
                u32x4& ef = Ef; u32x4& xe = Xe;
                SB();
                if constexpr (c > 0) act(IC<c - 1>{}, IC<11>{});
                SB();
                asm volatile(MFMA "%0, %1, %2, %0" : "+v"(ac) : "v"(ef), "v"(xe));
            }
        } else {
            constexpr int sl = m_slot(m), e = m_ent(m), hf = m_half(m);
            if constexpr (ent_first(m) && !ent_own(e))
                TW(asm volatile("s_waitcnt vmcnt(%0)" :: "n"(younger(SCH.hgap[e], G)) : "memory"));
            f32x16& ac = acc2[sl]; u32x4& hh = H[e][hf];
            if constexpr (m_first(m)) asm volatile(MFMA "%0, %1, %2, 0" : "=&v"(ac) : "v"(wf), "v"(hh));
            else if constexpr (ent_own(e)) asm volatile(MFMA "%0, %1, %2, %0" : "+v"(ac) : "v"(wf), "v"(hh));
            else asm volatile(MFMA "%0, %1, %2, %0" : "+v"(ac) : "v"(wf), "a"(hh));
        }
        SB();
        if constexpr (G + 12 < GE) {
            constexpr int lo = ((G + 12) % 144) * 1024;
            rdw(IC<(G + 12) % 18>{}, rb[lo / 49152], IC<lo % 49152>{});
        }
        if constexpr (g == 14 && c < 8) rde(IC<c + 1>{});
        if constexpr (G + 1 < GE) {
            if constexpr (SCH.dn[G + 1] >= 0 && SCH.dn[G + 1] % 3 == 0) set_m0(wl + ((SCH.dn[G + 1] / 3) % 12) * 12288);
        }
        if constexpr (SCH.dn[G] >= 0) dma(IC<SCH.dn[G] % 3>{}, wfb + (SCH.dn[G] / 3) * 12288);
        if constexpr (G >= 36 && G < 324 && g >= 1) act(IC<c - 1>{}, IC<(g < 12 ? g - 1 : g)>{});
        if constexpr (G >= 326 && G < 344) { act(IC<8>{}, IC<2 * (G - 326)>{}); act(IC<8>{}, IC<2 * (G - 326) + 1>{}); }
        if constexpr (s_hstore(G) >= 0) {
            constexpr int hf = s_hstore(G), cc = c - 1;
            __builtin_amdgcn_raw_buffer_store_b128(H[cc < 8 ? cc : 20][hf], rs, hf ? vh1 : vh0, hown + cc * 8192, 16);
        }
        if constexpr (s_pub(G) > 0) {
            if constexpr (G == 380) prefetch(vpf, hown + 7 * 8192); else prefetch(vpf & 2047u, hown + (s_pub(G) - 1) * 8192);
            publish(s_pub(G));
        }
        if constexpr (s_pub2(G) > 0) publish2(s_pub2(G));
        if constexpr (poll_of(G) >= 0) {
            TW(asm volatile("s_waitcnt vmcnt(%0)" :: "n"(younger(poll_of(G), G)) : "memory"));
            if constexpr (G < 500) { constexpr int G_ = G; { constexpr int G = G_ + 1; TW(check(G_ == 226 ? 2 : G_ == 298 ? 4 : G_ == 368 ? 6 : 9)); } }
            else ok1 = test2(1);
        }
        if constexpr (s_poll(G)) { if constexpr (G < 500) poll(); else poll2(); }
        if constexpr (SCH.hl[G] >= 0) hload(IC<SCH.hl[G] / 2>{}, IC<SCH.hl[G] % 2>{});
        if constexpr (G == 623) early(0);
        if constexpr (G == 626) early(1);
        if constexpr (G == 630) early(2);
        if constexpr (s_pstore(G) >= 0) {
            constexpr int sg = s_pstore(G) / 2, pt = s_pstore(G) % 2;
            const f32x16& a = acc2[sg];
            const u32x4 v = {pkbf(a[8 * pt], a[8 * pt + 1]), pkbf(a[8 * pt + 2], a[8 * pt + 3]), pkbf(a[8 * pt + 4], a[8 * pt + 5]), pkbf(a[8 * pt + 6], a[8 * pt + 7])};
            if constexpr (sg == 0) __builtin_amdgcn_raw_buffer_store_b128(v, rs, voff, phb + pt * 8192 + (2 * kq + par) * 1024, 16);
            else __builtin_amdgcn_raw_buffer_store_b128(v, rs, voff + pt * 1024, pgb + ((kq + sg) & 3) * 24576 + (sg - 1) * 8192, 16);
        }
        if constexpr (s_res(G)) {
            constexpr int i = (G - 586) / 2;
            set_m0(wl + 73728 + i * 1024);
            const u32 so = xfrag(i < 2 ? 2 * Town + i : 2 * Thalf + (kq >> 1));
            asm volatile("s_nop 0" ::: "memory");
            dma(IC<0>{}, so);
        }
        SB();
    });
    STAMP(20);

    {
        u32x2 xr[5];
        f32x4 gb[4];
        f32x2 gh;
        const u32 ra = wl + 73728 + (4 * (lane >> 5) + ((lane & 15) >> 2)) * 64 + ((lane >> 4) & 1) * 32 + (lane & 3) * 8;
        const u32 ga = lbase + L_EXF + 9216 + (lane >> 5) * 16;
        poll2();
        asm volatile("ds_read_b64_tr_b16 %0, %5\n\tds_read_b64_tr_b16 %1, %5 offset:512\n\tds_read_b64_tr_b16 %2, %5 offset:1024\n\t"
                     "ds_read_b64_tr_b16 %3, %5 offset:1536\n\tds_read_b64_tr_b16 %4, %6 offset:2048"
                     : "=&v"(xr[0]), "=&v"(xr[1]), "=&v"(xr[2]), "=&v"(xr[3]), "=&v"(xr[4]) : "v"(ra), "v"(ra + (kq & 1) * 512) : "memory");
        asm volatile("ds_read_b128 %0, %5\n\tds_read_b128 %1, %5 offset:32\n\tds_read_b128 %2, %5 offset:64\n\tds_read_b128 %3, %5 offset:96\n\t"
                     "ds_read_b64 %4, %6 offset:128\n\ts_waitcnt vmcnt(0) lgkmcnt(0)"
                     : "=&v"(gb[0]), "=&v"(gb[1]), "=&v"(gb[2]), "=&v"(gb[3]), "=&v"(gh)
                     : "v"(ga), "v"(ga + kq * 32 + par * 8) : "memory");
        asm volatile("" : "+v"(xr[0]), "+v"(xr[1]), "+v"(xr[2]), "+v"(xr[3]), "+v"(xr[4]), "+v"(fv));
        asm volatile("" :: "v"(pfd));
#pragma nounroll
        for (u32 spins = 0; spins < (1u << 20); ++spins) {
            if (__builtin_amdgcn_ballot_w64(fv < 3u) == 0ull) break;
            __builtin_amdgcn_s_sleep(1);
            asm volatile("buffer_load_dword %0, %1, %2, %3 offen sc1\n\ts_waitcnt vmcnt(0)" : "=v"(fv) : "v"(vfl), "s"(srd), "s"(fso2) : "memory");
        }
        STAMP(21);
        PLOAD2("=&v", 2, ok1, 2);
        PLOAD2("=&v", 1, ok1, 2);
        PLOAD2("+v", 0, ok1, 1);
        QLOAD4("+v", 0, ok1, 1);
        QLOAD4("+v", 1, ok1, 1);
        asm volatile("s_waitcnt vmcnt(0)" : "+v"(P[0]), "+v"(P[1]), "+v"(P[2]), "+v"(P[3]), "+v"(P[4]), "+v"(P[5]) :: "memory");
        asm volatile("" : "+v"(Q[0]), "+v"(Q[1]), "+v"(Q[2]), "+v"(Q[3]), "+v"(Q[4]), "+v"(Q[5]), "+v"(Q[6]), "+v"(Q[7]));
        STAMP(22);
        u16* orow = out + (size_t)(it * 128 + w * 32 + (lane & 31)) * DM + 4 * (lane >> 5);
        auto fin2 = [&](float a0, float a1, float g0, float g1, u32 xk) -> u32 {
            const u32 t = pkbf(a0 * -0.69314718055994530942f, a1 * -0.69314718055994530942f);
            const u32 gz = pkbf(bflo(t) * g0, bfhi(t) * g1);
            return pkbf(bflo(xk) + bflo(gz), bfhi(xk) + bfhi(gz));
        };
#pragma unroll
        for (int k = 0; k < 4; ++k) {
            u32x2 o;
#pragma unroll
            for (int j = 0; j < 2; ++j) {
                const int d = 2 * k + j;
                const u32 p0 = P[d >> 2][d & 3], p1 = P[2 + (d >> 2)][d & 3], p2 = P[4 + (d >> 2)][d & 3], xk = xr[k][j];
                o[j] = fin2(acc2[4][2 * d] + bflo(p0) + bflo(p1) + bflo(p2), acc2[4][2 * d + 1] + bfhi(p0) + bfhi(p1) + bfhi(p2),
                            gb[k][2 * j], gb[k][2 * j + 1], xk);
            }
            *(u32x2*)(orow + Town * 32 + 8 * k) = o;
        }
        float a0 = 0.f, a1 = 0.f;
#pragma unroll
        for (int r = 0; r < 8; ++r) { a0 += bflo(Q[r]); a1 += bfhi(Q[r]); }
        *(u32*)(orow + Thalf * 32 + 8 * kq + 2 * par) = fin2(a0, a1, gh[0], gh[1], par ? xr[4][1] : xr[4][0]);
    }
    asm volatile("k_mlp_end:");
}
#if defined(__HIP_DEVICE_COMPILE__)
#pragma clang attribute pop
#endif

extern "C" void kernel_launch(void* const* d_in, const int* in_sizes, int n_in, void* d_out, int out_size,
                              void* d_ws, size_t ws_size, hipStream_t stream) {
    const u16* x = (const u16*)d_in[0];
    char* ws = (char*)d_ws;
    (void)hipFuncSetAttribute((const void*)k_mlp, hipFuncAttributeMaxDynamicSharedMemorySize, MLP_LDS);
    k_prep<<<NB_PREP, 256, PREP_LDS, stream>>>(x, (const u16*)d_in[1], (const u16*)d_in[2], (const u16*)d_in[3],
                                               (const u16*)d_in[4], (const u16*)d_in[5], (const u16*)d_in[9],
                                               (const u16*)d_in[10], (const u16*)d_in[11], ws);
    k_conv<<<NB_PF + NB_CONV + NB_W1, 256, CONV_LDS, stream>>>((const u16*)d_in[6], (const u16*)d_in[8], ws);
    k_mlp<<<256, 256, MLP_LDS, stream>>>(ws, (u16*)d_out);
}
```

```cpp
#include <hip/hip_runtime.h>
#include <stdint.h>

typedef unsigned short u16;
typedef unsigned int u32;
typedef float f32x2 __attribute__((ext_vector_type(2)));
typedef float f32x4 __attribute__((ext_vector_type(4)));
typedef float f32x16 __attribute__((ext_vector_type(16)));
typedef _Float16 f16x2 __attribute__((ext_vector_type(2)));
typedef _Float16 f16x8 __attribute__((ext_vector_type(8)));
typedef __bf16 bf16x2 __attribute__((ext_vector_type(2)));
typedef __bf16 bf16x8 __attribute__((ext_vector_type(8)));
typedef short s16x4 __attribute__((ext_vector_type(4)));
typedef u32 u32x2 __attribute__((ext_vector_type(2)));
typedef u32 u32x4 __attribute__((ext_vector_type(4)));
#define LAS __attribute__((address_space(3)))
#define DI __device__ __forceinline__
#define RTSTAMP(k)
#define MARK(i)

constexpr int SEQ = 2048, DM = 576, NS = 128, FF = 4608;

constexpr u32 WS_MOD   = 0;
constexpr u32 WS_STAT  = 16384;
constexpr u32 WS_KSC   = 32768;
constexpr u32 WS_FLAG  = 36864;
constexpr u32 WS_EX    = 65536;
constexpr u32 WS_XT    = 1u << 20;
constexpr u32 WS_KR    = WS_XT + 2359296;
constexpr u32 WS_X1F   = WS_KR + 2359296;
constexpr u32 WS_WF    = WS_X1F + 2359296;
constexpr u32 WS_HF    = WS_WF + 10616832;
constexpr u32 WS_PART  = WS_HF + 18874368;
constexpr u32 WS_PH    = WS_PART + 6291456;
constexpr u32 WS_END   = WS_PH + 2097152;

DI float bflo(u32 u) { return __builtin_bit_cast(float, u << 16); }
DI float bfhi(u32 u) { return __builtin_bit_cast(float, u & 0xffff0000u); }
DI float bf2f(u16 v) { return __builtin_bit_cast(float, ((u32)v) << 16); }
DI u32 pkbf(float a, float b) { f32x2 v = {a, b}; return __builtin_bit_cast(u32, __builtin_convertvector(v, bf16x2)); }
DI u32 pkhf(float a, float b) { f32x2 v = {a, b}; return __builtin_bit_cast(u32, __builtin_convertvector(v, f16x2)); }
DI float rbf(float a) { return bflo(pkbf(a, a)); }
template <int CTRL, int RM = 0xf> DI float dppf(float v) {
    return __builtin_bit_cast(float, __builtin_amdgcn_update_dpp(0, __builtin_bit_cast(int, v), CTRL, RM, 0xf, true));
}
DI u32x4 trread2(const char* smem, u32 off0, u32 off1) {
    const u32x2 l = __builtin_bit_cast(u32x2, __builtin_amdgcn_ds_read_tr16_b64_v4i16((LAS s16x4*)(smem + off0)));
    const u32x2 h = __builtin_bit_cast(u32x2, __builtin_amdgcn_ds_read_tr16_b64_v4i16((LAS s16x4*)(smem + off1)));
    return (u32x4){l[0], l[1], h[0], h[1]};
}
DI void st16(char* ws, u32 off, u32x4 v) {
    __builtin_amdgcn_raw_buffer_store_b128(v, __builtin_amdgcn_make_buffer_rsrc(ws, 0, WS_END, 0x00020000), off, 0, 16);
}
DI void st4(char* ws, u32 off, u32 v) {
    __builtin_amdgcn_raw_buffer_store_b32(v, __builtin_amdgcn_make_buffer_rsrc(ws, 0, WS_END, 0x00020000), off, 0, 16);
}
DI float pow2i(int e) { e = e < -126 ? -126 : (e > 127 ? 127 : e); return __builtin_bit_cast(float, (u32)(e + 127) << 23); }
DI int expo(float x) { return (int)((__builtin_bit_cast(u32, x) >> 23) & 255u) - 127; }
DI u32 imgrow(u32 row) { return row * 1184u + (row >> 3) * 128u; }
DI f32x2 cmul(f32x2 z, f32x2 w, f32x2 wp) {
    const f32x2 zx = {z[0], z[0]}, zy = {z[1], z[1]};
    return __builtin_elementwise_fma(zy, wp, zx * w);
}
#define CODE_PREFETCH(sym) do { u32 lo_, hi_, len_; \
    asm volatile("s_getpc_b64 s[20:21]\n\ts_add_u32 s20, s20, " #sym "@rel32@lo+4\n\ts_addc_u32 s21, s21, " #sym "@rel32@hi+12\n\t" \
                 "s_mov_b32 %0, s20\n\ts_mov_b32 %1, s21\n\ts_mov_b32 %2, " #sym "_end-" #sym : "=s"(lo_), "=s"(hi_), "=s"(len_) :: "scc", "s20", "s21"); \
    const char* p_ = (const char*)(((unsigned long long)hi_ << 32) | lo_); \
    if (threadIdx.x * 128u < len_) { u32 t_; asm volatile("global_load_dword %0, %1, off\n\ts_waitcnt vmcnt(0)" : "=v"(t_) : "v"(p_ + threadIdx.x * 128u) : "memory"); } \
    } while (0)

#define DELAY1 20
#define DELAY2 0
#define XROT 0
#define NAV 26
constexpr int NA = NAV, NB = 36 - NA;
constexpr int NB_PF = 16, NB_LN = 64, NB_GEMV = 108, NB_KGEN = 576, NB_W2 = 144;
constexpr int NB_PREP = NB_PF + NB_LN + NB_GEMV + NB_KGEN + NB_W2;
constexpr int PREP_LDS = 38400;

extern "C" __global__ __launch_bounds__(256, 4) void k_prep(const u16* __restrict__ x, const u16* __restrict__ logA,
                                              const u16* __restrict__ Aim, const u16* __restrict__ Cre,
                                              const u16* __restrict__ Cim, const u16* __restrict__ logdt,
                                              const u16* __restrict__ w2, const u16* __restrict__ cvec,
                                              const u16* __restrict__ wmod, char* __restrict__ ws) {
    extern __shared__ __attribute__((aligned(16))) char smem[];
    RTSTAMP(0);
    const int tid = threadIdx.x, lane = tid & 63;
    const int wv = __builtin_amdgcn_readfirstlane(tid >> 6);
    int b = blockIdx.x;
    if (b >= NB_PF && b < NB_PF + NB_KGEN + NB_LN + NB_GEMV) b = b < NB_PF + NB_KGEN ? b + NB_LN + NB_GEMV : b - NB_KGEN;
    if (b < NB_PF) {
        if (b < 8) CODE_PREFETCH(k_prep); else CODE_PREFETCH(k_conv);
        if (b < 2) st16(ws, WS_FLAG + b * 4096 + tid * 16, (u32x4){0u, 0u, 0u, 0u});
    } else if ((b -= NB_PF) < NB_LN) {
        const int t0 = b * 32, tok = tid >> 3, sub = tid & 7;
        u32x4 v[9];
#pragma unroll
        for (int i = 0; i < 9; ++i) v[i] = *(const u32x4*)(x + (size_t)(t0 + tok) * DM + (sub + 8 * i) * 8);
        MARK(0);
#pragma unroll
        for (int i = 0; i < 9; ++i) *(u32x4*)(smem + imgrow(tok) + (sub + 8 * i) * 16) = v[i];
        __syncthreads();
        const int g = lane >> 4, qq = (lane & 15) >> 2, p = lane & 3;
#pragma unroll
        for (int i = 0; i < 9; ++i) {
            const int cb = wv * 9 + i;
            st16(ws, WS_XT + b * 36864 + cb * 1024 + (lane & 15) * 64 + g * 16,
                 trread2(smem, imgrow(8 * g + qq) + cb * 32 + p * 8, imgrow(8 * g + 4 + qq) + cb * 32 + p * 8));
        }
        MARK(1);
        float s = 0.f;
#pragma unroll
        for (int i = 0; i < 9; ++i)
#pragma unroll
            for (int k = 0; k < 4; ++k) s += bflo(v[i][k]) + bfhi(v[i][k]);
        s += dppf<0xB1>(s); s += dppf<0x4E>(s); s += dppf<0x141>(s);
        const float mean = s * (1.f / DM);
        float q = 0.f;
#pragma unroll
        for (int i = 0; i < 9; ++i)
#pragma unroll
            for (int k = 0; k < 4; ++k) {
                const float a = bflo(v[i][k]) - mean, c = bfhi(v[i][k]) - mean;
                q = fmaf(a, a, q); q = fmaf(c, c, q);
            }
        q += dppf<0xB1>(q); q += dppf<0x4E>(q); q += dppf<0x141>(q);
        if (sub == 0) st4(ws, WS_STAT + (t0 + tok) * 4, pkbf(mean, __builtin_amdgcn_sqrtf(rbf(rbf(q * (1.f / DM)) + 1e-5f))));
    } else if ((b -= NB_LN) < NB_GEMV) {
        const int seg = tid & 3, rg = tid >> 2;
        u32x4 v[9]; u16 cv[9];
#pragma unroll
        for (int i = 0; i < 9; ++i) {
            const int row = rg + 64 * i;
            cv[i] = cvec[row];
            v[i] = *(const u32x4*)(wmod + (size_t)row * (6 * DM) + b * 32 + seg * 8);
        }
        MARK(0);
        float acc[8];
#pragma unroll
        for (int k = 0; k < 8; ++k) acc[k] = 0.f;
#pragma unroll
        for (int i = 0; i < 9; ++i) {
            const float c = bf2f(cv[i]);
            const float sl = rbf(c * __builtin_amdgcn_rcpf(1.f + __builtin_amdgcn_exp2f(c * -1.44269504088896340736f)));
#pragma unroll
            for (int k = 0; k < 4; ++k) {
                acc[2 * k] = fmaf(sl, bflo(v[i][k]), acc[2 * k]);
                acc[2 * k + 1] = fmaf(sl, bfhi(v[i][k]), acc[2 * k + 1]);
            }
        }
#pragma unroll
        for (int k = 0; k < 8; ++k) { acc[k] += dppf<0x114>(acc[k]); acc[k] += dppf<0x118>(acc[k]); }
        float* red = (float*)smem;
        if ((lane & 15) >= 12) {
            *(f32x4*)(red + (tid >> 4) * 32 + seg * 8) = (f32x4){acc[0], acc[1], acc[2], acc[3]};
            *(f32x4*)(red + (tid >> 4) * 32 + seg * 8 + 4) = (f32x4){acc[4], acc[5], acc[6], acc[7]};
        }
        __syncthreads();
        MARK(1);
        if (tid < 32) {
            float t = 0.f;
#pragma unroll
            for (int i = 0; i < 16; ++i) t += red[i * 32 + tid];
            st4(ws, WS_MOD + (b * 32 + tid) * 4, __builtin_bit_cast(u32, rbf(t)));
        }
    } else if ((b -= NB_GEMV) < NB_KGEN) {
        const int d = b, hn = wv & 1, grp = wv >> 1;
        const int n = hn * 64 + lane, n2 = n ^ 64;
        const u16 la_ = logA[d * NS + n], ai_ = Aim[d * NS + n], cr_ = Cre[d * NS + n], ci_ = Cim[d * NS + n];
        const u16 cr2_ = Cre[d * NS + n2], ci2_ = Cim[d * NS + n2], ld_ = logdt[d];
        u32 ta[8];
#pragma unroll
        for (int j = 0; j < 8; j += 2) {
            const float l0 = (float)(2047 - tid * 8 - j);
            const u32 pk = pkbf(l0, l0 - 1.f);
            const float p0 = bflo(pk), p1 = bfhi(pk);
            ta[j] = (u32)fminf(4.f * p0, p0 + 1536.f); ta[j + 1] = (u32)fminf(4.f * p1, p1 + 1536.f);
        }
        asm volatile("" : "+v"(ta[0]), "+v"(ta[1]), "+v"(ta[2]), "+v"(ta[3]), "+v"(ta[4]), "+v"(ta[5]), "+v"(ta[6]), "+v"(ta[7]));
        MARK(0);
        const float cr = bf2f(cr_), ci = bf2f(ci_);
        const float dt = rbf(__builtin_amdgcn_exp2f(bf2f(ld_) * 1.44269504088896340736f));
        const float zr = -rbf(__builtin_amdgcn_exp2f(bf2f(la_) * 1.44269504088896340736f)) * dt, zi = bf2f(ai_) * dt;
        float bs = fabsf(cr) + fabsf(ci) + fabsf(bf2f(cr2_)) + fabsf(bf2f(ci2_));
        bs += dppf<0xB1>(bs); bs += dppf<0x4E>(bs); bs += dppf<0x141>(bs); bs += dppf<0x140>(bs);
        bs += dppf<0x142, 0xa>(bs); bs += dppf<0x143, 0xc>(bs);
        bs = __builtin_bit_cast(float, __builtin_amdgcn_readlane(__builtin_bit_cast(int, bs), 63));
        const int se = 14 - expo(2.f * dt * bs * 1.0001f);
        const float scale = pow2i(se > 100 ? 100 : se);
        const float zr2 = zr * 1.44269504088896340736f;
        auto cexp = [&](float p) -> f32x2 {
            const float mag = __builtin_amdgcn_exp2f(zr2 * p);
            const float ang = zi * p;
            const float rr = __builtin_rintf(ang * 0.15915494309189535f);
            float fr = fmaf(ang, 0.15915494309189535f, -rr);
            fr = fmaf(ang, 6.4206382e-9f, fr);
            return (f32x2){mag * __builtin_amdgcn_cosf(fr), mag * __builtin_amdgcn_sinf(fr)};
        };
        const float p1 = grp ? 4.f : 1.f;
        const f32x2 f1 = cexp(p1), c1 = cexp(16.f * p1), s0 = cexp(grp ? 512.f : 0.f), s1 = cexp(256.f * p1);
        const f32x2 fw = {f1[0], -f1[1]}, fwp = {f1[1], f1[0]};
        const f32x2 cwp = {-c1[1], c1[0]};
        const float es = 2.f * dt * scale;
        f32x2 za = cmul((f32x2){es * cr, es * ci}, s0, (f32x2){-s0[1], s0[0]});
        f32x2 zb = cmul(za, s1, (f32x2){-s1[1], s1[0]});
        f32x2 zf = {1.f, 0.f};
        char* tab = smem + wv * 8704;
        u32 keep[16];
#pragma unroll
        for (int k = 0; k < 16; ++k) {
            *(u32*)(tab + k * 272 + lane * 4) = pkhf(zf[0], zf[1]);
            *(u32*)(tab + (16 + k) * 272 + lane * 4) = pkhf(za[0], za[1]);
            keep[k] = pkhf(zb[0], zb[1]);
            zf = cmul(zf, fw, fwp); za = cmul(za, c1, cwp); zb = cmul(zb, c1, cwp);
        }
        const int r16 = lane & 15, g = lane >> 4;
        f16x8 fb[4];
        f32x4 acc0 = {0.f, 0.f, 0.f, 0.f}, acc1 = {0.f, 0.f, 0.f, 0.f};
        __builtin_amdgcn_fence(__ATOMIC_RELEASE, "wavefront");
        __builtin_amdgcn_wave_barrier();
#pragma unroll
        for (int ks = 0; ks < 4; ++ks) {
            fb[ks] = *(const f16x8*)(tab + r16 * 272 + ks * 64 + g * 16);
            const f16x8 fa = *(const f16x8*)(tab + (16 + r16) * 272 + ks * 64 + g * 16);
            acc0 = __builtin_amdgcn_mfma_f32_16x16x32_f16(fb[ks], fa, acc0, 0, 0, 0);
        }
        __builtin_amdgcn_fence(__ATOMIC_RELEASE, "wavefront");
        __builtin_amdgcn_wave_barrier();
#pragma unroll
        for (int k = 0; k < 16; ++k) *(u32*)(tab + (16 + k) * 272 + lane * 4) = keep[k];
        __builtin_amdgcn_fence(__ATOMIC_RELEASE, "wavefront");
        __builtin_amdgcn_wave_barrier();
#pragma unroll
        for (int ks = 0; ks < 4; ++ks) {
            const f16x8 fa = *(const f16x8*)(tab + (16 + r16) * 272 + ks * 64 + g * 16);
            acc1 = __builtin_amdgcn_mfma_f32_16x16x32_f16(fb[ks], fa, acc1, 0, 0, 0);
        }
        MARK(1);
        __builtin_amdgcn_fence(__ATOMIC_RELEASE, "wavefront");
        __builtin_amdgcn_wave_barrier();
        *(f32x4*)(tab + r16 * 64 + g * 16) = acc0;
        *(f32x4*)(tab + 1024 + r16 * 64 + g * 16) = acc1;
        __syncthreads();
        float kv[8];
#pragma unroll
        for (int j = 0; j < 8; ++j) {
            const u32 o = ta[j] + (ta[j] >= 2048u ? 2u * 8704u - 2048u : 0u);
            kv[j] = *(const float*)(smem + o) + *(const float*)(smem + o + 8704);
        }
        MARK(2);
        st16(ws, WS_KR + d * 4096 + tid * 16, (u32x4){pkhf(kv[0], kv[1]), pkhf(kv[2], kv[3]), pkhf(kv[4], kv[5]), pkhf(kv[6], kv[7])});
        if (tid == 0) st4(ws, WS_KSC + d * 4, __builtin_bit_cast(u32, pow2i(se > 100 ? -100 : -se)));
    } else {
        b -= NB_KGEN;
        const u16* src = w2 + (size_t)b * 32 * DM;
        if (DELAY1 > 0) __builtin_amdgcn_s_sleep(DELAY1);
        const int tok = tid >> 3, sub = tid & 7;
        u32x4 v[9];
#pragma unroll
        for (int i = 0; i < 9; ++i) v[i] = *(const u32x4*)(src + tok * DM + (sub + 8 * i) * 8);
        MARK(0);
#pragma unroll
        for (int i = 0; i < 9; ++i) *(u32x4*)(smem + imgrow(tok) + (sub + 8 * i) * 16) = v[i];
        __syncthreads();
        const int kq = b / 36, kk = b - kq * 36, sp = kk / 9, c = kk - sp * 9;
        const int par = wv & 1, rel = (sp - wv) & 3;
        const int e = rel == 0 ? c : 9 + c * 3 + (rel - 1);
        const int h = lane >> 5, blk = (lane >> 4) & 1, qq = (lane & 15) >> 2, p = lane & 3;
#pragma unroll
        for (int i = 0; i < 9; ++i) {
            const int sg = (i + 1) >> 1, s2r = i == 0 ? 0 : (i - 1) & 1;
            const int T = 9 * (wv >> 1) + (sg == 0 ? 4 : 5 * par + ((kq + sg) & 3));
            const int m = sg == 4 ? 252 + 2 * e + s2r
                        : e < NA ? 7 * e + i
                        : sg == 0 ? 7 * NA + 2 * NB + (e - NA) : 7 * NA + (sg == 1 ? 0 : (2 * sg - 1) * NB) + (e - NA) * 2 + s2r;
            const int r0 = 16 * (s2r ^ par) + 4 * h + qq;
            const u32 col = T * 64 + blk * 32 + p * 8;
            st16(ws, WS_WF + ((kq * 4 + wv) * 648 + 324 + m) * 1024 + lane * 16, trread2(smem, imgrow(r0) + col, imgrow(r0 + 8) + col));
        }
    }
    asm volatile("k_prep_end:");
}

constexpr int NB_CONV = 576, NB_W1 = 144;
constexpr int CONV_LDS = 36864 + 4096;
constexpr int UCP = 4640, KB_OFF = 4 * UCP;

extern "C" __global__ __launch_bounds__(256, 4) void k_conv(const u16* __restrict__ dskip, const u16* __restrict__ w1,
                                              char* __restrict__ ws) {
    extern __shared__ __attribute__((aligned(16))) char smem[];
    const int tid = threadIdx.x, lane = tid & 63;
    const int wv = __builtin_amdgcn_readfirstlane(tid >> 6);
    RTSTAMP(1);
    const float* mod = (const float*)(ws + WS_MOD);
    int b = blockIdx.x;
    if (b >= NB_PF) b = b < NB_PF + NB_CONV ? b + NB_W1 : b - NB_CONV;
    if (b < NB_PF) {
        CODE_PREFETCH(k_mlp);
    } else if ((b -= NB_PF) < NB_W1) {
        const int q = b / 9, c = b - q * 9, f0 = b * 32;
        if (DELAY2 > 0) __builtin_amdgcn_s_sleep(DELAY2);
        const int seg = tid & 3, rg = tid >> 2;
        u32x4 v[9]; float sb[9], cb[9];
#pragma unroll
        for (int i = 0; i < 9; ++i) {
            const int row = rg + 64 * i;
            v[i] = *(const u32x4*)(w1 + (size_t)row * FF + f0 + seg * 8);
            sb[i] = mod[3 * DM + row]; cb[i] = mod[4 * DM + row];
        }
        MARK(0);
        float S[8], B[8];
#pragma unroll
        for (int k = 0; k < 8; ++k) { S[k] = 0.f; B[k] = 0.f; }
#pragma unroll
        for (int i = 0; i < 9; ++i) {
            const float c1 = rbf(1.f + cb[i]);
            u32x4 o;
#pragma unroll
            for (int k = 0; k < 4; ++k) {
                const float lo = bflo(v[i][k]), hi = bfhi(v[i][k]);
                o[k] = pkbf(lo * c1, hi * c1);
                S[2 * k] += bflo(o[k]); S[2 * k + 1] += bfhi(o[k]);
                B[2 * k] = fmaf(sb[i], lo, B[2 * k]); B[2 * k + 1] = fmaf(sb[i], hi, B[2 * k + 1]);
            }
            *(u32x4*)(smem + (tid + 256 * i) * 16) = o;
        }
#pragma unroll
        for (int k = 0; k < 8; ++k) {
            S[k] += dppf<0x114>(S[k]); S[k] += dppf<0x118>(S[k]);
            B[k] += dppf<0x114>(B[k]); B[k] += dppf<0x118>(B[k]);
        }
        float* red = (float*)(smem + 36864);
        if ((lane & 15) >= 12) {
            float* r = red + (tid >> 4) * 64 + seg * 8;
            *(f32x4*)(r) = (f32x4){S[0], S[1], S[2], S[3]}; *(f32x4*)(r + 4) = (f32x4){S[4], S[5], S[6], S[7]};
            *(f32x4*)(r + 32) = (f32x4){B[0], B[1], B[2], B[3]}; *(f32x4*)(r + 36) = (f32x4){B[4], B[5], B[6], B[7]};
        }
        __syncthreads();
        MARK(1);
        const int h = lane >> 5, blk = (lane >> 4) & 1, qq = (lane & 15) >> 2, p = lane & 3;
#pragma unroll
        for (int i = 0; i < 9; ++i) {
            const int s = wv * 9 + i;
            const u32 a = (16 * s + 8 * h + qq) * 64 + (blk ^ (q & 1)) * 32 + p * 8;
            const int sl = s - XROT * (q & 3) < 0 ? s - XROT * (q & 3) + 36 : s - XROT * (q & 3);
            st16(ws, WS_WF + (q * 648 + c * 36 + sl) * 1024 + lane * 16, trread2(smem, a, a + 256));
        }
        if (wv == 0) {
            float t = 0.f;
#pragma unroll
            for (int i = 0; i < 16; ++i) t += red[i * 64 + lane];
            const float B_ = __builtin_bit_cast(float, __builtin_amdgcn_ds_bpermute((lane ^ 32) * 4, __builtin_bit_cast(int, t)));
            const float S_ = -t;
            const float s1 = rbf(S_), s2 = rbf(S_ - s1), s3 = S_ - s1 - s2;
            const float b1 = rbf(B_), b2 = rbf(B_ - b1), b3 = B_ - b1 - b2;
            const bool lo = lane < 32;
            st16(ws, WS_EX + q * 10240 + c * 1024 + (lane ^ ((q & 1) << 4)) * 16,
                 (u32x4){lo ? pkbf(s1, s2) : 0u, lo ? pkbf(s3, b1) : 0u, lo ? pkbf(b2, b3) : 0u, 0u});
        } else if (wv == 1 && c == 0) {
            const int s = q & 3, kq = q >> 2;
            const int d = lane < 32 ? (9 * (s >> 1) + 5 * (s & 1) + kq) * 32 + lane : (9 * (s >> 1) + 4) * 32 + (lane - 32);
            st4(ws, WS_EX + q * 10240 + 9216 + lane * 4, __builtin_bit_cast(u32, mod[5 * DM + d]));
        }
    } else {
        const int d = b - NB_W1;
        const int tp = tid > 0 ? tid * 8 - 4 : 0;
        const char* xb = ws + WS_XT + d * 64;
        const u32x4 x0 = *(const u32x4*)(xb + (tid >> 2) * 36864 + (tid & 3) * 16);
        const u32x2 xm = *(const u32x2*)(xb + (tp >> 5) * 36864 + (tp & 31) * 2);
        u32x4 st[3];
        st[0] = *(const u32x4*)(ws + WS_STAT + tp * 4);
        st[1] = *(const u32x4*)(ws + WS_STAT + tid * 32);
        st[2] = *(const u32x4*)(ws + WS_STAT + tid * 32 + 16);
        const u32x4 kv = *(const u32x4*)(ws + WS_KR + d * 4096 + tid * 16);
        const float sa = mod[d], ca = mod[DM + d], ga = mod[2 * DM + d];
        const float dsk = bf2f(dskip[d]);
        const float kinv = ((const float*)(ws + WS_KSC))[d];
        {
            const u32x4 z = {0u, 0u, 0u, 0u};
            if (tid < 96) *(u32x4*)(smem + (tid / 24) * UCP + (tid % 24) * 16) = z;
            else if (tid < 104) *(u32x4*)(smem + KB_OFF + 4096 + (tid - 96) * 16) = z;
        }
        MARK(0);
        const float c1 = rbf(1.f + ca);
        const int he = expo(64.f * fabsf(c1) + 2.f * fabsf(sa)) - 14;
        const float hs = pow2i(-he), oscale = pow2i(he) * kinv;
        const u32 xw[6] = {xm[0], xm[1], x0[0], x0[1], x0[2], x0[3]};
        u32 P[6], hb[4];
#pragma unroll
        for (int k = 0; k < 6; ++k) {
            const u32 s0 = st[k >> 1][(k & 1) * 2], s1 = st[k >> 1][(k & 1) * 2 + 1];
            const float n0 = (bflo(xw[k]) - bflo(s0)) * __builtin_amdgcn_rcpf(bfhi(s0));
            const float n1 = (bfhi(xw[k]) - bflo(s1)) * __builtin_amdgcn_rcpf(bfhi(s1));
            const u32 t = pkbf(fmaf(n0, c1, sa), fmaf(n1, c1, sa));
            if (k >= 2) hb[k - 2] = t;
            P[k] = pkhf(bflo(t) * hs, bfhi(t) * hs);
        }
        if (tid == 0) { P[0] = 0u; P[1] = 0u; }
        u32 Q[5];
#pragma unroll
        for (int k = 0; k < 5; ++k) Q[k] = __builtin_amdgcn_alignbit(P[k + 1], P[k], 16);
        {
            char* ub = smem + 384 + tid * 16;
            *(u32x4*)(ub) = (u32x4){Q[0], Q[1], Q[2], Q[3]};
            *(u32x4*)(ub + UCP) = (u32x4){P[1], P[2], P[3], P[4]};
            *(u32x4*)(ub + 2 * UCP) = (u32x4){Q[1], Q[2], Q[3], Q[4]};
            *(u32x4*)(ub + 3 * UCP) = (u32x4){P[2], P[3], P[4], P[5]};
            *(u32x4*)(smem + KB_OFF + tid * 16) = kv;
        }
        __syncthreads();
        MARK(1);
        const int c = lane & 15, g = lane >> 4;
        f32x4 acc[8];
#pragma unroll
        for (int j = 0; j < 8; ++j) acc[j] = (f32x4){0.f, 0.f, 0.f, 0.f};
#pragma unroll
        for (int rr = 0; rr < 2; ++rr) {
            const int r = 2 * wv + rr;
            const char* kb = smem + KB_OFF + 2 * (2076 - 4 * c + 8 * g - 32 * r);
            const char* ub = smem + (c & 3) * UCP + 2 * (8 * g + 64 * (c >> 2) + 32 * (1 - r) + 192);
            f16x8 kf[8], uf[8];
#pragma unroll
            for (int i = 0; i < 8; ++i) {
                const u32x2 a = *(const u32x2*)(kb - i * 512), a2 = *(const u32x2*)(kb - i * 512 + 8);
                kf[i] = __builtin_bit_cast(f16x8, (u32x4){a[0], a[1], a2[0], a2[1]});
                uf[i] = *(const f16x8*)(ub + i * 512);
            }
#pragma unroll
            for (int i = 0; i < 8; ++i)
#pragma unroll
                for (int dl = 0; dl + i < 8; ++dl)
                    acc[i + dl] = __builtin_amdgcn_mfma_f32_16x16x32_f16(uf[dl], kf[i], acc[i + dl], 0, 0, 0);
        }
        __syncthreads();
        MARK(2);
#pragma unroll
        for (int j = 0; j < 8; ++j) *(f32x4*)(smem + wv * 8192 + j * 1024 + lane * 16) = acc[j];
        __syncthreads();
        f32x4 y0 = *(const f32x4*)(smem + tid * 32), y1 = *(const f32x4*)(smem + tid * 32 + 16);
#pragma unroll
        for (int w = 1; w < 4; ++w) {
            y0 += *(const f32x4*)(smem + w * 8192 + tid * 32);
            y1 += *(const f32x4*)(smem + w * 8192 + tid * 32 + 16);
        }
        const float y[8] = {y0[0], y0[1], y0[2], y0[3], y1[0], y1[1], y1[2], y1[3]};
        u32x4 o;
#pragma unroll
        for (int k = 0; k < 4; ++k) {
            const u32 yb = pkbf(y[2 * k] * oscale, y[2 * k + 1] * oscale);
            const u32 sk = pkbf(bflo(hb[k]) * dsk, bfhi(hb[k]) * dsk);
            const u32 z = pkbf(bflo(yb) + bflo(sk), bfhi(yb) + bfhi(sk));
            const u32 gz = pkbf(bflo(z) * ga, bfhi(z) * ga);
            o[k] = pkbf(bflo(x0[k]) + bflo(gz), bfhi(x0[k]) + bfhi(gz));
        }
        MARK(3);
        st16(ws, WS_X1F + (tid >> 4) * 147456 + ((d / 48) * 12 + ((tid >> 2) & 3) * 3 + (d >> 4) % 3) * 1024 + (d & 15) * 64 + (tid & 3) * 16, o);
    }
    asm volatile("k_conv_end:");
}
constexpr u32 L_EXF = 147456;
constexpr int MLP_LDS = L_EXF + 12288;
constexpr int GE = 648;

#if defined(__HIP_DEVICE_COMPILE__)
#pragma clang attribute push(__attribute__((target("no-packed-fp32-ops"))), apply_to = function)
#endif
template <int N> struct IC { static constexpr int value = N; };
template <int A, int B, class F> DI void static_for(F&& f) {
    if constexpr (B - A == 1) f(IC<A>{});
    else if constexpr (A < B) { static_for<A, (A + B) / 2>(f); static_for<(A + B) / 2, B>(f); }
}
constexpr int imin(int a, int b) { return a < b ? a : b; }
constexpr int XW = 6;
#define XSP 2
constexpr int XI = 7;
constexpr int x_dmas(int j) { return j <= 0 ? 0 : imin((j + XSP - 1) / XSP, 3 * (19 - XI)); }
constexpr int G1 = 36;
#define PLG 614
constexpr int P1 = 36 * 5 + 7, P2 = 36 * 7 + 7, P3 = 400;
#define PREF 0
#define CD1 34
#define CD3 30
#define QW3 8
constexpr int C1 = P1 + CD1, C2 = P2 + CD1, C3 = P3 + CD3;
constexpr bool s_poll(int G) { return G == P1 || G == P2 || G == P3; }
#define PB2 606
#define PL4 640
constexpr int PUB2 = PB2, P4 = PL4;
constexpr int HS0 = 22, HS1 = 35;
constexpr int s_hstore(int G) { return G < 72 ? (G == 66 ? 0 : G == 71 ? 1 : -1) : G < 324 ? (G % 36 == HS0 ? 0 : G % 36 == HS1 ? 1 : -1) : G == 338 ? 0 : G == 346 ? 1 : -1; }
constexpr int B0 = 324 + 7 * NA;
constexpr int slot_end(int sg) { return B0 - 1 + (sg == 1 ? 2 : sg == 0 ? 3 : 2 * sg + 1) * NB; }
constexpr int s_pstore(int G) {
    for (int sg = 0; sg < 4; ++sg) { if (G == slot_end(sg) + 4) return 2 * sg; if (G == slot_end(sg) + 6) return 2 * sg + 1; }
    return -1;
}
constexpr bool s_res(int G) { return G == 620 || G == 622 || G == 624; }
constexpr int s_pub(int G) { return G == 36 * 3 + 24 ? 2 : G == 36 * 5 + 24 ? 4 : G == 372 ? 9 : 0; }
constexpr int m_slot(int m) { return m < 7 * NA ? (m % 7 + 1) / 2 : m < 7 * NA + 2 * NB ? 1 : m < 7 * NA + 3 * NB ? 0 : m < 252 ? 2 + (m - 7 * NA - 3 * NB) / (2 * NB) : 4; }
constexpr int m_ent(int m) { return m < 7 * NA ? m / 7 : m < 7 * NA + 2 * NB ? NA + (m - 7 * NA) / 2 : m < 7 * NA + 3 * NB ? NA + (m - 7 * NA - 2 * NB)
                                  : m < 252 ? NA + ((m - 7 * NA - 3 * NB) % (2 * NB)) / 2 : (m - 252) / 2; }
constexpr int m_half(int m) { return m < 7 * NA ? (m % 7 == 0 ? 0 : (m % 7 - 1) & 1) : m < 7 * NA + 2 * NB ? (m - 7 * NA) & 1 : m < 7 * NA + 3 * NB ? 0
                                   : m < 252 ? (m - 7 * NA - 3 * NB) & 1 : m & 1; }
constexpr bool m_first(int m) { return m == 0 || m == 1 || m == 3 || m == 5 || m == 252; }
constexpr bool ent_first(int m) { return m < 7 * NA ? m % 7 == 0 : (m < 7 * NA + 2 * NB && ((m - 7 * NA) & 1) == 0); }
constexpr bool ent_own(int e) { return e < 9; }
constexpr int first_use(int e) { return e < NA ? 324 + 7 * e : B0 + 2 * (e - NA); }
constexpr bool s_ring(int G) { return G + 12 < GE && (G < 100 ? G % 12 == 0 : G < 490 ? G % 36 == 24 : G % 18 == 6); }
constexpr int ring_slot(int grp) { const int u = (grp / 3) & 3; return (u == 0 ? 0 : u == 1 ? 6 : u == 2 ? 9 : 3) + grp % 3; }
constexpr int need_bar(int grp) { int g = 12 * grp - 12; while (g > 0 && !s_ring(g)) --g; return g; }
struct Sched {
    int dn[GE];
    int dgap[162];
    int hl[GE];
    int pre[GE + 1];
    int hgap[36];
    bool ok;
    constexpr Sched() : dn(), dgap(), hl(), pre(), hgap(), ok(true) {
        int nd = 3 * XW, nh = 0, last2 = -99, lastbar = 24;
        for (int n = 0; n < 162; ++n) dgap[n] = -1;
        for (int e = 0; e < 36; ++e) hgap[e] = -1;
        for (int G = 0; G < GE; ++G) {
            dn[G] = -1; hl[G] = -1;
            int n = 0;
            if (G >= G1) {
                if (s_ring(G)) lastbar = G;
                const bool fixed = s_hstore(G) >= 0 || s_pstore(G) >= 0 || s_res(G);
                const bool quiet = (G >= P1 && G < P1 + 17) || (G >= P2 && G < P2 + 17) || (G >= P3 && G < P3 + QW3);
                const int sp2 = G < 324 ? 4 : 5;
                if (fixed) last2 = 2 * G;
                else if (!quiet && 2 * G - last2 >= sp2) {
                    int dd = 1 << 20, dh = 1 << 20;
                    if (nd < 162 && (nd / 3 < 12 || (nd / 3 - 11) * 12 <= lastbar + 6)) dd = need_bar(nd / 3) - 40;
                    const int lim = G > C3 ? 54 : G > C2 ? 24 : G > C1 ? 12 : 0;
                    if (nh < lim) dh = first_use(9 + nh / 2) - 45;
                    if (dd < (1 << 20) || dh < (1 << 20)) {
                        if (dd <= dh) { dn[G] = nd; dgap[nd] = G; if (need_bar(nd / 3) <= G) ok = false; ++nd; }
                        else { hl[G] = 18 + nh; hgap[9 + nh / 2] = G; if (first_use(9 + nh / 2) <= G) ok = false; ++nh; }
                        last2 = last2 + sp2 > 2 * G - 1 ? last2 + sp2 : 2 * G - (G < 324 ? 0 : 1);
                        n = 1;
                    }
                }
                n += fixed + s_poll(G) + (G == P4) + (s_pub(G) > 0) + (G == PUB2);
            }
            pre[G + 1] = pre[G] + n;
        }
        if (nd != 162 || nh != 54) ok = false;
    }
};
constexpr Sched SCH{};
static_assert(SCH.ok, "schedule misses a deadline");
constexpr int younger(int a, int b) { return imin(SCH.pre[b] - SCH.pre[a + 1], 63); }
constexpr bool s_bar(int G) { return G >= G1 && (s_ring(G) || s_pub(G) > 0 || G == PUB2); }
constexpr int bar_wait(int G) {
    int n = 63;
    if (s_ring(G)) { int grp = 53; while (need_bar(grp) > G) --grp; n = G == 36 ? 3 : younger(SCH.dgap[3 * grp + 2], G); }
    if (G == PUB2) n = imin(n, younger(slot_end(3) + 6, G));
    if (s_pub(G) > 0) n = imin(n, younger(G == 372 ? 346 : 36 * s_pub(G) + HS1, G));
    return n;
}
constexpr int frag_lds(int f) { return ring_slot(f / 12) * 12288 + (f % 12) * 1024; }

constexpr u32 RESL = ring_slot(45) * 12288;
static_assert(SCH.dgap[161] < 620, "M0");
#define SB() __builtin_amdgcn_sched_barrier(0)
#define STAMP(i) do {} while (0)
#define TW(code) do { code; } while (0)
#define MFMA "v_mfma_f32_32x32x16_bf16 "

extern "C" __global__ __launch_bounds__(256, 1) void k_mlp(char* __restrict__ ws, u16* __restrict__ out) {
    extern __shared__ __attribute__((aligned(16))) char smem[];
    const int tid = threadIdx.x, lane = tid & 63;
    const int w = __builtin_amdgcn_readfirstlane(tid >> 6);
    const int b = blockIdx.x, xcd = b & 7, rr = b >> 3;
    const int kq = xcd >> 1, it = 8 * (xcd & 1) + (rr >> 2), s = rr & 3, q = 4 * kq + s, par = s & 1;
    const auto rs = __builtin_amdgcn_make_buffer_rsrc(ws, 0, WS_END, 0x00020000);
    const u32x4 srd = {(u32)(uintptr_t)ws, (u32)((uintptr_t)ws >> 32) & 0xffffu, WS_END, 0x00020000u};
    const u32 voff = lane * 16;
    const u32 lbase = (u32)(uintptr_t)(LAS char*)smem;
    const u32 wl = lbase + w * 3072;
    auto set_m0 = [&](u32 v) { asm volatile("s_mov_b32 m0, %0" :: "s"(v) : "memory"); };
    auto dma = [&](auto O, u32 so) {
        const u32 vo = voff; const u32x4 sr = srd;
        asm volatile("buffer_load_dwordx4 %0, %1, %2 offen offset:%3 lds" :: "v"(vo), "s"(sr), "s"(so), "n"(O.value * 1024) : "memory");
    };
    auto dma3 = [&](u32 dst, u32 so) {
        asm volatile("s_mov_b32 m0, %3\n\ts_nop 0\n\tbuffer_load_dwordx4 %0, %1, %2 offen lds\n\t"
                     "buffer_load_dwordx4 %0, %1, %2 offen offset:1024 lds\n\tbuffer_load_dwordx4 %0, %1, %2 offen offset:2048 lds"
                     :: "v"(voff), "s"(srd), "s"(so), "s"(dst) : "memory");
    };
    const u32 xsb = WS_X1F + it * 147456;
    const u32 wfb = WS_WF + q * 663552 + w * 3072;
    auto rq_dst = [&](int p) -> u32 {
        const int x = p <= 2 ? p - 1 : p <= 6 ? p - 2 : p - 3;
        return wl + (p == 0 ? 0 : p == 3 ? 12288 : p == 7 ? 24576 : p == 15 ? L_EXF : p > 15 ? ring_slot(p - 13) * 12288 : (3 + x % 9) * 12288);
    };
    auto rq_src = [&](int p) -> u32 {
        const int x = p <= 2 ? p - 1 : p <= 6 ? p - 2 : p - 3;
        return p == 0 ? wfb : p == 3 ? wfb + 12288 : p == 7 ? wfb + 24576 : p == 15 ? WS_EX + q * 10240 + w * 3072
             : p > 15 ? wfb + (p - 13) * 12288 : xsb + w * 3072 + x * 12288;
    };
#pragma unroll
    for (int p = 0; p < XI; ++p) dma3(rq_dst(p), rq_src(p));
    set_m0(rq_dst(XI));
    const u32 hfb = WS_HF + (it * 4 + kq) * 36 * 8192 + w * 2048;
    u32x4 Wf[18], Ef;
    const u32 rd0 = lbase + voff;
    const u32 rb[3] = {rd0, rd0 + 49152, rd0 + 98304};
    auto rdw = [&](auto F) {
        constexpr int lo = frag_lds(F.value);
        u32x4& d = Wf[F.value % 18]; const u32 base = rb[lo / 49152];
        asm volatile("ds_read_b128 %0, %1 offset:%2" : "=v"(d) : "v"(base), "n"(lo % 49152) : "memory");
    };
    auto rde = [&](auto C) {
        u32x4& d = Ef; const u32 base = rb[2];
        asm volatile("ds_read_b128 %0, %1 offset:%2" : "=v"(d) : "v"(base), "n"(L_EXF - 98304 + C.value * 1024) : "memory");
    };
    f32x16 acc[2];

    u32x4 Xa[36];
    f32x16 gram, rsum;
    {
        const u32 a0 = wl + (8 * (lane >> 5) + ((lane & 15) >> 2)) * 64 + ((lane >> 4) & 1) * 32 + (lane & 3) * 8;
        u32x4 ones = {0x3f803f80u, 0x3f803f80u, 0x3f803f80u, 0x3f803f80u};
        asm volatile("" : "+v"(ones));
        u32x2 t[2][6];
        auto step = [&](auto A) {
            constexpr int a = A.value;
            constexpr int pos = a <= 1 ? a + 1 : a <= 4 ? a + 2 : a + 3;
            constexpr int issued = 3 * XI + x_dmas(9 * a - 3);
            static_assert(issued >= 3 * pos + 3, "X order");
            u32x2 (&d)[6] = t[a & 1];
            const u32 ad = a0 + (3 + a % 9) * 12288;
            if constexpr (a % 4 == 0) asm volatile("s_waitcnt vmcnt(%0)\n\ts_barrier" :: "n"(issued - 3 * pos - 3) : "memory");
            else asm volatile("s_waitcnt vmcnt(%0)" :: "n"(issued - 3 * pos - 3) : "memory");
            asm volatile("ds_read_b64_tr_b16 %0, %6\n\tds_read_b64_tr_b16 %1, %6 offset:256\n\t"
                         "ds_read_b64_tr_b16 %2, %6 offset:1024\n\tds_read_b64_tr_b16 %3, %6 offset:1280\n\t"
                         "ds_read_b64_tr_b16 %4, %6 offset:2048\n\tds_read_b64_tr_b16 %5, %6 offset:2304"
                         : "=&v"(d[0]), "=&v"(d[1]), "=&v"(d[2]), "=&v"(d[3]), "=&v"(d[4]), "=&v"(d[5]) : "v"(ad) : "memory");
            rdw(IC<3 * a>{}); rdw(IC<3 * a + 1>{}); rdw(IC<3 * a + 2>{});
        };
        step(IC<0>{});
        STAMP(1);
        u32x4 xv;
        static_for<0, 108>([&](auto J) {
            constexpr int j = J.value, a = j / 9, z = (j % 9) / 3, k = j % 3;
            u32x2 (&d)[6] = t[a & 1];
            if constexpr (j % 9 == 0)
                asm volatile("s_waitcnt lgkmcnt(0)" : "+v"(d[0]), "+v"(d[1]), "+v"(d[2]), "+v"(d[3]), "+v"(d[4]), "+v"(d[5]) :: "memory");
            if constexpr (j % 9 == 6 && a < 11) step(IC<a + 1>{});
            u32x4& x = xv; u32x4& wf = Wf[(3 * a + z) % 18]; u32x4& on = ones;
            f32x16& ac = k == 0 ? acc[0] : k == 1 ? gram : rsum;
            if constexpr (k == 0) { xv = (u32x4){d[2 * z][0], d[2 * z][1], d[2 * z + 1][0], d[2 * z + 1][1]}; asm volatile("s_nop 1" : "+v"(x)); }
            u32x4& am = k == 0 ? wf : k == 1 ? x : on;
            if constexpr (j < 3) asm volatile(MFMA "%0, %1, %2, 0" : "=&v"(ac) : "v"(am), "v"(x));
            else asm volatile(MFMA "%0, %1, %2, %0" : "+v"(ac) : "v"(am), "v"(x));
            SB();
            if constexpr (k == 0) { Xa[3 * a + z] = xv; asm volatile("" : "+a"(Xa[3 * a + z])); }
            if constexpr (x_dmas(j + 1) > x_dmas(j)) {
                constexpr int n = x_dmas(j), p = XI + n / 3;
                static_assert(p < 12 || p == 15 || 9 * (p < 15 ? p - 12 : p - 13) <= j - (j % 9), "slot still holds unread x");
                dma(IC<n % 3>{}, rq_src(p));
                if constexpr (n % 3 == 2 && p < 18) set_m0(rq_dst(p + 1));
            }
            SB();
        });
    }
    float ysc;
    u32x4 Xe;
    {
        asm volatile("s_waitcnt vmcnt(6)\n\ts_barrier" ::: "memory");
        STAMP(2);
        static_for<36, 48>(rdw);
        rde(IC<0>{});
        set_m0(wl + ring_slot(XW) * 12288);
        SB();
    }
    float sq, mean;
    auto stats = [&](auto ST) {
        constexpr int st = ST.value;
        const int sel = (lane & 3) + 4 * ((lane & 31) >> 3);
        if constexpr (st == 0) sq = gram[0];
        if constexpr (st < 5) {
#pragma unroll
            for (int i = 1 + 3 * st; i < 4 + 3 * st; ++i) sq = sel == i ? gram[i] : sq;
            asm volatile("" : "+v"(sq));
        }
        if constexpr (st == 5) {
            const auto r = __builtin_amdgcn_permlane32_swap(__builtin_bit_cast(u32, sq), __builtin_bit_cast(u32, sq), false, false);
            const u32 r0 = r[0], r1 = r[1];
            sq = __builtin_bit_cast(float, (lane & 4) ? r1 : r0);
            mean = rsum[0] * (1.f / DM);
            asm volatile("" : "+v"(sq), "+v"(mean));
        }
        if constexpr (st == 6) { sq = rbf(rbf(fmaxf(fmaf(-mean, mean, sq * (1.f / DM)), 0.f)) + 1e-5f); asm volatile("" : "+v"(sq)); }
        if constexpr (st == 7) { sq = rbf(__builtin_amdgcn_sqrtf(sq)); ysc = -1.44269504088896340736f * __builtin_amdgcn_rcpf(sq); asm volatile("" : "+v"(sq), "+v"(ysc)); }
        if constexpr (st == 8) {
            const bool lo = lane < 32;
            Xe = (u32x4){lo ? pkbf(mean, mean) : 0u, lo ? pkbf(mean, sq) : 0u, lo ? pkbf(sq, sq) : 0u, 0u};
            asm volatile("" : "+v"(Xe));
        }
    };

    f32x16 acc2[5];
    float yv[16], ev[16];
    u32x4 H[36][2], pollv;
    auto flag_store = [&](u32 level, u32 so) {
        asm volatile("s_mov_b64 exec, 1\n\tbuffer_store_dword %0, off, %1, %2 sc1\n\ts_mov_b64 exec, -1" :: "v"(level), "s"(srd), "s"(so) : "memory");
    };
    auto act = [&](auto C, auto HS) {
        constexpr int t = HS.value / 2, e = C.value;
        if constexpr (HS.value % 2 == 0) {
            if constexpr (t < 16) { yv[t] = acc[C.value & 1][t] * ysc; asm volatile("" : "+v"(yv[t])); }
            if constexpr (t >= 2) { yv[t - 2] = yv[t - 2] * ev[t - 2]; asm volatile("" : "+v"(yv[t - 2])); }
            if constexpr (t < 16) { ev[t] = __builtin_amdgcn_exp2f(yv[t]); asm volatile("" : "+v"(ev[t])); }
        } else {
            if constexpr (t >= 1 && t < 17) { ev[t - 1] = 1.f + ev[t - 1]; asm volatile("" : "+v"(ev[t - 1])); }
            if constexpr (t >= 3 && (t & 1)) {
                u32 pk = pkbf(yv[t - 3], yv[t - 2]);
                asm volatile("" : "+v"(pk));
                H[e][(t - 3) / 8][((t - 3) / 2) & 3] = pk;
            }
            if constexpr (t >= 1 && t < 17) { ev[t - 1] = __builtin_amdgcn_rcpf(ev[t - 1]); asm volatile("" : "+v"(ev[t - 1])); }
        }
    };
    const u32 vh0 = voff + par * 1024, vh1 = voff + (par ^ 1) * 1024;
    const u32 hown = hfb + 9 * s * 8192;
    const u32 hpeer[3] = {hfb + 9 * ((s + 1) & 3) * 8192, hfb + 9 * ((s + 2) & 3) * 8192, hfb + 9 * ((s + 3) & 3) * 8192};
    auto hload = [&](auto E, auto HF) {
        constexpr int e = E.value, hf = HF.value, k = e - 9;
        const u32 so = hpeer[k % 3] + (k / 3) * 8192, vo = hf ? vh1 : vh0; const u32x4 sr = srd;
        u32x4& d = H[e][hf];
        asm volatile("buffer_load_dwordx4 %0, %1, %2, %3 offen sc1" : "=a"(d) : "v"(vo), "s"(sr), "s"(so) : "memory");
    };
    const u32 vzero = 0;
    const u32 fso1 = WS_FLAG + (it * 4 + kq) * 64;
    auto poll = [&]() {
        asm volatile("buffer_load_dwordx4 %0, %1, %2, %3 offen sc1" : "=v"(pollv) : "v"(vzero), "s"(srd), "s"(fso1) : "memory");
    };
    auto check = [&](u32 level) {
        asm volatile("" : "+v"(pollv));
#pragma nounroll
        for (u32 spins = 0; spins < (1u << 20); ++spins) {
            const bool bad = pollv[0] < level || pollv[1] < level || pollv[2] < level || pollv[3] < level;
            if (__builtin_amdgcn_ballot_w64(bad) == 0ull) break;
            __builtin_amdgcn_s_sleep(1);
            asm volatile("buffer_load_dwordx4 %0, %1, %2, %3 offen sc1\n\ts_waitcnt vmcnt(0)"
                         : "=v"(pollv) : "v"(vzero), "s"(srd), "s"(fso1) : "memory");
        }
    };
    const u32 pgb = WS_PART + (it * 4 + s) * 98304 + w * 2048;
    const u32 phb = WS_PH + (it * 2 + (s >> 1)) * 65536 + w * 16384;
    const int Town = 9 * (s >> 1) + 5 * par + kq, Thalf = 9 * (s >> 1) + 4;
    auto xfrag = [&](int ks) -> u32 { return xsb + ((ks / 3) * 12 + 3 * w + ks % 3) * 1024; };
    const u32 fso2 = WS_FLAG + 4096 + (it * 2 + (s >> 1)) * 64;
    u32 fv;
    const u32 vfl = (lane & 7) * 4;
    auto poll2 = [&]() {
        asm volatile("buffer_load_dword %0, %1, %2, %3 offen sc1" : "=v"(fv) : "v"(vfl), "s"(srd), "s"(fso2) : "memory");
    };
    auto check2 = [&]() {
        asm volatile("" : "+v"(fv));
#pragma nounroll
        for (u32 spins = 0; spins < (1u << 20); ++spins) {
            if (__builtin_amdgcn_ballot_w64(fv == 0u) == 0ull) break;
            __builtin_amdgcn_s_sleep(1);
            asm volatile("buffer_load_dword %0, %1, %2, %3 offen sc1\n\ts_waitcnt vmcnt(0)" : "=v"(fv) : "v"(vfl), "s"(srd), "s"(fso2) : "memory");
        }
    };
    u32x4 P[6];
    u32 Q[8];
    const u32 vq = voff + ((kq & 1) * 2 + par) * 4;
    const u32 pin = pgb + kq * 24576, qin = phb + (kq >> 1) * 8192;
    auto pload = [&](auto I) {
        constexpr int i = I.value;
        const u32x4 sr = srd;
        if constexpr (i < 4) {
            u32& d0 = Q[2 * i]; u32& d1 = Q[2 * i + 1]; const u32 vo = vq, so = qin + (i >> 1) * 4096;
            asm volatile("buffer_load_dword %0, %2, %3, %4 offen offset:%5 sc1\n\tbuffer_load_dword %1, %2, %3, %4 offen offset:%6 sc1"
                         : "=&v"(d0), "=&v"(d1) : "v"(vo), "s"(sr), "s"(so), "n"((i & 1) * 2048), "n"((i & 1) * 2048 + 1024) : "memory");
        } else {
            u32x4& d = P[i - 4]; const u32 vo = voff, so = pin + ((i - 4) >> 1) * 8192;
            asm volatile("buffer_load_dwordx4 %0, %1, %2, %3 offen offset:%4 sc1" : "=v"(d) : "v"(vo), "s"(sr), "s"(so), "n"(((i - 4) & 1) * 1024) : "memory");
        }
    };
    auto pstore = [&](int sg, int pt, u32x4 v) {
        if (sg == 0) __builtin_amdgcn_raw_buffer_store_b128(v, rs, voff, phb + pt * 8192 + (2 * kq + par) * 1024, 16);
        else __builtin_amdgcn_raw_buffer_store_b128(v, rs, voff + pt * 1024, pgb + ((kq + sg) & 3) * 24576 + (sg - 1) * 8192, 16);
    };

    static_for<G1, GE>([&](auto GG) {
        constexpr int G = GG.value, c = G / 36, g = G % 36, m = G - 324;
        if constexpr (g == 0 && G > 0) STAMP(2 + G / 36);
        if constexpr (s_bar(G)) {
            if constexpr (G % 6 == 0) TW(asm volatile("s_waitcnt vmcnt(%0) lgkmcnt(%1)\n\ts_barrier" :: "n"(bar_wait(G)), "n"(G == G1 ? 7 : 6) : "memory"));
            else TW(asm volatile("s_waitcnt vmcnt(%0)\n\ts_barrier" :: "n"(bar_wait(G)) : "memory"));
        } else if constexpr (G % 6 == 0) asm volatile("s_waitcnt lgkmcnt(%0)" :: "n"(G == G1 ? 7 : G + 6 < GE ? 6 : 0) : "memory");
        u32x4& wf = Wf[G % 18];
        if constexpr (G < 324) {
            f32x16& ac = acc[c & 1]; u32x4& xa = Xa[g];
            if constexpr (g == 0) asm volatile(MFMA "%0, %1, %2, 0" : "=&v"(ac) : "v"(wf), "a"(xa));
            else asm volatile(MFMA "%0, %1, %2, %0" : "+v"(ac) : "v"(wf), "a"(xa));
            if constexpr (g == (c == 1 ? 30 : 12)) {
                u32x4& ef = Ef; u32x4& xe = Xe;
                SB();
                if constexpr (c > 1) act(IC<c - 1>{}, IC<11>{});
                SB();
                asm volatile(MFMA "%0, %1, %2, %0" : "+v"(ac) : "v"(ef), "v"(xe));
            }
            if constexpr (G == 46) { u32x4& ef = Ef; u32x4& xe = Xe; f32x16& a0 = acc[0]; SB(); asm volatile(MFMA "%0, %1, %2, %0" : "+v"(a0) : "v"(ef), "v"(xe)); }
        } else {
            constexpr int sl = m_slot(m), e = m_ent(m), hf = m_half(m);
            if constexpr (ent_first(m) && !ent_own(e))
                TW(asm volatile("s_waitcnt vmcnt(%0)" :: "n"(younger(SCH.hgap[e], G)) : "memory"));
            f32x16& ac = acc2[sl]; u32x4& hh = H[e][hf];
            if constexpr (m_first(m)) asm volatile(MFMA "%0, %1, %2, 0" : "=&v"(ac) : "v"(wf), "v"(hh));
            else if constexpr (ent_own(e)) asm volatile(MFMA "%0, %1, %2, %0" : "+v"(ac) : "v"(wf), "v"(hh));
            else asm volatile(MFMA "%0, %1, %2, %0" : "+v"(ac) : "v"(wf), "a"(hh));
        }
        SB();
        if constexpr (G + 12 < GE) rdw(IC<G + 12>{});
        if constexpr (G == 48) rde(IC<1>{});
        if constexpr (g == (c == 1 ? 32 : 14) && c < 8) rde(IC<c + 1>{});
        if constexpr (SCH.dn[G] >= 0) {
            dma(IC<SCH.dn[G] % 3>{}, wfb + (SCH.dn[G] / 3) * 12288);
            if constexpr (SCH.dn[G] % 3 == 2 && SCH.dn[G] < 161) set_m0(wl + ring_slot(SCH.dn[G] / 3 + 1) * 12288);
        }
        if constexpr (G >= 36 && G < 45) stats(IC<G - 36>{});
        if constexpr (G >= 48 && G < 72) {
            act(IC<0>{}, IC<(3 * (G - 48) + 1) / 2>{});
            if constexpr (((G - 48) & 1) == 0) act(IC<0>{}, IC<(3 * (G - 48)) / 2 + 1>{});
        }
        if constexpr (G >= 72 && G < 324 && g >= 1) act(IC<c - 1>{}, IC<(g < 12 ? g - 1 : g)>{});
        if constexpr (G >= 326 && G < 344) { act(IC<8>{}, IC<2 * (G - 326)>{}); act(IC<8>{}, IC<2 * (G - 326) + 1>{}); }
        if constexpr (s_hstore(G) >= 0) {
            constexpr int hf = s_hstore(G), cc = c - 1;
            __builtin_amdgcn_raw_buffer_store_b128(H[cc][hf], rs, hf ? vh1 : vh0, hown + cc * 8192, 16);
        }
        if constexpr (s_pub(G) > 0) flag_store(s_pub(G), fso1 + s * 4);
        if constexpr (G == PUB2) flag_store(1u, fso2 + (2 * kq + par) * 4);
        if constexpr (G == C1 || G == C2 || G == C3) {
            TW(asm volatile("s_waitcnt vmcnt(%0)" :: "n"(younger(G == C1 ? P1 : G == C2 ? P2 : P3, G)) : "memory"));
            check(G == C1 ? 2 : G == C2 ? 4 : 9);
        }
        if constexpr (G == P1 || G == P2 || G == P3) poll();
        if constexpr (G == P4) poll2();
        if constexpr (SCH.hl[G] >= 0) hload(IC<SCH.hl[G] / 2>{}, IC<SCH.hl[G] % 2>{});
        if constexpr (s_pstore(G) >= 0) {
            constexpr int sg = s_pstore(G) / 2, pt = s_pstore(G) % 2;
            const f32x16& a = acc2[sg];
            const u32x4 v = {pkbf(a[8 * pt], a[8 * pt + 1]), pkbf(a[8 * pt + 2], a[8 * pt + 3]), pkbf(a[8 * pt + 4], a[8 * pt + 5]), pkbf(a[8 * pt + 6], a[8 * pt + 7])};
            pstore(sg, pt, v);
        }
        if constexpr (s_res(G)) {
            constexpr int i = (G - 620) / 2;
            set_m0(wl + RESL + i * 1024);
            const u32 so = xfrag(i < 2 ? 2 * Town + i : 2 * Thalf + (kq >> 1));
            asm volatile("s_nop 0" ::: "memory");
            dma(IC<0>{}, so);
        }
        SB();
    });
    STAMP(20);

    {
        u32x2 xr[5];
        f32x4 gb[4];
        f32x2 gh;
        const u32 ra = wl + RESL + (4 * (lane >> 5) + ((lane & 15) >> 2)) * 64 + ((lane >> 4) & 1) * 32 + (lane & 3) * 8;
        const u32 ga = lbase + L_EXF + 9216 + (lane >> 5) * 16;
        asm volatile("s_waitcnt vmcnt(1)" ::: "memory");
        asm volatile("ds_read_b64_tr_b16 %0, %5\n\tds_read_b64_tr_b16 %1, %5 offset:512\n\tds_read_b64_tr_b16 %2, %5 offset:1024\n\t"
                     "ds_read_b64_tr_b16 %3, %5 offset:1536\n\tds_read_b64_tr_b16 %4, %6 offset:2048"
                     : "=&v"(xr[0]), "=&v"(xr[1]), "=&v"(xr[2]), "=&v"(xr[3]), "=&v"(xr[4]) : "v"(ra), "v"(ra + (kq & 1) * 512) : "memory");
        asm volatile("ds_read_b128 %0, %5\n\tds_read_b128 %1, %5 offset:32\n\tds_read_b128 %2, %5 offset:64\n\tds_read_b128 %3, %5 offset:96\n\t"
                     "ds_read_b64 %4, %6 offset:128\n\ts_waitcnt vmcnt(0) lgkmcnt(0)"
                     : "=&v"(gb[0]), "=&v"(gb[1]), "=&v"(gb[2]), "=&v"(gb[3]), "=&v"(gh)
                     : "v"(ga), "v"(ga + kq * 32 + par * 8) : "memory");
        asm volatile("" : "+v"(xr[0]), "+v"(xr[1]), "+v"(xr[2]), "+v"(xr[3]), "+v"(xr[4]));
        check2();
        STAMP(21);
        static_for<0, 10>([&](auto I) { pload(IC<9 - I.value>{}); });
        asm volatile("s_waitcnt vmcnt(8)" : "+v"(P[0]), "+v"(P[1]), "+v"(P[2]), "+v"(P[3]), "+v"(P[4]), "+v"(P[5]) :: "memory");
        STAMP(22);
        u16* orow = out + (size_t)(it * 128 + w * 32 + (lane & 31)) * DM + 4 * (lane >> 5);
        auto fin2 = [&](float a0, float a1, float g0, float g1, u32 xk) -> u32 {
            const u32 t = pkbf(a0 * -0.69314718055994530942f, a1 * -0.69314718055994530942f);
            const u32 gz = pkbf(bflo(t) * g0, bfhi(t) * g1);
            return pkbf(bflo(xk) + bflo(gz), bfhi(xk) + bfhi(gz));
        };
#pragma unroll
        for (int k = 0; k < 4; ++k) {
            u32x2 o;
#pragma unroll
            for (int j = 0; j < 2; ++j) {
                const int d = 2 * k + j;
                const u32 p0 = P[d >> 2][d & 3], p1 = P[2 + (d >> 2)][d & 3], p2 = P[4 + (d >> 2)][d & 3], xk = xr[k][j];
                o[j] = fin2(acc2[4][2 * d] + bflo(p0) + bflo(p1) + bflo(p2), acc2[4][2 * d + 1] + bfhi(p0) + bfhi(p1) + bfhi(p2),
                            gb[k][2 * j], gb[k][2 * j + 1], xk);
            }
            *(u32x2*)(orow + Town * 32 + 8 * k) = o;
        }
        asm volatile("s_waitcnt vmcnt(4)" : "+v"(Q[0]), "+v"(Q[1]), "+v"(Q[2]), "+v"(Q[3]), "+v"(Q[4]), "+v"(Q[5]), "+v"(Q[6]), "+v"(Q[7]) :: "memory");
        float a0 = 0.f, a1 = 0.f;
#pragma unroll
        for (int r = 0; r < 8; ++r) { a0 += bflo(Q[r]); a1 += bfhi(Q[r]); }
        *(u32*)(orow + Thalf * 32 + 8 * kq + 2 * par) = fin2(a0, a1, gh[0], gh[1], par ? xr[4][1] : xr[4][0]);
    }
    asm volatile("k_mlp_end:");
}
#if defined(__HIP_DEVICE_COMPILE__)
#pragma clang attribute pop
#endif

extern "C" void kernel_launch(void* const* d_in, const int* in_sizes, int n_in, void* d_out, int out_size,
                              void* d_ws, size_t ws_size, hipStream_t stream) {
    const u16* x = (const u16*)d_in[0];
    char* ws = (char*)d_ws;
    (void)hipFuncSetAttribute((const void*)k_mlp, hipFuncAttributeMaxDynamicSharedMemorySize, MLP_LDS);
    k_prep<<<NB_PREP, 256, PREP_LDS, stream>>>(x, (const u16*)d_in[1], (const u16*)d_in[2], (const u16*)d_in[3],
                                               (const u16*)d_in[4], (const u16*)d_in[5], (const u16*)d_in[9],
                                               (const u16*)d_in[10], (const u16*)d_in[11], ws);
    k_conv<<<NB_PF + NB_CONV + NB_W1, 256, CONV_LDS, stream>>>((const u16*)d_in[6], (const u16*)d_in[8], ws);
    k_mlp<<<256, 256, MLP_LDS, stream>>>(ws, (u16*)d_out);
}
```

```cpp
#define DIAG (-1)
#include <hip/hip_runtime.h>
#include <stdint.h>

typedef unsigned short u16;
typedef unsigned int u32;
typedef float f32x2 __attribute__((ext_vector_type(2)));
typedef float f32x4 __attribute__((ext_vector_type(4)));
typedef float f32x16 __attribute__((ext_vector_type(16)));
typedef _Float16 f16x2 __attribute__((ext_vector_type(2)));
typedef _Float16 f16x8 __attribute__((ext_vector_type(8)));
typedef __bf16 bf16x2 __attribute__((ext_vector_type(2)));
typedef __bf16 bf16x8 __attribute__((ext_vector_type(8)));
typedef short s16x4 __attribute__((ext_vector_type(4)));
typedef u32 u32x2 __attribute__((ext_vector_type(2)));
typedef u32 u32x4 __attribute__((ext_vector_type(4)));
#define LAS __attribute__((address_space(3)))
#define DI __device__ __forceinline__
#define RTSTAMP(k)
#define MARK(i)

constexpr int SEQ = 2048, DM = 576, NS = 128, FF = 4608;

constexpr u32 WS_MOD   = 0;
constexpr u32 WS_STAT  = 16384;
constexpr u32 WS_KSC   = 32768;
constexpr u32 WS_FLAG  = 36864;
constexpr u32 WS_EX    = 65536;
constexpr u32 WS_XT    = 1u << 20;
constexpr u32 WS_KR    = WS_XT + 2359296;
constexpr u32 WS_X1F   = WS_KR + 2359296;
constexpr u32 WS_WF    = WS_X1F + 2359296;
constexpr u32 WS_HF    = WS_WF + 10616832;
constexpr u32 WS_PART  = WS_HF + 18874368;
constexpr u32 WS_PH    = WS_PART + 6291456;
constexpr u32 WS_END   = WS_PH + 2097152;

DI float bflo(u32 u) { return __builtin_bit_cast(float, u << 16); }
DI float bfhi(u32 u) { return __builtin_bit_cast(float, u & 0xffff0000u); }
DI float bf2f(u16 v) { return __builtin_bit_cast(float, ((u32)v) << 16); }
DI u32 pkbf(float a, float b) { f32x2 v = {a, b}; return __builtin_bit_cast(u32, __builtin_convertvector(v, bf16x2)); }
DI u32 pkhf(float a, float b) { f32x2 v = {a, b}; return __builtin_bit_cast(u32, __builtin_convertvector(v, f16x2)); }
DI float rbf(float a) { return bflo(pkbf(a, a)); }
template <int CTRL, int RM = 0xf> DI float dppf(float v) {
    return __builtin_bit_cast(float, __builtin_amdgcn_update_dpp(0, __builtin_bit_cast(int, v), CTRL, RM, 0xf, true));
}
DI u32x4 trread2(const char* smem, u32 off0, u32 off1) {
    const u32x2 l = __builtin_bit_cast(u32x2, __builtin_amdgcn_ds_read_tr16_b64_v4i16((LAS s16x4*)(smem + off0)));
    const u32x2 h = __builtin_bit_cast(u32x2, __builtin_amdgcn_ds_read_tr16_b64_v4i16((LAS s16x4*)(smem + off1)));
    return (u32x4){l[0], l[1], h[0], h[1]};
}
DI void st16(char* ws, u32 off, u32x4 v) {
    __builtin_amdgcn_raw_buffer_store_b128(v, __builtin_amdgcn_make_buffer_rsrc(ws, 0, WS_END, 0x00020000), off, 0, 16);
}
DI void st4(char* ws, u32 off, u32 v) {
    __builtin_amdgcn_raw_buffer_store_b32(v, __builtin_amdgcn_make_buffer_rsrc(ws, 0, WS_END, 0x00020000), off, 0, 16);
}
DI float pow2i(int e) { e = e < -126 ? -126 : (e > 127 ? 127 : e); return __builtin_bit_cast(float, (u32)(e + 127) << 23); }
DI int expo(float x) { return (int)((__builtin_bit_cast(u32, x) >> 23) & 255u) - 127; }
DI u32 imgrow(u32 row) { return row * 1184u + (row >> 3) * 128u; }
DI f32x2 cmul(f32x2 z, f32x2 w, f32x2 wp) {
    const f32x2 zx = {z[0], z[0]}, zy = {z[1], z[1]};
    return __builtin_elementwise_fma(zy, wp, zx * w);
}
#define CODE_PREFETCH(sym) do { u32 lo_, hi_, len_; \
    asm volatile("s_getpc_b64 s[20:21]\n\ts_add_u32 s20, s20, " #sym "@rel32@lo+4\n\ts_addc_u32 s21, s21, " #sym "@rel32@hi+12\n\t" \
                 "s_mov_b32 %0, s20\n\ts_mov_b32 %1, s21\n\ts_mov_b32 %2, " #sym "_end-" #sym : "=s"(lo_), "=s"(hi_), "=s"(len_) :: "scc", "s20", "s21"); \
    const char* p_ = (const char*)(((unsigned long long)hi_ << 32) | lo_); \
    if (threadIdx.x * 128u < len_) { u32 t_; asm volatile("global_load_dword %0, %1, off\n\ts_waitcnt vmcnt(0)" : "=v"(t_) : "v"(p_ + threadIdx.x * 128u) : "memory"); } \
    } while (0)

#define DELAY1 20
#define DELAY2 0
#define XROT 0
#define NAV 26
constexpr int NA = NAV, NB = 36 - NA;
constexpr int NB_PF = 16, NB_LN = 64, NB_GEMV = 108, NB_KGEN = 576, NB_W2 = 144;
constexpr int NB_PREP = NB_PF + NB_LN + NB_GEMV + NB_KGEN + NB_W2;
constexpr int PREP_LDS = 38400;

extern "C" __global__ __launch_bounds__(256, 4) void k_prep(const u16* __restrict__ x, const u16* __restrict__ logA,
                                              const u16* __restrict__ Aim, const u16* __restrict__ Cre,
                                              const u16* __restrict__ Cim, const u16* __restrict__ logdt,
                                              const u16* __restrict__ w2, const u16* __restrict__ cvec,
                                              const u16* __restrict__ wmod, char* __restrict__ ws) {
    extern __shared__ __attribute__((aligned(16))) char smem[];
    RTSTAMP(0);
    if (blockIdx.x == 20 && threadIdx.x == 0) *(volatile unsigned long long*)(ws + 49152 + 0 * 8) = __builtin_amdgcn_s_memrealtime();
    const int tid = threadIdx.x, lane = tid & 63;
    const int wv = __builtin_amdgcn_readfirstlane(tid >> 6);
    int b = blockIdx.x;
    if (b >= NB_PF && b < NB_PF + NB_KGEN + NB_LN + NB_GEMV) b = b < NB_PF + NB_KGEN ? b + NB_LN + NB_GEMV : b - NB_KGEN;
    if (b < NB_PF) {
        if (b < 8) CODE_PREFETCH(k_prep); else CODE_PREFETCH(k_conv);
        if (b < 2) st16(ws, WS_FLAG + b * 4096 + tid * 16, (u32x4){0u, 0u, 0u, 0u});
    } else if ((b -= NB_PF) < NB_LN) {
        const int t0 = b * 32, tok = tid >> 3, sub = tid & 7;
        u32x4 v[9];
#pragma unroll
        for (int i = 0; i < 9; ++i) v[i] = *(const u32x4*)(x + (size_t)(t0 + tok) * DM + (sub + 8 * i) * 8);
        MARK(0);
#pragma unroll
        for (int i = 0; i < 9; ++i) *(u32x4*)(smem + imgrow(tok) + (sub + 8 * i) * 16) = v[i];
        __syncthreads();
        const int g = lane >> 4, qq = (lane & 15) >> 2, p = lane & 3;
#pragma unroll
        for (int i = 0; i < 9; ++i) {
            const int cb = wv * 9 + i;
            st16(ws, WS_XT + b * 36864 + cb * 1024 + (lane & 15) * 64 + g * 16,
                 trread2(smem, imgrow(8 * g + qq) + cb * 32 + p * 8, imgrow(8 * g + 4 + qq) + cb * 32 + p * 8));
        }
        MARK(1);
        float s = 0.f;
#pragma unroll
        for (int i = 0; i < 9; ++i)
#pragma unroll
            for (int k = 0; k < 4; ++k) s += bflo(v[i][k]) + bfhi(v[i][k]);
        s += dppf<0xB1>(s); s += dppf<0x4E>(s); s += dppf<0x141>(s);
        const float mean = s * (1.f / DM);
        float q = 0.f;
#pragma unroll
        for (int i = 0; i < 9; ++i)
#pragma unroll
            for (int k = 0; k < 4; ++k) {
                const float a = bflo(v[i][k]) - mean, c = bfhi(v[i][k]) - mean;
                q = fmaf(a, a, q); q = fmaf(c, c, q);
            }
        q += dppf<0xB1>(q); q += dppf<0x4E>(q); q += dppf<0x141>(q);
        if (sub == 0) st4(ws, WS_STAT + (t0 + tok) * 4, pkbf(mean, __builtin_amdgcn_sqrtf(rbf(rbf(q * (1.f / DM)) + 1e-5f))));
    } else if ((b -= NB_LN) < NB_GEMV) {
        const int seg = tid & 3, rg = tid >> 2;
        u32x4 v[9]; u16 cv[9];
#pragma unroll
        for (int i = 0; i < 9; ++i) {
            const int row = rg + 64 * i;
            cv[i] = cvec[row];
            v[i] = *(const u32x4*)(wmod + (size_t)row * (6 * DM) + b * 32 + seg * 8);
        }
        MARK(0);
        float acc[8];
#pragma unroll
        for (int k = 0; k < 8; ++k) acc[k] = 0.f;
#pragma unroll
        for (int i = 0; i < 9; ++i) {
            const float c = bf2f(cv[i]);
            const float sl = rbf(c * __builtin_amdgcn_rcpf(1.f + __builtin_amdgcn_exp2f(c * -1.44269504088896340736f)));
#pragma unroll
            for (int k = 0; k < 4; ++k) {
                acc[2 * k] = fmaf(sl, bflo(v[i][k]), acc[2 * k]);
                acc[2 * k + 1] = fmaf(sl, bfhi(v[i][k]), acc[2 * k + 1]);
            }
        }
#pragma unroll
        for (int k = 0; k < 8; ++k) { acc[k] += dppf<0x114>(acc[k]); acc[k] += dppf<0x118>(acc[k]); }
        float* red = (float*)smem;
        if ((lane & 15) >= 12) {
            *(f32x4*)(red + (tid >> 4) * 32 + seg * 8) = (f32x4){acc[0], acc[1], acc[2], acc[3]};
            *(f32x4*)(red + (tid >> 4) * 32 + seg * 8 + 4) = (f32x4){acc[4], acc[5], acc[6], acc[7]};
        }
        __syncthreads();
        MARK(1);
        if (tid < 32) {
            float t = 0.f;
#pragma unroll
            for (int i = 0; i < 16; ++i) t += red[i * 32 + tid];
            st4(ws, WS_MOD + (b * 32 + tid) * 4, __builtin_bit_cast(u32, rbf(t)));
        }
    } else if ((b -= NB_GEMV) < NB_KGEN) {
        const int d = b, hn = wv & 1, grp = wv >> 1;
        const int n = hn * 64 + lane, n2 = n ^ 64;
        const u16 la_ = logA[d * NS + n], ai_ = Aim[d * NS + n], cr_ = Cre[d * NS + n], ci_ = Cim[d * NS + n];
        const u16 cr2_ = Cre[d * NS + n2], ci2_ = Cim[d * NS + n2], ld_ = logdt[d];
        u32 ta[8];
#pragma unroll
        for (int j = 0; j < 8; j += 2) {
            const float l0 = (float)(2047 - tid * 8 - j);
            const u32 pk = pkbf(l0, l0 - 1.f);
            const float p0 = bflo(pk), p1 = bfhi(pk);
            ta[j] = (u32)fminf(4.f * p0, p0 + 1536.f); ta[j + 1] = (u32)fminf(4.f * p1, p1 + 1536.f);
        }
        asm volatile("" : "+v"(ta[0]), "+v"(ta[1]), "+v"(ta[2]), "+v"(ta[3]), "+v"(ta[4]), "+v"(ta[5]), "+v"(ta[6]), "+v"(ta[7]));
        MARK(0);
        const float cr = bf2f(cr_), ci = bf2f(ci_);
        const float dt = rbf(__builtin_amdgcn_exp2f(bf2f(ld_) * 1.44269504088896340736f));
        const float zr = -rbf(__builtin_amdgcn_exp2f(bf2f(la_) * 1.44269504088896340736f)) * dt, zi = bf2f(ai_) * dt;
        float bs = fabsf(cr) + fabsf(ci) + fabsf(bf2f(cr2_)) + fabsf(bf2f(ci2_));
        bs += dppf<0xB1>(bs); bs += dppf<0x4E>(bs); bs += dppf<0x141>(bs); bs += dppf<0x140>(bs);
        bs += dppf<0x142, 0xa>(bs); bs += dppf<0x143, 0xc>(bs);
        bs = __builtin_bit_cast(float, __builtin_amdgcn_readlane(__builtin_bit_cast(int, bs), 63));
        const int se = 14 - expo(2.f * dt * bs * 1.0001f);
        const float scale = pow2i(se > 100 ? 100 : se);
        const float zr2 = zr * 1.44269504088896340736f;
        auto cexp = [&](float p) -> f32x2 {
            const float mag = __builtin_amdgcn_exp2f(zr2 * p);
            const float ang = zi * p;
            const float rr = __builtin_rintf(ang * 0.15915494309189535f);
            float fr = fmaf(ang, 0.15915494309189535f, -rr);
            fr = fmaf(ang, 6.4206382e-9f, fr);
            return (f32x2){mag * __builtin_amdgcn_cosf(fr), mag * __builtin_amdgcn_sinf(fr)};
        };
        const float p1 = grp ? 4.f : 1.f;
        const f32x2 f1 = cexp(p1), c1 = cexp(16.f * p1), s0 = cexp(grp ? 512.f : 0.f), s1 = cexp(256.f * p1);
        const f32x2 fw = {f1[0], -f1[1]}, fwp = {f1[1], f1[0]};
        const f32x2 cwp = {-c1[1], c1[0]};
        const float es = 2.f * dt * scale;
        f32x2 za = cmul((f32x2){es * cr, es * ci}, s0, (f32x2){-s0[1], s0[0]});
        f32x2 zb = cmul(za, s1, (f32x2){-s1[1], s1[0]});
        f32x2 zf = {1.f, 0.f};
        char* tab = smem + wv * 8704;
        u32 keep[16];
#pragma unroll
        for (int k = 0; k < 16; ++k) {
            *(u32*)(tab + k * 272 + lane * 4) = pkhf(zf[0], zf[1]);
            *(u32*)(tab + (16 + k) * 272 + lane * 4) = pkhf(za[0], za[1]);
            keep[k] = pkhf(zb[0], zb[1]);
            zf = cmul(zf, fw, fwp); za = cmul(za, c1, cwp); zb = cmul(zb, c1, cwp);
        }
        const int r16 = lane & 15, g = lane >> 4;
        f16x8 fb[4];
        f32x4 acc0 = {0.f, 0.f, 0.f, 0.f}, acc1 = {0.f, 0.f, 0.f, 0.f};
        __builtin_amdgcn_fence(__ATOMIC_RELEASE, "wavefront");
        __builtin_amdgcn_wave_barrier();
#pragma unroll
        for (int ks = 0; ks < 4; ++ks) {
            fb[ks] = *(const f16x8*)(tab + r16 * 272 + ks * 64 + g * 16);
            const f16x8 fa = *(const f16x8*)(tab + (16 + r16) * 272 + ks * 64 + g * 16);
            acc0 = __builtin_amdgcn_mfma_f32_16x16x32_f16(fb[ks], fa, acc0, 0, 0, 0);
        }
        __builtin_amdgcn_fence(__ATOMIC_RELEASE, "wavefront");
        __builtin_amdgcn_wave_barrier();
#pragma unroll
        for (int k = 0; k < 16; ++k) *(u32*)(tab + (16 + k) * 272 + lane * 4) = keep[k];
        __builtin_amdgcn_fence(__ATOMIC_RELEASE, "wavefront");
        __builtin_amdgcn_wave_barrier();
#pragma unroll
        for (int ks = 0; ks < 4; ++ks) {
            const f16x8 fa = *(const f16x8*)(tab + (16 + r16) * 272 + ks * 64 + g * 16);
            acc1 = __builtin_amdgcn_mfma_f32_16x16x32_f16(fb[ks], fa, acc1, 0, 0, 0);
        }
        MARK(1);
        __builtin_amdgcn_fence(__ATOMIC_RELEASE, "wavefront");
        __builtin_amdgcn_wave_barrier();
        *(f32x4*)(tab + r16 * 64 + g * 16) = acc0;
        *(f32x4*)(tab + 1024 + r16 * 64 + g * 16) = acc1;
        __syncthreads();
        float kv[8];
#pragma unroll
        for (int j = 0; j < 8; ++j) {
            const u32 o = ta[j] + (ta[j] >= 2048u ? 2u * 8704u - 2048u : 0u);
            kv[j] = *(const float*)(smem + o) + *(const float*)(smem + o + 8704);
        }
        MARK(2);
        st16(ws, WS_KR + d * 4096 + tid * 16, (u32x4){pkhf(kv[0], kv[1]), pkhf(kv[2], kv[3]), pkhf(kv[4], kv[5]), pkhf(kv[6], kv[7])});
        if (tid == 0) st4(ws, WS_KSC + d * 4, __builtin_bit_cast(u32, pow2i(se > 100 ? -100 : -se)));
    } else {
        b -= NB_KGEN;
        const u16* src = w2 + (size_t)b * 32 * DM;
        if (DELAY1 > 0) __builtin_amdgcn_s_sleep(DELAY1);
        const int tok = tid >> 3, sub = tid & 7;
        u32x4 v[9];
#pragma unroll
        for (int i = 0; i < 9; ++i) v[i] = *(const u32x4*)(src + tok * DM + (sub + 8 * i) * 8);
        MARK(0);
#pragma unroll
        for (int i = 0; i < 9; ++i) *(u32x4*)(smem + imgrow(tok) + (sub + 8 * i) * 16) = v[i];
        __syncthreads();
        const int kq = b / 36, kk = b - kq * 36, sp = kk / 9, c = kk - sp * 9;
        const int par = wv & 1, rel = (sp - wv) & 3;
        const int e = rel == 0 ? c : 9 + c * 3 + (rel - 1);
        const int h = lane >> 5, blk = (lane >> 4) & 1, qq = (lane & 15) >> 2, p = lane & 3;
#pragma unroll
        for (int i = 0; i < 9; ++i) {
            const int sg = (i + 1) >> 1, s2r = i == 0 ? 0 : (i - 1) & 1;
            const int T = 9 * (wv >> 1) + (sg == 0 ? 4 : 5 * par + ((kq + sg) & 3));
            const int m = sg == 4 ? 252 + 2 * e + s2r
                        : e < NA ? 7 * e + i
                        : sg == 0 ? 7 * NA + 2 * NB + (e - NA) : 7 * NA + (sg == 1 ? 0 : (2 * sg - 1) * NB) + (e - NA) * 2 + s2r;
            const int r0 = 16 * (s2r ^ par) + 4 * h + qq;
            const u32 col = T * 64 + blk * 32 + p * 8;
            st16(ws, WS_WF + ((kq * 4 + wv) * 648 + 324 + m) * 1024 + lane * 16, trread2(smem, imgrow(r0) + col, imgrow(r0 + 8) + col));
        }
    }
    asm volatile("k_prep_end:");
}

constexpr int NB_CONV = 576, NB_W1 = 144;
constexpr int CONV_LDS = 36864 + 4096;
constexpr int UCP = 4640, KB_OFF = 4 * UCP;

extern "C" __global__ __launch_bounds__(256, 4) void k_conv(const u16* __restrict__ dskip, const u16* __restrict__ w1,
                                              char* __restrict__ ws) {
    extern __shared__ __attribute__((aligned(16))) char smem[];
    const int tid = threadIdx.x, lane = tid & 63;
    const int wv = __builtin_amdgcn_readfirstlane(tid >> 6);
    RTSTAMP(1);
    if (blockIdx.x == 20 && threadIdx.x == 0) *(volatile unsigned long long*)(ws + 49152 + 1 * 8) = __builtin_amdgcn_s_memrealtime();
    const float* mod = (const float*)(ws + WS_MOD);
    int b = blockIdx.x;
    if (b >= NB_PF) b = b < NB_PF + NB_CONV ? b + NB_W1 : b - NB_CONV;
    if (b < NB_PF) {
        CODE_PREFETCH(k_mlp);
    } else if ((b -= NB_PF) < NB_W1) {
        const int q = b / 9, c = b - q * 9, f0 = b * 32;
        if (DELAY2 > 0) __builtin_amdgcn_s_sleep(DELAY2);
        const int seg = tid & 3, rg = tid >> 2;
        u32x4 v[9]; float sb[9], cb[9];
#pragma unroll
        for (int i = 0; i < 9; ++i) {
            const int row = rg + 64 * i;
            v[i] = *(const u32x4*)(w1 + (size_t)row * FF + f0 + seg * 8);
            sb[i] = mod[3 * DM + row]; cb[i] = mod[4 * DM + row];
        }
        MARK(0);
        float S[8], B[8];
#pragma unroll
        for (int k = 0; k < 8; ++k) { S[k] = 0.f; B[k] = 0.f; }
#pragma unroll
        for (int i = 0; i < 9; ++i) {
            const float c1 = rbf(1.f + cb[i]);
            u32x4 o;
#pragma unroll
            for (int k = 0; k < 4; ++k) {
                const float lo = bflo(v[i][k]), hi = bfhi(v[i][k]);
                o[k] = pkbf(lo * c1, hi * c1);
                S[2 * k] += bflo(o[k]); S[2 * k + 1] += bfhi(o[k]);
                B[2 * k] = fmaf(sb[i], lo, B[2 * k]); B[2 * k + 1] = fmaf(sb[i], hi, B[2 * k + 1]);
            }
            *(u32x4*)(smem + (tid + 256 * i) * 16) = o;
        }
#pragma unroll
        for (int k = 0; k < 8; ++k) {
            S[k] += dppf<0x114>(S[k]); S[k] += dppf<0x118>(S[k]);
            B[k] += dppf<0x114>(B[k]); B[k] += dppf<0x118>(B[k]);
        }
        float* red = (float*)(smem + 36864);
        if ((lane & 15) >= 12) {
            float* r = red + (tid >> 4) * 64 + seg * 8;
            *(f32x4*)(r) = (f32x4){S[0], S[1], S[2], S[3]}; *(f32x4*)(r + 4) = (f32x4){S[4], S[5], S[6], S[7]};
            *(f32x4*)(r + 32) = (f32x4){B[0], B[1], B[2], B[3]}; *(f32x4*)(r + 36) = (f32x4){B[4], B[5], B[6], B[7]};
        }
        __syncthreads();
        MARK(1);
        const int h = lane >> 5, blk = (lane >> 4) & 1, qq = (lane & 15) >> 2, p = lane & 3;
#pragma unroll
        for (int i = 0; i < 9; ++i) {
            const int s = wv * 9 + i;
            const u32 a = (16 * s + 8 * h + qq) * 64 + (blk ^ (q & 1)) * 32 + p * 8;
            const int sl = s - XROT * (q & 3) < 0 ? s - XROT * (q & 3) + 36 : s - XROT * (q & 3);
            st16(ws, WS_WF + (q * 648 + c * 36 + sl) * 1024 + lane * 16, trread2(smem, a, a + 256));
        }
        if (wv == 0) {
            float t = 0.f;
#pragma unroll
            for (int i = 0; i < 16; ++i) t += red[i * 64 + lane];
            const float B_ = __builtin_bit_cast(float, __builtin_amdgcn_ds_bpermute((lane ^ 32) * 4, __builtin_bit_cast(int, t)));
            const float S_ = -t;
            const float s1 = rbf(S_), s2 = rbf(S_ - s1), s3 = S_ - s1 - s2;
            const float b1 = rbf(B_), b2 = rbf(B_ - b1), b3 = B_ - b1 - b2;
            const bool lo = lane < 32;
            st16(ws, WS_EX + q * 10240 + c * 1024 + (lane ^ ((q & 1) << 4)) * 16,
                 (u32x4){lo ? pkbf(s1, s2) : 0u, lo ? pkbf(s3, b1) : 0u, lo ? pkbf(b2, b3) : 0u, 0u});
        } else if (wv == 1 && c == 0) {
            const int s = q & 3, kq = q >> 2;
            const int d = lane < 32 ? (9 * (s >> 1) + 5 * (s & 1) + kq) * 32 + lane : (9 * (s >> 1) + 4) * 32 + (lane - 32);
            st4(ws, WS_EX + q * 10240 + 9216 + lane * 4, __builtin_bit_cast(u32, mod[5 * DM + d]));
        }
    } else {
        const int d = b - NB_W1;
        const int tp = tid > 0 ? tid * 8 - 4 : 0;
        const char* xb = ws + WS_XT + d * 64;
        const u32x4 x0 = *(const u32x4*)(xb + (tid >> 2) * 36864 + (tid & 3) * 16);
        const u32x2 xm = *(const u32x2*)(xb + (tp >> 5) * 36864 + (tp & 31) * 2);
        u32x4 st[3];
        st[0] = *(const u32x4*)(ws + WS_STAT + tp * 4);
        st[1] = *(const u32x4*)(ws + WS_STAT + tid * 32);
        st[2] = *(const u32x4*)(ws + WS_STAT + tid * 32 + 16);
        const u32x4 kv = *(const u32x4*)(ws + WS_KR + d * 4096 + tid * 16);
        const float sa = mod[d], ca = mod[DM + d], ga = mod[2 * DM + d];
        const float dsk = bf2f(dskip[d]);
        const float kinv = ((const float*)(ws + WS_KSC))[d];
        {
            const u32x4 z = {0u, 0u, 0u, 0u};
            if (tid < 96) *(u32x4*)(smem + (tid / 24) * UCP + (tid % 24) * 16) = z;
            else if (tid < 104) *(u32x4*)(smem + KB_OFF + 4096 + (tid - 96) * 16) = z;
        }
        MARK(0);
        const float c1 = rbf(1.f + ca);
        const int he = expo(64.f * fabsf(c1) + 2.f * fabsf(sa)) - 14;
        const float hs = pow2i(-he), oscale = pow2i(he) * kinv;
        const u32 xw[6] = {xm[0], xm[1], x0[0], x0[1], x0[2], x0[3]};
        u32 P[6], hb[4];
#pragma unroll
        for (int k = 0; k < 6; ++k) {
            const u32 s0 = st[k >> 1][(k & 1) * 2], s1 = st[k >> 1][(k & 1) * 2 + 1];
            const float n0 = (bflo(xw[k]) - bflo(s0)) * __builtin_amdgcn_rcpf(bfhi(s0));
            const float n1 = (bfhi(xw[k]) - bflo(s1)) * __builtin_amdgcn_rcpf(bfhi(s1));
            const u32 t = pkbf(fmaf(n0, c1, sa), fmaf(n1, c1, sa));
            if (k >= 2) hb[k - 2] = t;
            P[k] = pkhf(bflo(t) * hs, bfhi(t) * hs);
        }
        if (tid == 0) { P[0] = 0u; P[1] = 0u; }
        u32 Q[5];
#pragma unroll
        for (int k = 0; k < 5; ++k) Q[k] = __builtin_amdgcn_alignbit(P[k + 1], P[k], 16);
        {
            char* ub = smem + 384 + tid * 16;
            *(u32x4*)(ub) = (u32x4){Q[0], Q[1], Q[2], Q[3]};
            *(u32x4*)(ub + UCP) = (u32x4){P[1], P[2], P[3], P[4]};
            *(u32x4*)(ub + 2 * UCP) = (u32x4){Q[1], Q[2], Q[3], Q[4]};
            *(u32x4*)(ub + 3 * UCP) = (u32x4){P[2], P[3], P[4], P[5]};
            *(u32x4*)(smem + KB_OFF + tid * 16) = kv;
        }
        __syncthreads();
        MARK(1);
        const int c = lane & 15, g = lane >> 4;
        f32x4 acc[8];
#pragma unroll
        for (int j = 0; j < 8; ++j) acc[j] = (f32x4){0.f, 0.f, 0.f, 0.f};
#pragma unroll
        for (int rr = 0; rr < 2; ++rr) {
            const int r = 2 * wv + rr;
            const char* kb = smem + KB_OFF + 2 * (2076 - 4 * c + 8 * g - 32 * r);
            const char* ub = smem + (c & 3) * UCP + 2 * (8 * g + 64 * (c >> 2) + 32 * (1 - r) + 192);
            f16x8 kf[8], uf[8];
#pragma unroll
            for (int i = 0; i < 8; ++i) {
                const u32x2 a = *(const u32x2*)(kb - i * 512), a2 = *(const u32x2*)(kb - i * 512 + 8);
                kf[i] = __builtin_bit_cast(f16x8, (u32x4){a[0], a[1], a2[0], a2[1]});
                uf[i] = *(const f16x8*)(ub + i * 512);
            }
#pragma unroll
            for (int i = 0; i < 8; ++i)
#pragma unroll
                for (int dl = 0; dl + i < 8; ++dl)
                    acc[i + dl] = __builtin_amdgcn_mfma_f32_16x16x32_f16(uf[dl], kf[i], acc[i + dl], 0, 0, 0);
        }
        __syncthreads();
        MARK(2);
#pragma unroll
        for (int j = 0; j < 8; ++j) *(f32x4*)(smem + wv * 8192 + j * 1024 + lane * 16) = acc[j];
        __syncthreads();
        f32x4 y0 = *(const f32x4*)(smem + tid * 32), y1 = *(const f32x4*)(smem + tid * 32 + 16);
#pragma unroll
        for (int w = 1; w < 4; ++w) {
            y0 += *(const f32x4*)(smem + w * 8192 + tid * 32);
            y1 += *(const f32x4*)(smem + w * 8192 + tid * 32 + 16);
        }
        const float y[8] = {y0[0], y0[1], y0[2], y0[3], y1[0], y1[1], y1[2], y1[3]};
        u32x4 o;
#pragma unroll
        for (int k = 0; k < 4; ++k) {
            const u32 yb = pkbf(y[2 * k] * oscale, y[2 * k + 1] * oscale);
            const u32 sk = pkbf(bflo(hb[k]) * dsk, bfhi(hb[k]) * dsk);
            const u32 z = pkbf(bflo(yb) + bflo(sk), bfhi(yb) + bfhi(sk));
            const u32 gz = pkbf(bflo(z) * ga, bfhi(z) * ga);
            o[k] = pkbf(bflo(x0[k]) + bflo(gz), bfhi(x0[k]) + bfhi(gz));
        }
        MARK(3);
        st16(ws, WS_X1F + (tid >> 4) * 147456 + ((d / 48) * 12 + ((tid >> 2) & 3) * 3 + (d >> 4) % 3) * 1024 + (d & 15) * 64 + (tid & 3) * 16, o);
    }
    asm volatile("k_conv_end:");
}
constexpr u32 L_EXF = 147456;
constexpr int MLP_LDS = L_EXF + 12288;
constexpr int GE = 648;

#if defined(__HIP_DEVICE_COMPILE__)
#pragma clang attribute push(__attribute__((target("no-packed-fp32-ops"))), apply_to = function)
#endif
template <int N> struct IC { static constexpr int value = N; };
template <int A, int B, class F> DI void static_for(F&& f) {
    if constexpr (B - A == 1) f(IC<A>{});
    else if constexpr (A < B) { static_for<A, (A + B) / 2>(f); static_for<(A + B) / 2, B>(f); }
}
constexpr int imin(int a, int b) { return a < b ? a : b; }
constexpr int XW = 6;
#define XSP 2
constexpr int XI = 7;
constexpr int x_dmas(int j) { return j <= 0 ? 0 : imin((j + XSP - 1) / XSP, 3 * (19 - XI)); }
constexpr int G1 = 36;
#define PLG 614
constexpr int P1 = 36 * 5 + 7, P2 = 36 * 7 + 7, P3 = 400;
#define PREF 0
#define CD1 34
#define CD3 30
#define QW3 8
constexpr int C1 = P1 + CD1, C2 = P2 + CD1, C3 = P3 + CD3;
constexpr bool s_poll(int G) { return G == P1 || G == P2 || G == P3; }
#define PB2 606
#define PL4 640
constexpr int PUB2 = PB2, P4 = PL4;
constexpr int HS0 = 22, HS1 = 35;
constexpr int s_hstore(int G) { return G < 72 ? (G == 66 ? 0 : G == 71 ? 1 : -1) : G < 324 ? (G % 36 == HS0 ? 0 : G % 36 == HS1 ? 1 : -1) : G == 338 ? 0 : G == 346 ? 1 : -1; }
constexpr int B0 = 324 + 7 * NA;
constexpr int slot_end(int sg) { return B0 - 1 + (sg == 1 ? 2 : sg == 0 ? 3 : 2 * sg + 1) * NB; }
constexpr int s_pstore(int G) {
    for (int sg = 0; sg < 4; ++sg) { if (G == slot_end(sg) + 4) return 2 * sg; if (G == slot_end(sg) + 6) return 2 * sg + 1; }
    return -1;
}
constexpr bool s_res(int G) { return G == 620 || G == 622 || G == 624; }
constexpr int s_pub(int G) { return G == 36 * 3 + 24 ? 2 : G == 36 * 5 + 24 ? 4 : G == 372 ? 9 : 0; }
constexpr int m_slot(int m) { return m < 7 * NA ? (m % 7 + 1) / 2 : m < 7 * NA + 2 * NB ? 1 : m < 7 * NA + 3 * NB ? 0 : m < 252 ? 2 + (m - 7 * NA - 3 * NB) / (2 * NB) : 4; }
constexpr int m_ent(int m) { return m < 7 * NA ? m / 7 : m < 7 * NA + 2 * NB ? NA + (m - 7 * NA) / 2 : m < 7 * NA + 3 * NB ? NA + (m - 7 * NA - 2 * NB)
                                  : m < 252 ? NA + ((m - 7 * NA - 3 * NB) % (2 * NB)) / 2 : (m - 252) / 2; }
constexpr int m_half(int m) { return m < 7 * NA ? (m % 7 == 0 ? 0 : (m % 7 - 1) & 1) : m < 7 * NA + 2 * NB ? (m - 7 * NA) & 1 : m < 7 * NA + 3 * NB ? 0
                                   : m < 252 ? (m - 7 * NA - 3 * NB) & 1 : m & 1; }
constexpr bool m_first(int m) { return m == 0 || m == 1 || m == 3 || m == 5 || m == 252; }
constexpr bool ent_first(int m) { return m < 7 * NA ? m % 7 == 0 : (m < 7 * NA + 2 * NB && ((m - 7 * NA) & 1) == 0); }
constexpr bool ent_own(int e) { return e < 9; }
constexpr int first_use(int e) { return e < NA ? 324 + 7 * e : B0 + 2 * (e - NA); }
constexpr bool s_ring(int G) { return G + 12 < GE && (G < 100 ? G % 12 == 0 : G < 490 ? G % 36 == 24 : G % 18 == 6); }
constexpr int ring_slot(int grp) { const int u = (grp / 3) & 3; return (u == 0 ? 0 : u == 1 ? 6 : u == 2 ? 9 : 3) + grp % 3; }
constexpr int need_bar(int grp) { int g = 12 * grp - 12; while (g > 0 && !s_ring(g)) --g; return g; }
struct Sched {
    int dn[GE];
    int dgap[162];
    int hl[GE];
    int pre[GE + 1];
    int hgap[36];
    bool ok;
    constexpr Sched() : dn(), dgap(), hl(), pre(), hgap(), ok(true) {
        int nd = 3 * XW, nh = 0, last2 = -99, lastbar = 24;
        for (int n = 0; n < 162; ++n) dgap[n] = -1;
        for (int e = 0; e < 36; ++e) hgap[e] = -1;
        for (int G = 0; G < GE; ++G) {
            dn[G] = -1; hl[G] = -1;
            int n = 0;
            if (G >= G1) {
                if (s_ring(G)) lastbar = G;
                const bool fixed = s_hstore(G) >= 0 || s_pstore(G) >= 0 || s_res(G);
                const bool quiet = (G >= P1 && G < P1 + 17) || (G >= P2 && G < P2 + 17) || (G >= P3 && G < P3 + QW3);
                const int sp2 = G < 324 ? 4 : 5;
                if (fixed) last2 = 2 * G;
                else if (!quiet && 2 * G - last2 >= sp2) {
                    int dd = 1 << 20, dh = 1 << 20;
                    if (nd < 162 && (nd / 3 < 12 || (nd / 3 - 11) * 12 <= lastbar + 6)) dd = need_bar(nd / 3) - 40;
                    const int lim = G > C3 ? 54 : G > C2 ? 24 : G > C1 ? 12 : 0;
                    if (nh < lim) dh = first_use(9 + nh / 2) - 45;
                    if (dd < (1 << 20) || dh < (1 << 20)) {
                        if (dd <= dh) { dn[G] = nd; dgap[nd] = G; if (need_bar(nd / 3) <= G) ok = false; ++nd; }
                        else { hl[G] = 18 + nh; hgap[9 + nh / 2] = G; if (first_use(9 + nh / 2) <= G) ok = false; ++nh; }
                        last2 = last2 + sp2 > 2 * G - 1 ? last2 + sp2 : 2 * G - (G < 324 ? 0 : 1);
                        n = 1;
                    }
                }
                n += fixed + s_poll(G) + (G == P4) + (s_pub(G) > 0) + (G == PUB2);
            }
            pre[G + 1] = pre[G] + n;
        }
        if (nd != 162 || nh != 54) ok = false;
    }
};
constexpr Sched SCH{};
static_assert(SCH.ok, "schedule misses a deadline");
constexpr int younger(int a, int b) { return imin(SCH.pre[b] - SCH.pre[a + 1], 63); }
constexpr bool s_bar(int G) { return G >= G1 && (s_ring(G) || s_pub(G) > 0 || G == PUB2); }
constexpr int bar_wait(int G) {
    int n = 63;
    if (s_ring(G)) { int grp = 53; while (need_bar(grp) > G) --grp; n = G == 36 ? 3 : younger(SCH.dgap[3 * grp + 2], G); }
    if (G == PUB2) n = imin(n, younger(slot_end(3) + 6, G));
    if (s_pub(G) > 0) n = imin(n, younger(G == 372 ? 346 : 36 * s_pub(G) + HS1, G));
    return n;
}
constexpr int frag_lds(int f) { return ring_slot(f / 12) * 12288 + (f % 12) * 1024; }

constexpr u32 RESL = ring_slot(45) * 12288;
static_assert(SCH.dgap[161] < 620, "M0");
#define SB() __builtin_amdgcn_sched_barrier(0)
#define STAMP(i) do {} while (0)
#define TW(code) do { code; } while (0)
#define MFMA "v_mfma_f32_32x32x16_bf16 "

extern "C" __global__ __launch_bounds__(256, 1) void k_mlp(char* __restrict__ ws, u16* __restrict__ out) {
    extern __shared__ __attribute__((aligned(16))) char smem[];
    if (blockIdx.x == 20 && threadIdx.x == 0) *(volatile unsigned long long*)(ws + 49152 + 2 * 8) = __builtin_amdgcn_s_memrealtime();
    const int tid = threadIdx.x, lane = tid & 63;
    const int w = __builtin_amdgcn_readfirstlane(tid >> 6);
    const int b = blockIdx.x, xcd = b & 7, rr = b >> 3;
    const int kq = xcd >> 1, it = 8 * (xcd & 1) + (rr >> 2), s = rr & 3, q = 4 * kq + s, par = s & 1;
    const auto rs = __builtin_amdgcn_make_buffer_rsrc(ws, 0, WS_END, 0x00020000);
    const u32x4 srd = {(u32)(uintptr_t)ws, (u32)((uintptr_t)ws >> 32) & 0xffffu, WS_END, 0x00020000u};
    const u32 voff = lane * 16;
    const u32 lbase = (u32)(uintptr_t)(LAS char*)smem;
    const u32 wl = lbase + w * 3072;
    auto set_m0 = [&](u32 v) { asm volatile("s_mov_b32 m0, %0" :: "s"(v) : "memory"); };
    auto dma = [&](auto O, u32 so) {
        const u32 vo = voff; const u32x4 sr = srd;
        asm volatile("buffer_load_dwordx4 %0, %1, %2 offen offset:%3 lds" :: "v"(vo), "s"(sr), "s"(so), "n"(O.value * 1024) : "memory");
    };
    auto dma3 = [&](u32 dst, u32 so) {
        asm volatile("s_mov_b32 m0, %3\n\ts_nop 0\n\tbuffer_load_dwordx4 %0, %1, %2 offen lds\n\t"
                     "buffer_load_dwordx4 %0, %1, %2 offen offset:1024 lds\n\tbuffer_load_dwordx4 %0, %1, %2 offen offset:2048 lds"
                     :: "v"(voff), "s"(srd), "s"(so), "s"(dst) : "memory");
    };
    const u32 xsb = WS_X1F + it * 147456;
    const u32 wfb = WS_WF + q * 663552 + w * 3072;
    auto rq_dst = [&](int p) -> u32 {
        const int x = p <= 2 ? p - 1 : p <= 6 ? p - 2 : p - 3;
        return wl + (p == 0 ? 0 : p == 3 ? 12288 : p == 7 ? 24576 : p == 15 ? L_EXF : p > 15 ? ring_slot(p - 13) * 12288 : (3 + x % 9) * 12288);
    };
    auto rq_src = [&](int p) -> u32 {
        const int x = p <= 2 ? p - 1 : p <= 6 ? p - 2 : p - 3;
        return p == 0 ? wfb : p == 3 ? wfb + 12288 : p == 7 ? wfb + 24576 : p == 15 ? WS_EX + q * 10240 + w * 3072
             : p > 15 ? wfb + (p - 13) * 12288 : xsb + w * 3072 + x * 12288;
    };
#pragma unroll
    for (int p = 0; p < XI; ++p) dma3(rq_dst(p), rq_src(p));
    set_m0(rq_dst(XI));
    const u32 hfb = WS_HF + (it * 4 + kq) * 36 * 8192 + w * 2048;
    u32x4 Wf[18], Ef;
    const u32 rd0 = lbase + voff;
    const u32 rb[3] = {rd0, rd0 + 49152, rd0 + 98304};
    auto rdw = [&](auto F) {
        constexpr int lo = frag_lds(F.value);
        u32x4& d = Wf[F.value % 18]; const u32 base = rb[lo / 49152];
        asm volatile("ds_read_b128 %0, %1 offset:%2" : "=v"(d) : "v"(base), "n"(lo % 49152) : "memory");
    };
    auto rde = [&](auto C) {
        u32x4& d = Ef; const u32 base = rb[2];
        asm volatile("ds_read_b128 %0, %1 offset:%2" : "=v"(d) : "v"(base), "n"(L_EXF - 98304 + C.value * 1024) : "memory");
    };
    f32x16 acc[2];

    u32x4 Xa[36];
    f32x16 gram, rsum;
    {
        const u32 a0 = wl + (8 * (lane >> 5) + ((lane & 15) >> 2)) * 64 + ((lane >> 4) & 1) * 32 + (lane & 3) * 8;
        u32x4 ones = {0x3f803f80u, 0x3f803f80u, 0x3f803f80u, 0x3f803f80u};
        asm volatile("" : "+v"(ones));
        u32x2 t[2][6];
        auto step = [&](auto A) {
            constexpr int a = A.value;
            constexpr int pos = a <= 1 ? a + 1 : a <= 4 ? a + 2 : a + 3;
            constexpr int issued = 3 * XI + x_dmas(9 * a - 3);
            static_assert(issued >= 3 * pos + 3, "X order");
            u32x2 (&d)[6] = t[a & 1];
            const u32 ad = a0 + (3 + a % 9) * 12288;
            if constexpr (a % 4 == 0) asm volatile("s_waitcnt vmcnt(%0)\n\ts_barrier" :: "n"(issued - 3 * pos - 3) : "memory");
            else asm volatile("s_waitcnt vmcnt(%0)" :: "n"(issued - 3 * pos - 3) : "memory");
            asm volatile("ds_read_b64_tr_b16 %0, %6\n\tds_read_b64_tr_b16 %1, %6 offset:256\n\t"
                         "ds_read_b64_tr_b16 %2, %6 offset:1024\n\tds_read_b64_tr_b16 %3, %6 offset:1280\n\t"
                         "ds_read_b64_tr_b16 %4, %6 offset:2048\n\tds_read_b64_tr_b16 %5, %6 offset:2304"
                         : "=&v"(d[0]), "=&v"(d[1]), "=&v"(d[2]), "=&v"(d[3]), "=&v"(d[4]), "=&v"(d[5]) : "v"(ad) : "memory");
            rdw(IC<3 * a>{}); rdw(IC<3 * a + 1>{}); rdw(IC<3 * a + 2>{});
        };
        step(IC<0>{});
        STAMP(1);
        u32x4 xv;
        static_for<0, 108>([&](auto J) {
            constexpr int j = J.value, a = j / 9, z = (j % 9) / 3, k = j % 3;
            u32x2 (&d)[6] = t[a & 1];
            if constexpr (j % 9 == 0)
                asm volatile("s_waitcnt lgkmcnt(0)" : "+v"(d[0]), "+v"(d[1]), "+v"(d[2]), "+v"(d[3]), "+v"(d[4]), "+v"(d[5]) :: "memory");
            if constexpr (j % 9 == 6 && a < 11) step(IC<a + 1>{});
            u32x4& x = xv; u32x4& wf = Wf[(3 * a + z) % 18]; u32x4& on = ones;
            f32x16& ac = k == 0 ? acc[0] : k == 1 ? gram : rsum;
            if constexpr (k == 0) { xv = (u32x4){d[2 * z][0], d[2 * z][1], d[2 * z + 1][0], d[2 * z + 1][1]}; asm volatile("s_nop 1" : "+v"(x)); }
            u32x4& am = k == 0 ? wf : k == 1 ? x : on;
            if constexpr (j < 3) asm volatile(MFMA "%0, %1, %2, 0" : "=&v"(ac) : "v"(am), "v"(x));
            else asm volatile(MFMA "%0, %1, %2, %0" : "+v"(ac) : "v"(am), "v"(x));
            SB();
            if constexpr (k == 0) { Xa[3 * a + z] = xv; asm volatile("" : "+a"(Xa[3 * a + z])); }
            if constexpr (x_dmas(j + 1) > x_dmas(j)) {
                constexpr int n = x_dmas(j), p = XI + n / 3;
                static_assert(p < 12 || p == 15 || 9 * (p < 15 ? p - 12 : p - 13) <= j - (j % 9), "slot still holds unread x");
                dma(IC<n % 3>{}, rq_src(p));
                if constexpr (n % 3 == 2 && p < 18) set_m0(rq_dst(p + 1));
            }
            SB();
        });
    }
    float ysc;
    u32x4 Xe;
    {
        asm volatile("s_waitcnt vmcnt(6)\n\ts_barrier" ::: "memory");
        STAMP(2);
        static_for<36, 48>(rdw);
        rde(IC<0>{});
        set_m0(wl + ring_slot(XW) * 12288);
        SB();
    }
    float sq, mean;
    auto stats = [&](auto ST) {
        constexpr int st = ST.value;
        const int sel = (lane & 3) + 4 * ((lane & 31) >> 3);
        if constexpr (st == 0) sq = gram[0];
        if constexpr (st < 5) {
#pragma unroll
            for (int i = 1 + 3 * st; i < 4 + 3 * st; ++i) sq = sel == i ? gram[i] : sq;
            asm volatile("" : "+v"(sq));
        }
        if constexpr (st == 5) {
            const auto r = __builtin_amdgcn_permlane32_swap(__builtin_bit_cast(u32, sq), __builtin_bit_cast(u32, sq), false, false);
            const u32 r0 = r[0], r1 = r[1];
            sq = __builtin_bit_cast(float, (lane & 4) ? r1 : r0);
            mean = rsum[0] * (1.f / DM);
            asm volatile("" : "+v"(sq), "+v"(mean));
        }
        if constexpr (st == 6) { sq = rbf(rbf(fmaxf(fmaf(-mean, mean, sq * (1.f / DM)), 0.f)) + 1e-5f); asm volatile("" : "+v"(sq)); }
        if constexpr (st == 7) { sq = rbf(__builtin_amdgcn_sqrtf(sq)); ysc = -1.44269504088896340736f * __builtin_amdgcn_rcpf(sq); asm volatile("" : "+v"(sq), "+v"(ysc)); }
        if constexpr (st == 8) {
            const bool lo = lane < 32;
            Xe = (u32x4){lo ? pkbf(mean, mean) : 0u, lo ? pkbf(mean, sq) : 0u, lo ? pkbf(sq, sq) : 0u, 0u};
            asm volatile("" : "+v"(Xe));
        }
    };

    f32x16 acc2[5];
    float yv[16], ev[16];
    u32x4 H[36][2], pollv;
    auto flag_store = [&](u32 level, u32 so) {
        asm volatile("s_mov_b64 exec, 1\n\tbuffer_store_dword %0, off, %1, %2 sc1\n\ts_mov_b64 exec, -1" :: "v"(level), "s"(srd), "s"(so) : "memory");
    };
    auto act = [&](auto C, auto HS) {
        constexpr int t = HS.value / 2, e = C.value;
        if constexpr (HS.value % 2 == 0) {
            if constexpr (t < 16) { yv[t] = acc[C.value & 1][t] * ysc; asm volatile("" : "+v"(yv[t])); }
            if constexpr (t >= 2) { yv[t - 2] = yv[t - 2] * ev[t - 2]; asm volatile("" : "+v"(yv[t - 2])); }
            if constexpr (t < 16) { ev[t] = __builtin_amdgcn_exp2f(yv[t]); asm volatile("" : "+v"(ev[t])); }
        } else {
            if constexpr (t >= 1 && t < 17) { ev[t - 1] = 1.f + ev[t - 1]; asm volatile("" : "+v"(ev[t - 1])); }
            if constexpr (t >= 3 && (t & 1)) {
                u32 pk = pkbf(yv[t - 3], yv[t - 2]);
                asm volatile("" : "+v"(pk));
                H[e][(t - 3) / 8][((t - 3) / 2) & 3] = pk;
            }
            if constexpr (t >= 1 && t < 17) { ev[t - 1] = __builtin_amdgcn_rcpf(ev[t - 1]); asm volatile("" : "+v"(ev[t - 1])); }
        }
    };
    const u32 vh0 = voff + par * 1024, vh1 = voff + (par ^ 1) * 1024;
    const u32 hown = hfb + 9 * s * 8192;
    const u32 hpeer[3] = {hfb + 9 * ((s + 1) & 3) * 8192, hfb + 9 * ((s + 2) & 3) * 8192, hfb + 9 * ((s + 3) & 3) * 8192};
    auto hload = [&](auto E, auto HF) {
        constexpr int e = E.value, hf = HF.value, k = e - 9;
        const u32 so = hpeer[k % 3] + (k / 3) * 8192, vo = hf ? vh1 : vh0; const u32x4 sr = srd;
        u32x4& d = H[e][hf];
        asm volatile("buffer_load_dwordx4 %0, %1, %2, %3 offen sc1" : "=a"(d) : "v"(vo), "s"(sr), "s"(so) : "memory");
    };
    const u32 vzero = 0;
    const u32 fso1 = WS_FLAG + (it * 4 + kq) * 64;
    auto poll = [&]() {
        asm volatile("buffer_load_dwordx4 %0, %1, %2, %3 offen sc1" : "=v"(pollv) : "v"(vzero), "s"(srd), "s"(fso1) : "memory");
    };
    auto check = [&](u32 level) {
        asm volatile("" : "+v"(pollv));
#pragma nounroll
        for (u32 spins = 0; spins < (1u << 20); ++spins) {
            const bool bad = pollv[0] < level || pollv[1] < level || pollv[2] < level || pollv[3] < level;
            if (__builtin_amdgcn_ballot_w64(bad) == 0ull) break;
            __builtin_amdgcn_s_sleep(1);
            asm volatile("buffer_load_dwordx4 %0, %1, %2, %3 offen sc1\n\ts_waitcnt vmcnt(0)"
                         : "=v"(pollv) : "v"(vzero), "s"(srd), "s"(fso1) : "memory");
        }
    };
    const u32 pgb = WS_PART + (it * 4 + s) * 98304 + w * 2048;
    const u32 phb = WS_PH + (it * 2 + (s >> 1)) * 65536 + w * 16384;
    const int Town = 9 * (s >> 1) + 5 * par + kq, Thalf = 9 * (s >> 1) + 4;
    auto xfrag = [&](int ks) -> u32 { return xsb + ((ks / 3) * 12 + 3 * w + ks % 3) * 1024; };
    const u32 fso2 = WS_FLAG + 4096 + (it * 2 + (s >> 1)) * 64;
    u32 fv;
    const u32 vfl = (lane & 7) * 4;
    auto poll2 = [&]() {
        asm volatile("buffer_load_dword %0, %1, %2, %3 offen sc1" : "=v"(fv) : "v"(vfl), "s"(srd), "s"(fso2) : "memory");
    };
    auto check2 = [&]() {
        asm volatile("" : "+v"(fv));
#pragma nounroll
        for (u32 spins = 0; spins < (1u << 20); ++spins) {
            if (__builtin_amdgcn_ballot_w64(fv == 0u) == 0ull) break;
            __builtin_amdgcn_s_sleep(1);
            asm volatile("buffer_load_dword %0, %1, %2, %3 offen sc1\n\ts_waitcnt vmcnt(0)" : "=v"(fv) : "v"(vfl), "s"(srd), "s"(fso2) : "memory");
        }
    };
    u32x4 P[6];
    u32 Q[8];
    const u32 vq = voff + ((kq & 1) * 2 + par) * 4;
    const u32 pin = pgb + kq * 24576, qin = phb + (kq >> 1) * 8192;
    auto pload = [&](auto I) {
        constexpr int i = I.value;
        const u32x4 sr = srd;
        if constexpr (i < 4) {
            u32& d0 = Q[2 * i]; u32& d1 = Q[2 * i + 1]; const u32 vo = vq, so = qin + (i >> 1) * 4096;
            asm volatile("buffer_load_dword %0, %2, %3, %4 offen offset:%5 sc1\n\tbuffer_load_dword %1, %2, %3, %4 offen offset:%6 sc1"
                         : "=&v"(d0), "=&v"(d1) : "v"(vo), "s"(sr), "s"(so), "n"((i & 1) * 2048), "n"((i & 1) * 2048 + 1024) : "memory");
        } else {
            u32x4& d = P[i - 4]; const u32 vo = voff, so = pin + ((i - 4) >> 1) * 8192;
            asm volatile("buffer_load_dwordx4 %0, %1, %2, %3 offen offset:%4 sc1" : "=v"(d) : "v"(vo), "s"(sr), "s"(so), "n"(((i - 4) & 1) * 1024) : "memory");
        }
    };
    auto pstore = [&](int sg, int pt, u32x4 v) {
        if (sg == 0) __builtin_amdgcn_raw_buffer_store_b128(v, rs, voff, phb + pt * 8192 + (2 * kq + par) * 1024, 16);
        else __builtin_amdgcn_raw_buffer_store_b128(v, rs, voff + pt * 1024, pgb + ((kq + sg) & 3) * 24576 + (sg - 1) * 8192, 16);
    };

    static_for<G1, GE>([&](auto GG) {
        constexpr int G = GG.value, c = G / 36, g = G % 36, m = G - 324;
        if constexpr (g == 0 && G > 0) STAMP(2 + G / 36);
        if constexpr (s_bar(G)) {
            if constexpr (G % 6 == 0) TW(asm volatile("s_waitcnt vmcnt(%0) lgkmcnt(%1)\n\ts_barrier" :: "n"(bar_wait(G)), "n"(G == G1 ? 7 : 6) : "memory"));
            else TW(asm volatile("s_waitcnt vmcnt(%0)\n\ts_barrier" :: "n"(bar_wait(G)) : "memory"));
        } else if constexpr (G % 6 == 0) asm volatile("s_waitcnt lgkmcnt(%0)" :: "n"(G == G1 ? 7 : G + 6 < GE ? 6 : 0) : "memory");
        u32x4& wf = Wf[G % 18];
        if constexpr (G < 324) {
            f32x16& ac = acc[c & 1]; u32x4& xa = Xa[g];
            if constexpr (g == 0) asm volatile(MFMA "%0, %1, %2, 0" : "=&v"(ac) : "v"(wf), "a"(xa));
            else asm volatile(MFMA "%0, %1, %2, %0" : "+v"(ac) : "v"(wf), "a"(xa));
            if constexpr (g == (c == 1 ? 30 : 12)) {
                u32x4& ef = Ef; u32x4& xe = Xe;
                SB();
                if constexpr (c > 1) act(IC<c - 1>{}, IC<11>{});
                SB();
                asm volatile(MFMA "%0, %1, %2, %0" : "+v"(ac) : "v"(ef), "v"(xe));
            }
            if constexpr (G == 46) { u32x4& ef = Ef; u32x4& xe = Xe; f32x16& a0 = acc[0]; SB(); asm volatile(MFMA "%0, %1, %2, %0" : "+v"(a0) : "v"(ef), "v"(xe)); }
        } else {
            constexpr int sl = m_slot(m), e = m_ent(m), hf = m_half(m);
            if constexpr (ent_first(m) && !ent_own(e))
                TW(asm volatile("s_waitcnt vmcnt(%0)" :: "n"(younger(SCH.hgap[e], G)) : "memory"));
            f32x16& ac = acc2[sl]; u32x4& hh = H[e][hf];
            if constexpr (m_first(m)) asm volatile(MFMA "%0, %1, %2, 0" : "=&v"(ac) : "v"(wf), "v"(hh));
            else if constexpr (ent_own(e)) asm volatile(MFMA "%0, %1, %2, %0" : "+v"(ac) : "v"(wf), "v"(hh));
            else asm volatile(MFMA "%0, %1, %2, %0" : "+v"(ac) : "v"(wf), "a"(hh));
        }
        SB();
        if constexpr (G + 12 < GE) rdw(IC<G + 12>{});
        if constexpr (G == 48) rde(IC<1>{});
        if constexpr (g == (c == 1 ? 32 : 14) && c < 8) rde(IC<c + 1>{});
        if constexpr (SCH.dn[G] >= 0) {
            dma(IC<SCH.dn[G] % 3>{}, wfb + (SCH.dn[G] / 3) * 12288);
            if constexpr (SCH.dn[G] % 3 == 2 && SCH.dn[G] < 161) set_m0(wl + ring_slot(SCH.dn[G] / 3 + 1) * 12288);
        }
        if constexpr (G >= 36 && G < 45) stats(IC<G - 36>{});
        if constexpr (G >= 48 && G < 72) {
            act(IC<0>{}, IC<(3 * (G - 48) + 1) / 2>{});
            if constexpr (((G - 48) & 1) == 0) act(IC<0>{}, IC<(3 * (G - 48)) / 2 + 1>{});
        }
        if constexpr (G >= 72 && G < 324 && g >= 1) act(IC<c - 1>{}, IC<(g < 12 ? g - 1 : g)>{});
        if constexpr (G >= 326 && G < 344) { act(IC<8>{}, IC<2 * (G - 326)>{}); act(IC<8>{}, IC<2 * (G - 326) + 1>{}); }
        if constexpr (s_hstore(G) >= 0) {
            constexpr int hf = s_hstore(G), cc = c - 1;
            __builtin_amdgcn_raw_buffer_store_b128(H[cc][hf], rs, hf ? vh1 : vh0, hown + cc * 8192, 16);
        }
        if constexpr (s_pub(G) > 0) flag_store(s_pub(G), fso1 + s * 4);
        if constexpr (G == PUB2) flag_store(1u, fso2 + (2 * kq + par) * 4);
        if constexpr (G == C1 || G == C2 || G == C3) {
            TW(asm volatile("s_waitcnt vmcnt(%0)" :: "n"(younger(G == C1 ? P1 : G == C2 ? P2 : P3, G)) : "memory"));
            check(G == C1 ? 2 : G == C2 ? 4 : 9);
        }
        if constexpr (G == P1 || G == P2 || G == P3) poll();
        if constexpr (G == P4) poll2();
        if constexpr (SCH.hl[G] >= 0) hload(IC<SCH.hl[G] / 2>{}, IC<SCH.hl[G] % 2>{});
        if constexpr (s_pstore(G) >= 0) {
            constexpr int sg = s_pstore(G) / 2, pt = s_pstore(G) % 2;
            const f32x16& a = acc2[sg];
            const u32x4 v = {pkbf(a[8 * pt], a[8 * pt + 1]), pkbf(a[8 * pt + 2], a[8 * pt + 3]), pkbf(a[8 * pt + 4], a[8 * pt + 5]), pkbf(a[8 * pt + 6], a[8 * pt + 7])};
            pstore(sg, pt, v);
        }
        if constexpr (s_res(G)) {
            constexpr int i = (G - 620) / 2;
            set_m0(wl + RESL + i * 1024);
            const u32 so = xfrag(i < 2 ? 2 * Town + i : 2 * Thalf + (kq >> 1));
            asm volatile("s_nop 0" ::: "memory");
            dma(IC<0>{}, so);
        }
        SB();
    });
    STAMP(20);

    {
        u32x2 xr[5];
        f32x4 gb[4];
        f32x2 gh;
        const u32 ra = wl + RESL + (4 * (lane >> 5) + ((lane & 15) >> 2)) * 64 + ((lane >> 4) & 1) * 32 + (lane & 3) * 8;
        const u32 ga = lbase + L_EXF + 9216 + (lane >> 5) * 16;
        asm volatile("s_waitcnt vmcnt(1)" ::: "memory");
        asm volatile("ds_read_b64_tr_b16 %0, %5\n\tds_read_b64_tr_b16 %1, %5 offset:512\n\tds_read_b64_tr_b16 %2, %5 offset:1024\n\t"
                     "ds_read_b64_tr_b16 %3, %5 offset:1536\n\tds_read_b64_tr_b16 %4, %6 offset:2048"
                     : "=&v"(xr[0]), "=&v"(xr[1]), "=&v"(xr[2]), "=&v"(xr[3]), "=&v"(xr[4]) : "v"(ra), "v"(ra + (kq & 1) * 512) : "memory");
        asm volatile("ds_read_b128 %0, %5\n\tds_read_b128 %1, %5 offset:32\n\tds_read_b128 %2, %5 offset:64\n\tds_read_b128 %3, %5 offset:96\n\t"
                     "ds_read_b64 %4, %6 offset:128\n\ts_waitcnt vmcnt(0) lgkmcnt(0)"
                     : "=&v"(gb[0]), "=&v"(gb[1]), "=&v"(gb[2]), "=&v"(gb[3]), "=&v"(gh)
                     : "v"(ga), "v"(ga + kq * 32 + par * 8) : "memory");
        asm volatile("" : "+v"(xr[0]), "+v"(xr[1]), "+v"(xr[2]), "+v"(xr[3]), "+v"(xr[4]));
        check2();
        STAMP(21);
        static_for<0, 10>([&](auto I) { pload(IC<9 - I.value>{}); });
        asm volatile("s_waitcnt vmcnt(8)" : "+v"(P[0]), "+v"(P[1]), "+v"(P[2]), "+v"(P[3]), "+v"(P[4]), "+v"(P[5]) :: "memory");
        STAMP(22);
        u16* orow = out + (size_t)(it * 128 + w * 32 + (lane & 31)) * DM + 4 * (lane >> 5);
        auto fin2 = [&](float a0, float a1, float g0, float g1, u32 xk) -> u32 {
            const u32 t = pkbf(a0 * -0.69314718055994530942f, a1 * -0.69314718055994530942f);
            const u32 gz = pkbf(bflo(t) * g0, bfhi(t) * g1);
            return pkbf(bflo(xk) + bflo(gz), bfhi(xk) + bfhi(gz));
        };
#pragma unroll
        for (int k = 0; k < 4; ++k) {
            u32x2 o;
#pragma unroll
            for (int j = 0; j < 2; ++j) {
                const int d = 2 * k + j;
                const u32 p0 = P[d >> 2][d & 3], p1 = P[2 + (d >> 2)][d & 3], p2 = P[4 + (d >> 2)][d & 3], xk = xr[k][j];
                o[j] = fin2(acc2[4][2 * d] + bflo(p0) + bflo(p1) + bflo(p2), acc2[4][2 * d + 1] + bfhi(p0) + bfhi(p1) + bfhi(p2),
                            gb[k][2 * j], gb[k][2 * j + 1], xk);
            }
            *(u32x2*)(orow + Town * 32 + 8 * k) = o;
        }
        asm volatile("s_waitcnt vmcnt(4)" : "+v"(Q[0]), "+v"(Q[1]), "+v"(Q[2]), "+v"(Q[3]), "+v"(Q[4]), "+v"(Q[5]), "+v"(Q[6]), "+v"(Q[7]) :: "memory");
        float a0 = 0.f, a1 = 0.f;
#pragma unroll
        for (int r = 0; r < 8; ++r) { a0 += bflo(Q[r]); a1 += bfhi(Q[r]); }
        *(u32*)(orow + Thalf * 32 + 8 * kq + 2 * par) = fin2(a0, a1, gh[0], gh[1], par ? xr[4][1] : xr[4][0]);
    }
    asm volatile("k_mlp_end:");
}
#if defined(__HIP_DEVICE_COMPILE__)
#pragma clang attribute pop
#endif

extern "C" __global__ void k_diag(char* ws) {
    const unsigned long long now = __builtin_amdgcn_s_memrealtime();
    const volatile unsigned long long* t = (const volatile unsigned long long*)(ws + 49152);
    unsigned long long tt[4] = {t[0], t[1], t[2], now};
    unsigned long long d = DIAG < 0 ? 0 : (tt[DIAG + 1] - tt[DIAG]) * 5;
    if (d > 100000) d = 100000;
    while (__builtin_amdgcn_s_memrealtime() - now < d) __builtin_amdgcn_s_sleep(1);
}
extern "C" void kernel_launch(void* const* d_in, const int* in_sizes, int n_in, void* d_out, int out_size,
                              void* d_ws, size_t ws_size, hipStream_t stream) {
    const u16* x = (const u16*)d_in[0];
    char* ws = (char*)d_ws;
    (void)hipFuncSetAttribute((const void*)k_mlp, hipFuncAttributeMaxDynamicSharedMemorySize, MLP_LDS);
    k_prep<<<NB_PREP, 256, PREP_LDS, stream>>>(x, (const u16*)d_in[1], (const u16*)d_in[2], (const u16*)d_in[3],
                                               (const u16*)d_in[4], (const u16*)d_in[5], (const u16*)d_in[9],
                                               (const u16*)d_in[10], (const u16*)d_in[11], ws);
    k_conv<<<NB_PF + NB_CONV + NB_W1, 256, CONV_LDS, stream>>>((const u16*)d_in[6], (const u16*)d_in[8], ws);
    k_mlp<<<256, 256, MLP_LDS, stream>>>(ws, (u16*)d_out);
    k_diag<<<1, 64, 0, stream>>>(ws);
}
```

```cpp
#include <hip/hip_runtime.h>
#include <stdint.h>

typedef unsigned short u16;
typedef unsigned int u32;
typedef float f32x2 __attribute__((ext_vector_type(2)));
typedef float f32x4 __attribute__((ext_vector_type(4)));
typedef float f32x16 __attribute__((ext_vector_type(16)));
typedef _Float16 f16x2 __attribute__((ext_vector_type(2)));
typedef _Float16 f16x8 __attribute__((ext_vector_type(8)));
typedef __bf16 bf16x2 __attribute__((ext_vector_type(2)));
typedef __bf16 bf16x8 __attribute__((ext_vector_type(8)));
typedef short s16x4 __attribute__((ext_vector_type(4)));
typedef u32 u32x2 __attribute__((ext_vector_type(2)));
typedef u32 u32x4 __attribute__((ext_vector_type(4)));
#define LAS __attribute__((address_space(3)))
#define DI __device__ __forceinline__
#define RTSTAMP(k)
#define MARK(i)

constexpr int SEQ = 2048, DM = 576, NS = 128, FF = 4608;

constexpr u32 WS_MOD   = 0;
constexpr u32 WS_STAT  = 16384;
constexpr u32 WS_KSC   = 32768;
constexpr u32 WS_FLAG  = 36864;
constexpr u32 WS_EX    = 65536;
constexpr u32 WS_XT    = 1u << 20;
constexpr u32 WS_KR    = WS_XT + 2359296;
constexpr u32 WS_X1F   = WS_KR + 2359296;
constexpr u32 WS_WF    = WS_X1F + 2359296;
constexpr u32 WS_HF    = WS_WF + 10616832;
constexpr u32 WS_PART  = WS_HF + 18874368;
constexpr u32 WS_PH    = WS_PART + 6291456;
constexpr u32 WS_END   = WS_PH + 1048576;

DI float bflo(u32 u) { return __builtin_bit_cast(float, u << 16); }
DI float bfhi(u32 u) { return __builtin_bit_cast(float, u & 0xffff0000u); }
DI float bf2f(u16 v) { return __builtin_bit_cast(float, ((u32)v) << 16); }
DI u32 pkbf(float a, float b) { f32x2 v = {a, b}; return __builtin_bit_cast(u32, __builtin_convertvector(v, bf16x2)); }
DI u32 pkhf(float a, float b) { f32x2 v = {a, b}; return __builtin_bit_cast(u32, __builtin_convertvector(v, f16x2)); }
DI float rbf(float a) { return bflo(pkbf(a, a)); }
template <int CTRL, int RM = 0xf> DI float dppf(float v) {
    return __builtin_bit_cast(float, __builtin_amdgcn_update_dpp(0, __builtin_bit_cast(int, v), CTRL, RM, 0xf, true));
}
DI u32x4 trread2(const char* smem, u32 off0, u32 off1) {
    const u32x2 l = __builtin_bit_cast(u32x2, __builtin_amdgcn_ds_read_tr16_b64_v4i16((LAS s16x4*)(smem + off0)));
    const u32x2 h = __builtin_bit_cast(u32x2, __builtin_amdgcn_ds_read_tr16_b64_v4i16((LAS s16x4*)(smem + off1)));
    return (u32x4){l[0], l[1], h[0], h[1]};
}
DI void st16(char* ws, u32 off, u32x4 v) {
    __builtin_amdgcn_raw_buffer_store_b128(v, __builtin_amdgcn_make_buffer_rsrc(ws, 0, WS_END, 0x00020000), off, 0, 16);
}
DI void st4(char* ws, u32 off, u32 v) {
    __builtin_amdgcn_raw_buffer_store_b32(v, __builtin_amdgcn_make_buffer_rsrc(ws, 0, WS_END, 0x00020000), off, 0, 16);
}
DI float pow2i(int e) { e = e < -126 ? -126 : (e > 127 ? 127 : e); return __builtin_bit_cast(float, (u32)(e + 127) << 23); }
DI int expo(float x) { return (int)((__builtin_bit_cast(u32, x) >> 23) & 255u) - 127; }
DI u32 imgrow(u32 row) { return row * 1184u + (row >> 3) * 128u; }
DI f32x2 cmul(f32x2 z, f32x2 w, f32x2 wp) {
    const f32x2 zx = {z[0], z[0]}, zy = {z[1], z[1]};
    return __builtin_elementwise_fma(zy, wp, zx * w);
}
#define CODE_PREFETCH(sym, part) do { u32 lo_, hi_, len_; \
    asm volatile("s_getpc_b64 s[20:21]\n\ts_add_u32 s20, s20, " #sym "@rel32@lo+4\n\ts_addc_u32 s21, s21, " #sym "@rel32@hi+12\n\t" \
                 "s_mov_b32 %0, s20\n\ts_mov_b32 %1, s21\n\ts_mov_b32 %2, " #sym "_end-" #sym : "=s"(lo_), "=s"(hi_), "=s"(len_) :: "scc", "s20", "s21"); \
    const char* p_ = (const char*)(((unsigned long long)hi_ << 32) | lo_); \
    const u32 o_ = (threadIdx.x + (part) * 256u) * 128u; \
    if (o_ < len_) { u32 t_; asm volatile("global_load_dword %0, %1, off\n\ts_waitcnt vmcnt(0)" : "=v"(t_) : "v"(p_ + o_) : "memory"); } \
    } while (0)

#define DELAY1 20
#define DELAY2 0
#define NAV 26
constexpr int NA = NAV, NB = 36 - NA;
constexpr int NB_PF = 16, NB_LN = 64, NB_GEMV = 108, NB_KGEN = 576, NB_W2 = 144;
constexpr int NB_PREP = NB_PF + NB_LN + NB_GEMV + NB_KGEN + NB_W2;
constexpr int PREP_LDS = 38400;

extern "C" __global__ __launch_bounds__(256, 4) void k_prep(const u16* __restrict__ x, const u16* __restrict__ logA,
                                              const u16* __restrict__ Aim, const u16* __restrict__ Cre,
                                              const u16* __restrict__ Cim, const u16* __restrict__ logdt,
                                              const u16* __restrict__ w2, const u16* __restrict__ cvec,
                                              const u16* __restrict__ wmod, char* __restrict__ ws) {
    extern __shared__ __attribute__((aligned(16))) char smem[];
    RTSTAMP(0);
    const int tid = threadIdx.x, lane = tid & 63;
    const int wv = __builtin_amdgcn_readfirstlane(tid >> 6);
    int b = blockIdx.x;
    if (b >= NB_PF && b < NB_PF + NB_KGEN + NB_LN + NB_GEMV) b = b < NB_PF + NB_KGEN ? b + NB_LN + NB_GEMV : b - NB_KGEN;
    if (b < NB_PF) {
        if (b < 8) CODE_PREFETCH(k_prep, 0); else CODE_PREFETCH(k_conv, 0);
        if (b < 2) st16(ws, WS_FLAG + b * 4096 + tid * 16, (u32x4){0u, 0u, 0u, 0u});
    } else if ((b -= NB_PF) < NB_LN) {
        const int t0 = b * 32, tok = tid >> 3, sub = tid & 7;
        u32x4 v[9];
#pragma unroll
        for (int i = 0; i < 9; ++i) v[i] = *(const u32x4*)(x + (size_t)(t0 + tok) * DM + (sub + 8 * i) * 8);
        MARK(0);
#pragma unroll
        for (int i = 0; i < 9; ++i) *(u32x4*)(smem + imgrow(tok) + (sub + 8 * i) * 16) = v[i];
        __syncthreads();
        const int g = lane >> 4, qq = (lane & 15) >> 2, p = lane & 3;
#pragma unroll
        for (int i = 0; i < 9; ++i) {
            const int cb = wv * 9 + i;
            st16(ws, WS_XT + b * 36864 + cb * 1024 + (lane & 15) * 64 + g * 16,
                 trread2(smem, imgrow(8 * g + qq) + cb * 32 + p * 8, imgrow(8 * g + 4 + qq) + cb * 32 + p * 8));
        }
        MARK(1);
        float s = 0.f;
#pragma unroll
        for (int i = 0; i < 9; ++i)
#pragma unroll
            for (int k = 0; k < 4; ++k) s += bflo(v[i][k]) + bfhi(v[i][k]);
        s += dppf<0xB1>(s); s += dppf<0x4E>(s); s += dppf<0x141>(s);
        const float mean = s * (1.f / DM);
        float q = 0.f;
#pragma unroll
        for (int i = 0; i < 9; ++i)
#pragma unroll
            for (int k = 0; k < 4; ++k) {
                const float a = bflo(v[i][k]) - mean, c = bfhi(v[i][k]) - mean;
                q = fmaf(a, a, q); q = fmaf(c, c, q);
            }
        q += dppf<0xB1>(q); q += dppf<0x4E>(q); q += dppf<0x141>(q);
        if (sub == 0) st4(ws, WS_STAT + (t0 + tok) * 4, pkbf(mean, __builtin_amdgcn_sqrtf(rbf(rbf(q * (1.f / DM)) + 1e-5f))));
    } else if ((b -= NB_LN) < NB_GEMV) {
        const int seg = tid & 3, rg = tid >> 2;
        u32x4 v[9]; u16 cv[9];
#pragma unroll
        for (int i = 0; i < 9; ++i) {
            const int row = rg + 64 * i;
            cv[i] = cvec[row];
            v[i] = *(const u32x4*)(wmod + (size_t)row * (6 * DM) + b * 32 + seg * 8);
        }
        MARK(0);
        float acc[8];
#pragma unroll
        for (int k = 0; k < 8; ++k) acc[k] = 0.f;
#pragma unroll
        for (int i = 0; i < 9; ++i) {
            const float c = bf2f(cv[i]);
            const float sl = rbf(c * __builtin_amdgcn_rcpf(1.f + __builtin_amdgcn_exp2f(c * -1.44269504088896340736f)));
#pragma unroll
            for (int k = 0; k < 4; ++k) {
                acc[2 * k] = fmaf(sl, bflo(v[i][k]), acc[2 * k]);
                acc[2 * k + 1] = fmaf(sl, bfhi(v[i][k]), acc[2 * k + 1]);
            }
        }
#pragma unroll
        for (int k = 0; k < 8; ++k) { acc[k] += dppf<0x114>(acc[k]); acc[k] += dppf<0x118>(acc[k]); }
        float* red = (float*)smem;
        if ((lane & 15) >= 12) {
            *(f32x4*)(red + (tid >> 4) * 32 + seg * 8) = (f32x4){acc[0], acc[1], acc[2], acc[3]};
            *(f32x4*)(red + (tid >> 4) * 32 + seg * 8 + 4) = (f32x4){acc[4], acc[5], acc[6], acc[7]};
        }
        __syncthreads();
        MARK(1);
        if (tid < 32) {
            float t = 0.f;
#pragma unroll
            for (int i = 0; i < 16; ++i) t += red[i * 32 + tid];
            st4(ws, WS_MOD + (b * 32 + tid) * 4, __builtin_bit_cast(u32, rbf(t)));
        }
    } else if ((b -= NB_GEMV) < NB_KGEN) {
        const int d = b, hn = wv & 1, grp = wv >> 1;
        const int n = hn * 64 + lane, n2 = n ^ 64;
        const u16 la_ = logA[d * NS + n], ai_ = Aim[d * NS + n], cr_ = Cre[d * NS + n], ci_ = Cim[d * NS + n];
        const u16 cr2_ = Cre[d * NS + n2], ci2_ = Cim[d * NS + n2], ld_ = logdt[d];
        u32 ta[8];
#pragma unroll
        for (int j = 0; j < 8; j += 2) {
            const float l0 = (float)(2047 - tid * 8 - j);
            const u32 pk = pkbf(l0, l0 - 1.f);
            const float p0 = bflo(pk), p1 = bfhi(pk);
            ta[j] = (u32)fminf(4.f * p0, p0 + 1536.f); ta[j + 1] = (u32)fminf(4.f * p1, p1 + 1536.f);
        }
        asm volatile("" : "+v"(ta[0]), "+v"(ta[1]), "+v"(ta[2]), "+v"(ta[3]), "+v"(ta[4]), "+v"(ta[5]), "+v"(ta[6]), "+v"(ta[7]));
        MARK(0);
        const float cr = bf2f(cr_), ci = bf2f(ci_);
        const float dt = rbf(__builtin_amdgcn_exp2f(bf2f(ld_) * 1.44269504088896340736f));
        const float zr = -rbf(__builtin_amdgcn_exp2f(bf2f(la_) * 1.44269504088896340736f)) * dt, zi = bf2f(ai_) * dt;
        float bs = fabsf(cr) + fabsf(ci) + fabsf(bf2f(cr2_)) + fabsf(bf2f(ci2_));
        bs += dppf<0xB1>(bs); bs += dppf<0x4E>(bs); bs += dppf<0x141>(bs); bs += dppf<0x140>(bs);
        bs += dppf<0x142, 0xa>(bs); bs += dppf<0x143, 0xc>(bs);
        bs = __builtin_bit_cast(float, __builtin_amdgcn_readlane(__builtin_bit_cast(int, bs), 63));
        const int se = 14 - expo(2.f * dt * bs * 1.0001f);
        const float scale = pow2i(se > 100 ? 100 : se);
        const float zr2 = zr * 1.44269504088896340736f;
        auto cexp = [&](float p) -> f32x2 {
            const float mag = __builtin_amdgcn_exp2f(zr2 * p);
            const float ang = zi * p;
            const float rr = __builtin_rintf(ang * 0.15915494309189535f);
            float fr = fmaf(ang, 0.15915494309189535f, -rr);
            fr = fmaf(ang, 6.4206382e-9f, fr);
            return (f32x2){mag * __builtin_amdgcn_cosf(fr), mag * __builtin_amdgcn_sinf(fr)};
        };
        const float p1 = grp ? 4.f : 1.f;
        const f32x2 f1 = cexp(p1), c1 = cexp(16.f * p1), s0 = cexp(grp ? 512.f : 0.f), s1 = cexp(256.f * p1);
        const f32x2 fw = {f1[0], -f1[1]}, fwp = {f1[1], f1[0]};
        const f32x2 cwp = {-c1[1], c1[0]};
        const float es = 2.f * dt * scale;
        f32x2 za = cmul((f32x2){es * cr, es * ci}, s0, (f32x2){-s0[1], s0[0]});
        f32x2 zb = cmul(za, s1, (f32x2){-s1[1], s1[0]});
        f32x2 zf = {1.f, 0.f};
        char* tab = smem + wv * 8704;
        u32 keep[16];
#pragma unroll
        for (int k = 0; k < 16; ++k) {
            *(u32*)(tab + k * 272 + lane * 4) = pkhf(zf[0], zf[1]);
            *(u32*)(tab + (16 + k) * 272 + lane * 4) = pkhf(za[0], za[1]);
            keep[k] = pkhf(zb[0], zb[1]);
            zf = cmul(zf, fw, fwp); za = cmul(za, c1, cwp); zb = cmul(zb, c1, cwp);
        }
        const int r16 = lane & 15, g = lane >> 4;
        f16x8 fb[4];
        f32x4 acc0 = {0.f, 0.f, 0.f, 0.f}, acc1 = {0.f, 0.f, 0.f, 0.f};
        __builtin_amdgcn_fence(__ATOMIC_RELEASE, "wavefront");
        __builtin_amdgcn_wave_barrier();
#pragma unroll
        for (int ks = 0; ks < 4; ++ks) {
            fb[ks] = *(const f16x8*)(tab + r16 * 272 + ks * 64 + g * 16);
            const f16x8 fa = *(const f16x8*)(tab + (16 + r16) * 272 + ks * 64 + g * 16);
            acc0 = __builtin_amdgcn_mfma_f32_16x16x32_f16(fb[ks], fa, acc0, 0, 0, 0);
        }
        __builtin_amdgcn_fence(__ATOMIC_RELEASE, "wavefront");
        __builtin_amdgcn_wave_barrier();
#pragma unroll
        for (int k = 0; k < 16; ++k) *(u32*)(tab + (16 + k) * 272 + lane * 4) = keep[k];
        __builtin_amdgcn_fence(__ATOMIC_RELEASE, "wavefront");
        __builtin_amdgcn_wave_barrier();
#pragma unroll
        for (int ks = 0; ks < 4; ++ks) {
            const f16x8 fa = *(const f16x8*)(tab + (16 + r16) * 272 + ks * 64 + g * 16);
            acc1 = __builtin_amdgcn_mfma_f32_16x16x32_f16(fb[ks], fa, acc1, 0, 0, 0);
        }
        MARK(1);
        __builtin_amdgcn_fence(__ATOMIC_RELEASE, "wavefront");
        __builtin_amdgcn_wave_barrier();
        *(f32x4*)(tab + r16 * 64 + g * 16) = acc0;
        *(f32x4*)(tab + 1024 + r16 * 64 + g * 16) = acc1;
        __syncthreads();
        float kv[8];
#pragma unroll
        for (int j = 0; j < 8; ++j) {
            const u32 o = ta[j] + (ta[j] >= 2048u ? 2u * 8704u - 2048u : 0u);
            kv[j] = *(const float*)(smem + o) + *(const float*)(smem + o + 8704);
        }
        MARK(2);
        st16(ws, WS_KR + d * 4096 + tid * 16, (u32x4){pkhf(kv[0], kv[1]), pkhf(kv[2], kv[3]), pkhf(kv[4], kv[5]), pkhf(kv[6], kv[7])});
        if (tid == 0) st4(ws, WS_KSC + d * 4, __builtin_bit_cast(u32, pow2i(se > 100 ? -100 : -se)));
    } else {
        b -= NB_KGEN;
        const u16* src = w2 + (size_t)b * 32 * DM;
        if (DELAY1 > 0) __builtin_amdgcn_s_sleep(DELAY1);
        const int tok = tid >> 3, sub = tid & 7;
        u32x4 v[9];
#pragma unroll
        for (int i = 0; i < 9; ++i) v[i] = *(const u32x4*)(src + tok * DM + (sub + 8 * i) * 8);
        MARK(0);
#pragma unroll
        for (int i = 0; i < 9; ++i) *(u32x4*)(smem + imgrow(tok) + (sub + 8 * i) * 16) = v[i];
        __syncthreads();
        const int kq = b / 36, kk = b - kq * 36, sp = kk / 9, c = kk - sp * 9;
        const int rel = (sp - wv) & 3;
        const int e = rel == 0 ? c : 9 + c * 3 + (rel - 1);
        const int r0 = 4 * (lane >> 4) + ((lane & 15) >> 2), p = lane & 3;
#pragma unroll
        for (int i = 0; i < 9; ++i) {
            const int sg = (i + 1) >> 1, j = i == 0 ? 0 : (i - 1) & 1;
            const int T = 9 * wv + (sg == 0 ? 8 : 2 * ((kq + sg) & 3) + j);
            const int m = sg == 4 ? 252 + 2 * e + j
                        : e < NA ? 7 * e + i
                        : sg == 0 ? 7 * NA + 2 * NB + (e - NA) : 7 * NA + (sg == 1 ? 0 : (2 * sg - 1) * NB) + (e - NA) * 2 + j;
            const u32 col = T * 32 + p * 8;
            st16(ws, WS_WF + ((kq * 4 + wv) * 648 + 324 + m) * 1024 + lane * 16, trread2(smem, imgrow(r0) + col, imgrow(r0 + 16) + col));
        }
    }
    asm volatile("k_prep_end:");
}

constexpr int NB_CONV = 576, NB_W1 = 144;
constexpr int CONV_LDS = 36864 + 4096;
constexpr int UCP = 4640, KB_OFF = 4 * UCP;

extern "C" __global__ __launch_bounds__(256, 4) void k_conv(const u16* __restrict__ dskip, const u16* __restrict__ w1,
                                              char* __restrict__ ws) {
    extern __shared__ __attribute__((aligned(16))) char smem[];
    const int tid = threadIdx.x, lane = tid & 63;
    const int wv = __builtin_amdgcn_readfirstlane(tid >> 6);
    RTSTAMP(1);
    const float* mod = (const float*)(ws + WS_MOD);
    int b = blockIdx.x;
    if (b >= NB_PF) b = b < NB_PF + NB_CONV ? b + NB_W1 : b - NB_CONV;
    if (b < NB_PF) {
        CODE_PREFETCH(k_mlp, b >> 3);
    } else if ((b -= NB_PF) < NB_W1) {
        const int q = b / 9, c = b - q * 9, f0 = b * 32;
        if (DELAY2 > 0) __builtin_amdgcn_s_sleep(DELAY2);
        const int seg = tid & 3, rg = tid >> 2;
        u32x4 v[9]; float sb[9], cb[9];
#pragma unroll
        for (int i = 0; i < 9; ++i) {
            const int row = rg + 64 * i;
            v[i] = *(const u32x4*)(w1 + (size_t)row * FF + f0 + seg * 8);
            sb[i] = mod[3 * DM + row]; cb[i] = mod[4 * DM + row];
        }
        MARK(0);
        float S[8], B[8];
#pragma unroll
        for (int k = 0; k < 8; ++k) { S[k] = 0.f; B[k] = 0.f; }
#pragma unroll
        for (int i = 0; i < 9; ++i) {
            const float c1 = rbf(1.f + cb[i]);
            u32x4 o;
#pragma unroll
            for (int k = 0; k < 4; ++k) {
                const float lo = bflo(v[i][k]), hi = bfhi(v[i][k]);
                o[k] = pkbf(lo * c1, hi * c1);
                S[2 * k] += bflo(o[k]); S[2 * k + 1] += bfhi(o[k]);
                B[2 * k] = fmaf(sb[i], lo, B[2 * k]); B[2 * k + 1] = fmaf(sb[i], hi, B[2 * k + 1]);
            }
            *(u32x4*)(smem + ((tid ^ ((tid >> 3) & 2)) + 256 * i) * 16) = o;
        }
#pragma unroll
        for (int k = 0; k < 8; ++k) {
            S[k] += dppf<0x114>(S[k]); S[k] += dppf<0x118>(S[k]);
            B[k] += dppf<0x114>(B[k]); B[k] += dppf<0x118>(B[k]);
        }
        float* red = (float*)(smem + 36864);
        if ((lane & 15) >= 12) {
            float* r = red + (tid >> 4) * 64 + seg * 8;
            *(f32x4*)(r) = (f32x4){S[0], S[1], S[2], S[3]}; *(f32x4*)(r + 4) = (f32x4){S[4], S[5], S[6], S[7]};
            *(f32x4*)(r + 32) = (f32x4){B[0], B[1], B[2], B[3]}; *(f32x4*)(r + 36) = (f32x4){B[4], B[5], B[6], B[7]};
        }
        __syncthreads();
        MARK(1);
        const int g = lane >> 4, qq = (lane & 15) >> 2, p = lane & 3;
#pragma unroll
        for (int i = 0; i < 9; ++i) {
            const int s = wv * 9 + i;
            const u32 a = (32 * (s >> 1) + 4 * g + qq) * 64 + (((s & 1) ^ (g & 1)) * 32) + p * 8;
            st16(ws, WS_WF + (q * 648 + c * 36 + s) * 1024 + lane * 16, trread2(smem, a, a + 1024));
        }
        if (wv == 0) {
            float t = 0.f;
#pragma unroll
            for (int i = 0; i < 16; ++i) t += red[i * 64 + lane];
            const float B_ = __builtin_bit_cast(float, __builtin_amdgcn_ds_bpermute((lane ^ 32) * 4, __builtin_bit_cast(int, t)));
            const float S_ = -t;
            const float s1 = rbf(S_), s2 = rbf(S_ - s1), s3 = S_ - s1 - s2;
            const float b1 = rbf(B_), b2 = rbf(B_ - b1), b3 = B_ - b1 - b2;
            if (lane < 32) st16(ws, WS_EX + q * 8192 + (c * 32 + lane) * 16, (u32x4){pkbf(s1, s2), pkbf(s3, b1), pkbf(b2, b3), 0u});
        } else if (wv == 1 && c == 0 && lane < 48) {
            const int d = (q & 3) * 144 + (lane < 32 ? (q >> 2) * 32 + lane : 96 + lane);
            st4(ws, WS_EX + q * 8192 + 4608 + lane * 4, __builtin_bit_cast(u32, mod[5 * DM + d]));
        }
    } else {
        const int d = b - NB_W1;
        const int tp = tid > 0 ? tid * 8 - 4 : 0;
        const char* xb = ws + WS_XT + d * 64;
        const u32x4 x0 = *(const u32x4*)(xb + (tid >> 2) * 36864 + (tid & 3) * 16);
        const u32x2 xm = *(const u32x2*)(xb + (tp >> 5) * 36864 + (tp & 31) * 2);
        u32x4 st[3];
        st[0] = *(const u32x4*)(ws + WS_STAT + tp * 4);
        st[1] = *(const u32x4*)(ws + WS_STAT + tid * 32);
        st[2] = *(const u32x4*)(ws + WS_STAT + tid * 32 + 16);
        const u32x4 kv = *(const u32x4*)(ws + WS_KR + d * 4096 + tid * 16);
        const float sa = mod[d], ca = mod[DM + d], ga = mod[2 * DM + d];
        const float dsk = bf2f(dskip[d]);
        const float kinv = ((const float*)(ws + WS_KSC))[d];
        {
            const u32x4 z = {0u, 0u, 0u, 0u};
            if (tid < 96) *(u32x4*)(smem + (tid / 24) * UCP + (tid % 24) * 16) = z;
            else if (tid < 104) *(u32x4*)(smem + KB_OFF + 4096 + (tid - 96) * 16) = z;
        }
        MARK(0);
        const float c1 = rbf(1.f + ca);
        const int he = expo(64.f * fabsf(c1) + 2.f * fabsf(sa)) - 14;
        const float hs = pow2i(-he), oscale = pow2i(he) * kinv;
        const u32 xw[6] = {xm[0], xm[1], x0[0], x0[1], x0[2], x0[3]};
        u32 P[6], hb[4];
#pragma unroll
        for (int k = 0; k < 6; ++k) {
            const u32 s0 = st[k >> 1][(k & 1) * 2], s1 = st[k >> 1][(k & 1) * 2 + 1];
            const float n0 = (bflo(xw[k]) - bflo(s0)) * __builtin_amdgcn_rcpf(bfhi(s0));
            const float n1 = (bfhi(xw[k]) - bflo(s1)) * __builtin_amdgcn_rcpf(bfhi(s1));
            const u32 t = pkbf(fmaf(n0, c1, sa), fmaf(n1, c1, sa));
            if (k >= 2) hb[k - 2] = t;
            P[k] = pkhf(bflo(t) * hs, bfhi(t) * hs);
        }
        if (tid == 0) { P[0] = 0u; P[1] = 0u; }
        u32 Q[5];
#pragma unroll
        for (int k = 0; k < 5; ++k) Q[k] = __builtin_amdgcn_alignbit(P[k + 1], P[k], 16);
        {
            char* ub = smem + 384 + tid * 16;
            *(u32x4*)(ub) = (u32x4){Q[0], Q[1], Q[2], Q[3]};
            *(u32x4*)(ub + UCP) = (u32x4){P[1], P[2], P[3], P[4]};
            *(u32x4*)(ub + 2 * UCP) = (u32x4){Q[1], Q[2], Q[3], Q[4]};
            *(u32x4*)(ub + 3 * UCP) = (u32x4){P[2], P[3], P[4], P[5]};
            *(u32x4*)(smem + KB_OFF + tid * 16) = kv;
        }
        __syncthreads();
        MARK(1);
        const int c = lane & 15, g = lane >> 4;
        f32x4 acc[8];
#pragma unroll
        for (int j = 0; j < 8; ++j) acc[j] = (f32x4){0.f, 0.f, 0.f, 0.f};
#pragma unroll
        for (int rr = 0; rr < 2; ++rr) {
            const int r = 2 * wv + rr;
            const char* kb = smem + KB_OFF + 2 * (2076 - 4 * c + 8 * g - 32 * r);
            const char* ub = smem + (c & 3) * UCP + 2 * (8 * g + 64 * (c >> 2) + 32 * (1 - r) + 192);
            f16x8 kf[8], uf[8];
#pragma unroll
            for (int i = 0; i < 8; ++i) {
                const u32x2 a = *(const u32x2*)(kb - i * 512), a2 = *(const u32x2*)(kb - i * 512 + 8);
                kf[i] = __builtin_bit_cast(f16x8, (u32x4){a[0], a[1], a2[0], a2[1]});
                uf[i] = *(const f16x8*)(ub + i * 512);
            }
#pragma unroll
            for (int i = 0; i < 8; ++i)
#pragma unroll
                for (int dl = 0; dl + i < 8; ++dl)
                    acc[i + dl] = __builtin_amdgcn_mfma_f32_16x16x32_f16(uf[dl], kf[i], acc[i + dl], 0, 0, 0);
        }
        __syncthreads();
        MARK(2);
#pragma unroll
        for (int j = 0; j < 8; ++j) *(f32x4*)(smem + wv * 8192 + j * 1024 + lane * 16) = acc[j];
        __syncthreads();
        f32x4 y0 = *(const f32x4*)(smem + tid * 32), y1 = *(const f32x4*)(smem + tid * 32 + 16);
#pragma unroll
        for (int w = 1; w < 4; ++w) {
            y0 += *(const f32x4*)(smem + w * 8192 + tid * 32);
            y1 += *(const f32x4*)(smem + w * 8192 + tid * 32 + 16);
        }
        const float y[8] = {y0[0], y0[1], y0[2], y0[3], y1[0], y1[1], y1[2], y1[3]};
        u32x4 o;
#pragma unroll
        for (int k = 0; k < 4; ++k) {
            const u32 yb = pkbf(y[2 * k] * oscale, y[2 * k + 1] * oscale);
            const u32 sk = pkbf(bflo(hb[k]) * dsk, bfhi(hb[k]) * dsk);
            const u32 z = pkbf(bflo(yb) + bflo(sk), bfhi(yb) + bfhi(sk));
            const u32 gz = pkbf(bflo(z) * ga, bfhi(z) * ga);
            o[k] = pkbf(bflo(x0[k]) + bflo(gz), bfhi(x0[k]) + bfhi(gz));
        }
        MARK(3);
        st16(ws, WS_X1F + (tid >> 4) * 147456 + ((d / 48) * 12 + ((tid >> 2) & 3) * 3 + (d >> 4) % 3) * 1024 + (d & 15) * 64 + (tid & 3) * 16, o);
    }
    asm volatile("k_conv_end:");
}
constexpr u32 L_EXF = 147456;
constexpr int MLP_LDS = L_EXF + 12288;
constexpr int GE = 648;

#if defined(__HIP_DEVICE_COMPILE__)
#pragma clang attribute push(__attribute__((target("no-packed-fp32-ops"))), apply_to = function)
#endif
template <int N> struct IC { static constexpr int value = N; };
template <int A, int B, class F> DI void static_for(F&& f) {
    if constexpr (B - A == 1) f(IC<A>{});
    else if constexpr (A < B) { static_for<A, (A + B) / 2>(f); static_for<(A + B) / 2, B>(f); }
}
constexpr int imin(int a, int b) { return a < b ? a : b; }
constexpr int XW = 6;
#define XSP 7
#define XIV 8
constexpr int XI = XIV;
constexpr int x_dmas(int j) { return j <= 0 ? 0 : imin((4 * j + XSP - 1) / XSP, 3 * (19 - XI)); }
constexpr int G1 = 36;
#define PLG 614
constexpr int P1 = 36 * 5 + 7, P2 = 36 * 7 + 7, P3 = 400;
#define PREF 0
#define CD1 34
#define CD3 30
#define QW3 8
constexpr int C1 = P1 + CD1, C2 = P2 + CD1, C3 = P3 + CD3;
constexpr bool s_poll(int G) { return G == P1 || G == P2 || G == P3; }
#define PB2 606
#define PL4 640
constexpr int PUB2 = PB2, P4 = PL4;
constexpr int HS0 = 22, HS1 = 35;
constexpr int s_hstore(int G) { return G < 72 ? (G == 64 ? 0 : G == 71 ? 1 : -1) : G < 324 ? (G % 36 == HS0 ? 0 : G % 36 == HS1 ? 1 : -1) : G == 338 ? 0 : G == 346 ? 1 : -1; }
constexpr int B0 = 324 + 7 * NA;
constexpr int slot_end(int sg) { return B0 - 1 + (sg == 1 ? 2 : sg == 0 ? 3 : 2 * sg + 1) * NB; }
constexpr int s_pstore(int G) {
    for (int sg = 0; sg < 4; ++sg) { if (G == slot_end(sg) + 4) return 2 * sg; if (G == slot_end(sg) + 6 && sg > 0) return 2 * sg + 1; }
    return -1;
}
constexpr bool s_res(int G) { return G == 620 || G == 622 || G == 624; }
constexpr int s_pub(int G) { return G == 36 * 3 + 24 ? 2 : G == 36 * 5 + 24 ? 4 : G == 372 ? 9 : 0; }
constexpr int m_slot(int m) { return m < 7 * NA ? (m % 7 + 1) / 2 : m < 7 * NA + 2 * NB ? 1 : m < 7 * NA + 3 * NB ? 0 : m < 252 ? 2 + (m - 7 * NA - 3 * NB) / (2 * NB) : 4; }
constexpr int m_ent(int m) { return m < 7 * NA ? m / 7 : m < 7 * NA + 2 * NB ? NA + (m - 7 * NA) / 2 : m < 7 * NA + 3 * NB ? NA + (m - 7 * NA - 2 * NB)
                                  : m < 252 ? NA + ((m - 7 * NA - 3 * NB) % (2 * NB)) / 2 : (m - 252) / 2; }
constexpr int m_half(int m) { return m < 7 * NA ? (m % 7 == 0 ? 0 : (m % 7 - 1) & 1) : m < 7 * NA + 2 * NB ? (m - 7 * NA) & 1 : m < 7 * NA + 3 * NB ? 0
                                   : m < 252 ? (m - 7 * NA - 3 * NB) & 1 : m & 1; }
constexpr bool m_first(int m) { return m < 7 || m == 252 || m == 253; }
constexpr bool ent_first(int m) { return m < 7 * NA ? m % 7 == 0 : (m < 7 * NA + 2 * NB && ((m - 7 * NA) & 1) == 0); }
constexpr bool ent_own(int e) { return e < 9; }
constexpr int first_use(int e) { return e < NA ? 324 + 7 * e : B0 + 2 * (e - NA); }
constexpr bool s_ring(int G) { return G + 12 < GE && (G < 100 ? G % 12 == 0 : G < 490 ? G % 36 == 24 : G % 18 == 6); }
constexpr int ring_slot(int grp) { const int u = (grp / 3) & 3; return (u == 0 ? 0 : u == 1 ? 6 : u == 2 ? 9 : 3) + grp % 3; }
constexpr int need_bar(int grp) { int g = 12 * grp - 12; while (g > 0 && !s_ring(g)) --g; return g; }
struct Sched {
    int dn[GE];
    int dgap[162];
    int hl[GE];
    int pre[GE + 1];
    int hgap[36];
    bool ok;
    constexpr Sched() : dn(), dgap(), hl(), pre(), hgap(), ok(true) {
        int nd = 3 * XW, nh = 0, last2 = -99, lastbar = 24;
        for (int n = 0; n < 162; ++n) dgap[n] = -1;
        for (int e = 0; e < 36; ++e) hgap[e] = -1;
        for (int G = 0; G < GE; ++G) {
            dn[G] = -1; hl[G] = -1;
            int n = 0;
            if (G >= G1) {
                if (s_ring(G)) lastbar = G;
                const bool fixed = s_hstore(G) >= 0 || s_pstore(G) >= 0 || s_res(G);
                const bool quiet = (G >= P1 && G < P1 + 17) || (G >= P2 && G < P2 + 17) || (G >= P3 && G < P3 + QW3);
                const int sp2 = G < 324 ? 4 : 5;
                if (fixed) last2 = 2 * G;
                else if (!quiet && 2 * G - last2 >= sp2) {
                    int dd = 1 << 20, dh = 1 << 20;
                    if (nd < 162 && (nd / 3 < 12 || (nd / 3 - 11) * 12 <= lastbar + 6)) dd = need_bar(nd / 3) - 40;
                    const int lim = G > C3 ? 54 : G > C2 ? 24 : G > C1 ? 12 : 0;
                    if (nh < lim) dh = first_use(9 + nh / 2) - 45;
                    if (dd < (1 << 20) || dh < (1 << 20)) {
                        if (dd <= dh) { dn[G] = nd; dgap[nd] = G; if (need_bar(nd / 3) <= G) ok = false; ++nd; }
                        else { hl[G] = 18 + nh; hgap[9 + nh / 2] = G; if (first_use(9 + nh / 2) <= G) ok = false; ++nh; }
                        last2 = last2 + sp2 > 2 * G - 1 ? last2 + sp2 : 2 * G - (G < 324 ? 0 : 1);
                        n = 1;
                    }
                }
                n += fixed + s_poll(G) + (G == P4) + (s_pub(G) > 0) + (G == PUB2);
            }
            pre[G + 1] = pre[G] + n;
        }
        if (nd != 162 || nh != 54) ok = false;
    }
};
constexpr Sched SCH{};
static_assert(SCH.ok, "schedule misses a deadline");
constexpr int younger(int a, int b) { return imin(SCH.pre[b] - SCH.pre[a + 1], 63); }
constexpr bool s_bar(int G) { return G >= G1 && (s_ring(G) || s_pub(G) > 0 || G == PUB2); }
constexpr int bar_wait(int G) {
    int n = 63;
    if (s_ring(G)) { int grp = 53; while (need_bar(grp) > G) --grp; n = G == 36 ? 3 : younger(SCH.dgap[3 * grp + 2], G); }
    if (G == PUB2) n = imin(n, younger(slot_end(3) + 6, G));
    if (s_pub(G) > 0) n = imin(n, younger(G == 372 ? 346 : 36 * s_pub(G) + HS1, G));
    return n;
}
constexpr int frag_lds(int f) { return ring_slot(f / 12) * 12288 + (f % 12) * 1024; }

constexpr u32 RESL = ring_slot(45) * 12288;
static_assert(SCH.dgap[161] < 620, "M0");
#define OAUX 0
#define SB() __builtin_amdgcn_sched_barrier(0)
#define STAMP(i) do {} while (0)
#define TW(code) do { code; } while (0)
#define MFMA "v_mfma_f32_16x16x32_bf16 "

extern "C" __global__ __launch_bounds__(256, 1) void k_mlp(char* __restrict__ ws, u16* __restrict__ out) {
    extern __shared__ __attribute__((aligned(16))) char smem[];
    const int tid = threadIdx.x, lane = tid & 63;
    const int w = __builtin_amdgcn_readfirstlane(tid >> 6);
    const int b = blockIdx.x, xcd = b & 7, rr = b >> 3;
#define MAPV 1
    const int s = rr & 3;
    const int kq = MAPV == 0 ? xcd >> 1 : MAPV == 1 ? 2 * (xcd >> 2) + ((rr >> 2) & 1) : (rr >> 2) & 3;
    const int it = MAPV == 0 ? 8 * (xcd & 1) + (rr >> 2) : MAPV == 1 ? 4 * (xcd & 3) + (rr >> 3) : 2 * xcd + (rr >> 4);
    const int q = 4 * kq + s;
    const auto rs = __builtin_amdgcn_make_buffer_rsrc(ws, 0, WS_END, 0x00020000);
    const u32x4 srd = {(u32)(uintptr_t)ws, (u32)((uintptr_t)ws >> 32) & 0xffffu, WS_END, 0x00020000u};
    const u32 voff = lane * 16;
    const u32 lbase = (u32)(uintptr_t)(LAS char*)smem;
    const u32 wl = lbase + w * 3072;
    auto set_m0 = [&](u32 v) { asm volatile("s_mov_b32 m0, %0" :: "s"(v) : "memory"); };
    auto dma = [&](auto O, u32 so) {
        const u32 vo = voff; const u32x4 sr = srd;
        asm volatile("buffer_load_dwordx4 %0, %1, %2 offen offset:%3 lds" :: "v"(vo), "s"(sr), "s"(so), "n"(O.value * 1024) : "memory");
    };
    auto dma3 = [&](u32 dst, u32 so) {
        asm volatile("s_mov_b32 m0, %3\n\ts_nop 0\n\tbuffer_load_dwordx4 %0, %1, %2 offen lds\n\t"
                     "buffer_load_dwordx4 %0, %1, %2 offen offset:1024 lds\n\tbuffer_load_dwordx4 %0, %1, %2 offen offset:2048 lds"
                     :: "v"(voff), "s"(srd), "s"(so), "s"(dst) : "memory");
    };
    const u32 xsb = WS_X1F + it * 147456;
    const u32 wfb = WS_WF + q * 663552 + w * 3072;
    auto rq_dst = [&](int p) -> u32 {
        const int x = p <= 2 ? p - 1 : p <= 6 ? p - 2 : p - 3;
        return wl + (p == 0 ? 0 : p == 3 ? 12288 : p == 7 ? 24576 : p == 15 ? L_EXF : p > 15 ? ring_slot(p - 13) * 12288 : (3 + x % 9) * 12288);
    };
    auto rq_src = [&](int p) -> u32 {
        const int x = p <= 2 ? p - 1 : p <= 6 ? p - 2 : p - 3;
        return p == 0 ? wfb : p == 3 ? wfb + 12288 : p == 7 ? wfb + 24576 : p == 15 ? WS_EX + q * 8192 + w * 3072
             : p > 15 ? wfb + (p - 13) * 12288 : xsb + w * 3072 + x * 12288;
    };
#pragma unroll
    for (int p = 0; p < XI; ++p) dma3(rq_dst(p), rq_src(p));
    set_m0(rq_dst(XI));
    const u32 hfb = WS_HF + (it * 4 + kq) * 36 * 8192 + w * 2048;
    u32x4 Wf[18], Ef[2];
    const u32 rd0 = lbase + voff;
    const u32 rb[3] = {rd0, rd0 + 49152, rd0 + 98304};
    auto rdw = [&](auto F) {
        constexpr int lo = frag_lds(F.value);
        u32x4& d = Wf[F.value % 18]; const u32 base = rb[lo / 49152];
        asm volatile("ds_read_b128 %0, %1 offset:%2" : "=v"(d) : "v"(base), "n"(lo % 49152) : "memory");
    };
    const u32 rex = lbase + L_EXF + (lane & 15) * 16;
    auto rde = [&](auto C) {
        u32x4& d0 = Ef[0]; u32x4& d1 = Ef[1]; const u32 base = rex;
        asm volatile("ds_read_b128 %0, %2 offset:%3\n\tds_read_b128 %1, %2 offset:%4" : "=&v"(d0), "=&v"(d1) : "v"(base), "n"(C.value * 512), "n"(C.value * 512 + 256) : "memory");
    };
    f32x4 acc[2][4];

    u32x4 Xa[36];
    f32x4 gram[2], rsum[2];
    {
        const u32 a0 = wl + (4 * (lane >> 4) + ((lane & 15) >> 2)) * 64 + (lane & 3) * 8;
        u32x4 ones = {0x3f803f80u, 0x3f803f80u, 0x3f803f80u, 0x3f803f80u};
        asm volatile("" : "+v"(ones));
        u32x2 t[3][4];
        const u32 xb[3] = {a0 + 36864, a0 + 73728, a0 + 110592};
        auto xrd = [&](auto KS, auto I) {
            constexpr int hk = 2 * KS.value + (I.value >> 1), sl = (hk / 3) % 9;
            u32x2& d = t[KS.value % 3][I.value]; const u32 base = xb[sl / 3];
            asm volatile("ds_read_b64_tr_b16 %0, %1 offset:%2" : "=v"(d) : "v"(base), "n"((sl % 3) * 12288 + (hk % 3) * 1024 + (I.value & 1) * 32) : "memory");
        };
        auto admit = [&](auto KS, auto J) {
            constexpr int ks = KS.value, x = (2 * ks + 1) / 3, pos = x <= 1 ? x + 1 : x <= 4 ? x + 2 : x + 3;
            constexpr int issued = 3 * XI + x_dmas(J.value);
            static_assert(issued >= 3 * pos + 3, "X order");
            if constexpr (ks % 6 == 0) asm volatile("s_waitcnt vmcnt(%0)\n\ts_barrier" :: "n"(issued - 3 * pos - 3) : "memory");
            else if constexpr (ks % 3 != 2) asm volatile("s_waitcnt vmcnt(%0)" :: "n"(issued - 3 * pos - 3) : "memory");
        };
        static_for<0, 2>([&](auto KS) {
            admit(KS, IC<0>{});
            static_for<0, 4>([&](auto I) { xrd(KS, I); });
            rdw(IC<2 * KS.value>{}); rdw(IC<2 * KS.value + 1>{});
        });
        STAMP(1);
        u32x4 xv[2];
        static_for<0, 72>([&](auto J) {
            constexpr int j = J.value, ks = j / 4, k = j % 4;
            u32x2 (&d)[4] = t[ks % 3];
            u32x4& x0 = xv[0]; u32x4& x1 = xv[1]; u32x4& on = ones;
            if constexpr (k == 0) {
                asm volatile("s_waitcnt lgkmcnt(%4)" : "+v"(d[0]), "+v"(d[1]), "+v"(d[2]), "+v"(d[3]) : "n"(ks < 17 ? 6 : 0) : "memory");
                xv[0] = (u32x4){d[0][0], d[0][1], d[2][0], d[2][1]};
                xv[1] = (u32x4){d[1][0], d[1][1], d[3][0], d[3][1]};
                asm volatile("s_nop 1" : "+v"(x0), "+v"(x1));
            }
            f32x4& c0 = k < 2 ? acc[0][2 * (k & 1)] : k == 2 ? gram[0] : rsum[0];
            f32x4& c1 = k < 2 ? acc[0][2 * (k & 1) + 1] : k == 2 ? gram[1] : rsum[1];
            u32x4& am0 = k < 2 ? Wf[(2 * ks + k) % 18] : k == 2 ? x0 : on;
            u32x4& am1 = k < 2 ? Wf[(2 * ks + k) % 18] : k == 2 ? x1 : on;
            if constexpr (j < 4) asm volatile(MFMA "%0, %1, %2, 0" : "=&v"(c0) : "v"(am0), "v"(x0));
            else asm volatile(MFMA "%0, %1, %2, %0" : "+v"(c0) : "v"(am0), "v"(x0));
            SB();
            if constexpr (ks < 16) {
                if constexpr (k == 0) admit(IC<ks + 2>{}, J);
                if constexpr (k < 2) { xrd(IC<ks + 2>{}, IC<2 * k>{}); xrd(IC<ks + 2>{}, IC<2 * k + 1>{}); }
                else rdw(IC<2 * ks + 2 + k>{});
            }
            SB();
            if constexpr (j < 4) asm volatile(MFMA "%0, %1, %2, 0" : "=&v"(c1) : "v"(am1), "v"(x1));
            else asm volatile(MFMA "%0, %1, %2, %0" : "+v"(c1) : "v"(am1), "v"(x1));
            SB();
            if constexpr (k >= 2) { Xa[2 * ks + k - 2] = xv[k - 2]; asm volatile("" : "+a"(Xa[2 * ks + k - 2])); }
            if constexpr (x_dmas(j + 1) > x_dmas(j)) {
                constexpr int n = x_dmas(j), p = XI + n / 3;
                static_assert(p < 12 || p == 15 || (3 * (p < 15 ? p - 12 : p - 13) + 2) / 2 <= ks, "slot still holds unread x");
                dma(IC<n % 3>{}, rq_src(p));
                if constexpr (n % 3 == 2 && p < 18) set_m0(rq_dst(p + 1));
            }
            SB();
        });
    }
    float ysc[2];
    u32x4 Xe[2];
    {
        asm volatile("s_waitcnt vmcnt(6)\n\ts_barrier" ::: "memory");
        STAMP(2);
        static_for<36, 48>(rdw);
        rde(IC<0>{});
        set_m0(wl + ring_slot(XW) * 12288);
        SB();
    }
    float sq[2], mean[2];
    auto stats = [&](auto ST) {
        constexpr int st = ST.value >> 1, t = ST.value & 1;
        if constexpr (st == 0) {
            const float a = (lane & 1) ? gram[t][1] : gram[t][0], c = (lane & 1) ? gram[t][3] : gram[t][2];
            float v = (lane & 2) ? c : a;
            const u32 src = ((lane & 15) + 4 * (lane & 12)) * 4;
            float& d = sq[t];
            asm volatile("ds_bpermute_b32 %0, %1, %2" : "=v"(d) : "v"(src), "v"(v) : "memory");
            mean[t] = rsum[t][0] * (1.f / DM);
            asm volatile("" : "+v"(mean[t]));
        }
        if constexpr (st == 1) { asm volatile("" : "+v"(sq[t])); sq[t] = rbf(rbf(fmaxf(fmaf(-mean[t], mean[t], sq[t] * (1.f / DM)), 0.f)) + 1e-5f); asm volatile("" : "+v"(sq[t])); }
        if constexpr (st == 2) { sq[t] = rbf(__builtin_amdgcn_sqrtf(sq[t])); ysc[t] = -1.44269504088896340736f * __builtin_amdgcn_rcpf(sq[t]); asm volatile("" : "+v"(sq[t]), "+v"(ysc[t])); }
        if constexpr (st == 3) {
            const bool lo = lane < 16;
            Xe[t] = (u32x4){lo ? pkbf(mean[t], mean[t]) : 0u, lo ? pkbf(mean[t], sq[t]) : 0u, lo ? pkbf(sq[t], sq[t]) : 0u, 0u};
            asm volatile("" : "+v"(Xe[t]));
        }
    };
    auto corr = [&](auto C) {
        f32x4 (&a)[4] = acc[C.value & 1]; u32x4 (&e)[2] = Ef; u32x4 (&x)[2] = Xe;
        asm volatile(MFMA "%0, %4, %6, %0\n\t" MFMA "%1, %4, %7, %1\n\t" MFMA "%2, %5, %6, %2\n\t" MFMA "%3, %5, %7, %3"
                     : "+v"(a[0]), "+v"(a[1]), "+v"(a[2]), "+v"(a[3]) : "v"(e[0]), "v"(e[1]), "v"(x[0]), "v"(x[1]));
    };

    f32x4 acc2[5][4];
    float yv[16], ev[16];
    u32x4 H[36][2], pollv;
    auto flag_store = [&](u32 level, u32 so) {
        asm volatile("s_mov_b64 exec, 1\n\tbuffer_store_dword %0, off, %1, %2 sc1\n\ts_mov_b64 exec, -1" :: "v"(level), "s"(srd), "s"(so) : "memory");
    };
    auto act = [&](auto C, auto HS, auto PT) {
        constexpr int t = HS.value / 2, e = C.value, pt = PT.value;
        if constexpr (HS.value % 2 == 0) {
            if constexpr (pt != 1 && t < 16) { yv[t] = acc[e & 1][2 * ((t >> 2) & 1) + (t >> 3)][t & 3] * ysc[t >> 3]; asm volatile("" :: "v"(yv[t])); }
            if constexpr (pt != 0 && t >= 2) { yv[t - 2] = yv[t - 2] * ev[t - 2]; asm volatile("" :: "v"(yv[t - 2])); }
            if constexpr (pt != 0 && t < 16) { ev[t] = __builtin_amdgcn_exp2f(yv[t]); asm volatile("" :: "v"(ev[t])); }
        } else {
            if constexpr (pt != 1 && t >= 1 && t < 17) { ev[t - 1] = 1.f + ev[t - 1]; asm volatile("" :: "v"(ev[t - 1])); }
            if constexpr (pt != 0 && t >= 3 && (t & 1)) {
                u32 pk = pkbf(yv[t - 3], yv[t - 2]);
                asm volatile("" :: "v"(pk));
                H[e][(t - 3) / 8][((t - 3) / 2) & 3] = pk;
            }
            if constexpr (pt != 0 && t >= 1 && t < 17) { ev[t - 1] = __builtin_amdgcn_rcpf(ev[t - 1]); asm volatile("" :: "v"(ev[t - 1])); }
        }
    };
    const u32 hown = hfb + 9 * s * 8192;
    const u32 hpeer[3] = {hfb + 9 * ((s + 1) & 3) * 8192, hfb + 9 * ((s + 2) & 3) * 8192, hfb + 9 * ((s + 3) & 3) * 8192};
    auto hload = [&](auto E, auto HF) {
        constexpr int e = E.value, hf = HF.value, k = e - 9;
        const u32 so = hpeer[k % 3] + (k / 3) * 8192, vo = voff; const u32x4 sr = srd;
        u32x4& d = H[e][hf];
        asm volatile("buffer_load_dwordx4 %0, %1, %2, %3 offen offset:%4 sc1" : "=a"(d) : "v"(vo), "s"(sr), "s"(so), "n"(hf * 1024) : "memory");
    };
    const u32 vzero = 0;
    const u32 fso1 = WS_FLAG + (it * 4 + kq) * 64;
    auto poll = [&]() {
        asm volatile("buffer_load_dwordx4 %0, %1, %2, %3 offen sc1" : "=v"(pollv) : "v"(vzero), "s"(srd), "s"(fso1) : "memory");
    };
    auto check = [&](u32 level) {
        asm volatile("" : "+v"(pollv));
#pragma nounroll
        for (u32 spins = 0; spins < (1u << 20); ++spins) {
            const bool bad = pollv[0] < level || pollv[1] < level || pollv[2] < level || pollv[3] < level;
            if (__builtin_amdgcn_ballot_w64(bad) == 0ull) break;
            __builtin_amdgcn_s_sleep(1);
            asm volatile("buffer_load_dwordx4 %0, %1, %2, %3 offen sc1\n\ts_waitcnt vmcnt(0)"
                         : "=v"(pollv) : "v"(vzero), "s"(srd), "s"(fso1) : "memory");
        }
    };
    const u32 pgb = WS_PART + (it * 4 + s) * 98304 + w * 2048;
    const u32 phb = WS_PH + (it * 4 + s) * 16384 + w * 4096;
    auto xfrag = [&](int ks) -> u32 { return xsb + ((ks / 3) * 12 + 3 * w + ks % 3) * 1024; };
    const u32 fso2 = WS_FLAG + 4096 + (it * 4 + s) * 64;
    u32 fv;
    const u32 vfl = (lane & 3) * 4;
    auto poll2 = [&]() {
        asm volatile("buffer_load_dword %0, %1, %2, %3 offen sc1" : "=v"(fv) : "v"(vfl), "s"(srd), "s"(fso2) : "memory");
    };
    auto check2 = [&]() {
        asm volatile("" : "+v"(fv));
#pragma nounroll
        for (u32 spins = 0; spins < (1u << 20); ++spins) {
            if (__builtin_amdgcn_ballot_w64(fv == 0u) == 0ull) break;
            __builtin_amdgcn_s_sleep(1);
            asm volatile("buffer_load_dword %0, %1, %2, %3 offen sc1\n\ts_waitcnt vmcnt(0)" : "=v"(fv) : "v"(vfl), "s"(srd), "s"(fso2) : "memory");
        }
    };
    static_for<G1, GE>([&](auto GG) {
        constexpr int G = GG.value, c = G / 36, g = G % 36, m = G - 324;
        if constexpr (g == 0 && G > 0) STAMP(2 + G / 36);
        if constexpr (s_bar(G)) {
            if constexpr (G % 6 == 0) TW(asm volatile("s_waitcnt vmcnt(%0) lgkmcnt(%1)\n\ts_barrier" :: "n"(bar_wait(G)), "n"(G == G1 ? 8 : 6) : "memory"));
            else TW(asm volatile("s_waitcnt vmcnt(%0)\n\ts_barrier" :: "n"(bar_wait(G)) : "memory"));
        } else if constexpr (G % 6 == 0) asm volatile("s_waitcnt lgkmcnt(%0)" :: "n"(G == 42 ? 4 : G + 6 < GE ? 6 : 0) : "memory");
        u32x4& wf = Wf[G % 18];
        constexpr int sl = G < 324 ? 0 : m_slot(m), e = G < 324 ? 0 : m_ent(m), tl = G < 324 ? (g & 1) : m_half(m);
        constexpr bool first = G < 324 ? g < 2 : m_first(m), own = G >= 324 && ent_own(e);
        if constexpr (G >= 324) {
            if constexpr (ent_first(m) && !own) TW(asm volatile("s_waitcnt vmcnt(%0)" :: "n"(younger(SCH.hgap[e], G)) : "memory"));
        }
        auto mm = [&](auto TT) {
            constexpr int tt = TT.value;
            f32x4& ac = G < 324 ? acc[c & 1][2 * tl + tt] : acc2[sl][2 * tl + tt];
            u32x4& a = wf; u32x4& bo = G < 324 ? Xa[(g & ~1) + tt] : H[e][tt];
            if constexpr (first && own) asm volatile(MFMA "%0, %1, %2, 0" : "=&v"(ac) : "v"(a), "v"(bo));
            else if constexpr (first) asm volatile(MFMA "%0, %1, %2, 0" : "=&v"(ac) : "v"(a), "a"(bo));
            else if constexpr (own) asm volatile(MFMA "%0, %1, %2, %0" : "+v"(ac) : "v"(a), "v"(bo));
            else asm volatile(MFMA "%0, %1, %2, %0" : "+v"(ac) : "v"(a), "a"(bo));
            SB();
        };
        constexpr int hs = G >= 72 && G < 324 && g >= 1 ? (g < 12 ? g - 1 : g) : -1;
        mm(IC<0>{});
        if constexpr (G + 12 < GE) rdw(IC<G + 12>{});
        if constexpr (hs >= 0) act(IC<c - 1>{}, IC<hs>{}, IC<0>{});
        if constexpr (G >= 52 && G < 70) act(IC<0>{}, IC<2 * (G - 52)>{}, IC<2>{});
        if constexpr (G >= 326 && G < 344) act(IC<8>{}, IC<2 * (G - 326)>{}, IC<2>{});
        SB();
        mm(IC<1>{});
        if constexpr (G == 50) { corr(IC<0>{}); SB(); rde(IC<1>{}); }
        if constexpr (G < 324 && g == (c == 1 ? 30 : 11)) {
            if constexpr (c > 1) { act(IC<c - 1>{}, IC<10>{}, IC<1>{}); SB(); }
            corr(IC<c>{}); SB();
            if constexpr (c > 1) act(IC<c - 1>{}, IC<11>{}, IC<2>{});
        } else if constexpr (hs >= 0) act(IC<c - 1>{}, IC<hs>{}, IC<1>{});
        if constexpr (g == (c == 1 ? 32 : 14) && c < 8) rde(IC<c + 1>{});
        if constexpr (SCH.dn[G] >= 0) {
            dma(IC<SCH.dn[G] % 3>{}, wfb + (SCH.dn[G] / 3) * 12288);
            if constexpr (SCH.dn[G] % 3 == 2 && SCH.dn[G] < 161) set_m0(wl + ring_slot(SCH.dn[G] / 3 + 1) * 12288);
        }
        if constexpr (G == 36 || G == 37) stats(IC<G - 36>{});
        if constexpr (G >= 43 && G < 49) stats(IC<G - 41>{});
        if constexpr (G >= 52 && G < 70) act(IC<0>{}, IC<2 * (G - 52) + 1>{}, IC<2>{});
        if constexpr (G >= 326 && G < 344) act(IC<8>{}, IC<2 * (G - 326) + 1>{}, IC<2>{});
        if constexpr (s_hstore(G) >= 0) {
            constexpr int hf = s_hstore(G), cc = c - 1;
            __builtin_amdgcn_raw_buffer_store_b128(H[cc][hf], rs, voff + hf * 1024, hown + cc * 8192, 16);
        }
        if constexpr (s_pub(G) > 0) flag_store(s_pub(G), fso1 + s * 4);
        if constexpr (G == PUB2) flag_store(1u, fso2 + kq * 4);
        if constexpr (G == C1 || G == C2 || G == C3) {
            TW(asm volatile("s_waitcnt vmcnt(%0)" :: "n"(younger(G == C1 ? P1 : G == C2 ? P2 : P3, G)) : "memory"));
            check(G == C1 ? 2 : G == C2 ? 4 : 9);
        }
        if constexpr (G == P1 || G == P2 || G == P3) poll();
        if constexpr (G == P4) poll2();
        if constexpr (SCH.hl[G] >= 0) hload(IC<SCH.hl[G] / 2>{}, IC<SCH.hl[G] % 2>{});
        if constexpr (s_pstore(G) >= 0) {
            constexpr int sg = s_pstore(G) / 2, pt = s_pstore(G) % 2;
            const f32x4 a0 = acc2[sg][2 * pt], a1 = acc2[sg][2 * pt + 1];
            const u32x4 v = {pkbf(a0[0], a0[1]), pkbf(a0[2], a0[3]), pkbf(a1[0], a1[1]), pkbf(a1[2], a1[3])};
            if constexpr (sg == 0) __builtin_amdgcn_raw_buffer_store_b128(v, rs, voff, phb + kq * 1024, 16);
            else __builtin_amdgcn_raw_buffer_store_b128(v, rs, voff + pt * 1024, pgb + ((kq + sg) & 3) * 24576 + (sg - 1) * 8192, 16);
        }
        if constexpr (s_res(G)) {
            constexpr int i = (G - 620) / 2;
            set_m0(wl + RESL + i * 1024);
            const u32 so = xfrag(9 * s + (i < 2 ? 2 * kq + i : 8));
            asm volatile("s_nop 0" ::: "memory");
            dma(IC<0>{}, so);
        }
        SB();
    });
    STAMP(20);

    {
        u32x2 xr[5];
        f32x4 gb[2];
        f32x2 gh;
        const u32 ra = wl + RESL + (4 * (lane >> 4) + ((lane & 15) >> 2)) * 64 + (lane & 3) * 8;
        const u32 ga = lbase + L_EXF + 4608 + (lane >> 4) * 16;
        asm volatile("s_waitcnt vmcnt(1)" ::: "memory");
        asm volatile("ds_read_b64_tr_b16 %0, %5\n\tds_read_b64_tr_b16 %1, %5 offset:32\n\tds_read_b64_tr_b16 %2, %5 offset:1024\n\t"
                     "ds_read_b64_tr_b16 %3, %5 offset:1056\n\tds_read_b64_tr_b16 %4, %6 offset:2048"
                     : "=&v"(xr[0]), "=&v"(xr[1]), "=&v"(xr[2]), "=&v"(xr[3]), "=&v"(xr[4]) : "v"(ra), "v"(ra + (kq >> 1) * 32) : "memory");
        asm volatile("ds_read_b128 %0, %3\n\tds_read_b128 %1, %3 offset:64\n\tds_read_b64 %2, %4 offset:128\n\ts_waitcnt vmcnt(0) lgkmcnt(0)"
                     : "=&v"(gb[0]), "=&v"(gb[1]), "=&v"(gh) : "v"(ga), "v"(ga + (kq & 1) * 8) : "memory");
        asm volatile("" : "+v"(xr[0]), "+v"(xr[1]), "+v"(xr[2]), "+v"(xr[3]), "+v"(xr[4]));
        check2();
        STAMP(21);
        u32x4 P[6];
        u32 Q[4];
        const u32 pin = pgb + kq * 24576;
        asm volatile("buffer_load_dwordx4 %4, %10, %11, %14 offen sc1\n\tbuffer_load_dwordx4 %5, %10, %11, %14 offen offset:1024 sc1\n\t"
                     "buffer_load_dwordx4 %2, %10, %11, %13 offen sc1\n\tbuffer_load_dwordx4 %3, %10, %11, %13 offen offset:1024 sc1\n\t"
                     "buffer_load_dwordx4 %0, %10, %11, %12 offen sc1\n\tbuffer_load_dwordx4 %1, %10, %11, %12 offen offset:1024 sc1\n\t"
                     "buffer_load_dword %6, %15, %11, %16 offen sc1\n\tbuffer_load_dword %7, %15, %11, %16 offen offset:1024 sc1\n\t"
                     "buffer_load_dword %8, %15, %11, %16 offen offset:2048 sc1\n\tbuffer_load_dword %9, %15, %11, %16 offen offset:3072 sc1\n\t"
                     "s_waitcnt vmcnt(0)"
                     : "=&v"(P[0]), "=&v"(P[1]), "=&v"(P[2]), "=&v"(P[3]), "=&v"(P[4]), "=&v"(P[5]), "=&v"(Q[0]), "=&v"(Q[1]), "=&v"(Q[2]), "=&v"(Q[3])
                     : "v"(voff), "s"(srd), "s"(pin), "s"(pin + 8192), "s"(pin + 16384), "v"(voff + kq * 4), "s"(phb) : "memory");
        STAMP(22);
        const auto ro = __builtin_amdgcn_make_buffer_rsrc(out, 0, SEQ * DM * 2, 0x00020000);
        const u32 ov = ((it * 128 + w * 32 + (lane & 15)) * DM + s * 144 + 4 * (lane >> 4)) * 2;
        auto fin2 = [&](float a0, float a1, float g0, float g1, u32 xk) -> u32 {
            const u32 t = pkbf(a0 * -0.69314718055994530942f, a1 * -0.69314718055994530942f);
            const u32 gz = pkbf(bflo(t) * g0, bfhi(t) * g1);
            return pkbf(bflo(xk) + bflo(gz), bfhi(xk) + bfhi(gz));
        };
#pragma unroll
        for (int k = 0; k < 4; ++k) {
            u32x2 o;
#pragma unroll
            for (int j = 0; j < 2; ++j) {
                const int d = 2 * (k & 1) + j;
                const u32 p0 = P[k >> 1][d], p1 = P[2 + (k >> 1)][d], p2 = P[4 + (k >> 1)][d], xk = xr[k][j];
                o[j] = fin2(acc2[4][k][2 * j] + bflo(p0) + bflo(p1) + bflo(p2), acc2[4][k][2 * j + 1] + bfhi(p0) + bfhi(p1) + bfhi(p2),
                            gb[k >> 1][2 * j], gb[k >> 1][2 * j + 1], xk);
            }
            __builtin_amdgcn_raw_buffer_store_b64(o, ro, ov + (k >> 1) * 32, kq * 64 + (k & 1) * (16 * DM * 2), OAUX);
        }
        const u32 oh = fin2(bflo(Q[0]) + bflo(Q[1]) + bflo(Q[2]) + bflo(Q[3]), bfhi(Q[0]) + bfhi(Q[1]) + bfhi(Q[2]) + bfhi(Q[3]),
                            gh[0], gh[1], (kq & 1) ? xr[4][1] : xr[4][0]);
        __builtin_amdgcn_raw_buffer_store_b32(oh, ro, ov + 256, (kq & 1) * 4 + (kq >> 1) * (16 * DM * 2), OAUX);
    }
    asm volatile("k_mlp_end:");
}
#if defined(__HIP_DEVICE_COMPILE__)
#pragma clang attribute pop
#endif

extern "C" void kernel_launch(void* const* d_in, const int* in_sizes, int n_in, void* d_out, int out_size,
                              void* d_ws, size_t ws_size, hipStream_t stream) {
    const u16* x = (const u16*)d_in[0];
    char* ws = (char*)d_ws;
    (void)hipFuncSetAttribute((const void*)k_mlp, hipFuncAttributeMaxDynamicSharedMemorySize, MLP_LDS);
    k_prep<<<NB_PREP, 256, PREP_LDS, stream>>>(x, (const u16*)d_in[1], (const u16*)d_in[2], (const u16*)d_in[3],
                                               (const u16*)d_in[4], (const u16*)d_in[5], (const u16*)d_in[9],
                                               (const u16*)d_in[10], (const u16*)d_in[11], ws);
    k_conv<<<NB_PF + NB_CONV + NB_W1, 256, CONV_LDS, stream>>>((const u16*)d_in[6], (const u16*)d_in[8], ws);
    k_mlp<<<256, 256, MLP_LDS, stream>>>(ws, (u16*)d_out);
}
```

```cpp
#include <hip/hip_runtime.h>
#include <stdint.h>

typedef unsigned short u16;
typedef unsigned int u32;
typedef float f32x2 __attribute__((ext_vector_type(2)));
typedef float f32x4 __attribute__((ext_vector_type(4)));
typedef float f32x16 __attribute__((ext_vector_type(16)));
typedef _Float16 f16x2 __attribute__((ext_vector_type(2)));
typedef _Float16 f16x8 __attribute__((ext_vector_type(8)));
typedef __bf16 bf16x2 __attribute__((ext_vector_type(2)));
typedef __bf16 bf16x8 __attribute__((ext_vector_type(8)));
typedef short s16x4 __attribute__((ext_vector_type(4)));
typedef u32 u32x2 __attribute__((ext_vector_type(2)));
typedef u32 u32x4 __attribute__((ext_vector_type(4)));
#define LAS __attribute__((address_space(3)))
#define DI __device__ __forceinline__
#define RTSTAMP(k)
#define MARK(i)

constexpr int SEQ = 2048, DM = 576, NS = 128, FF = 4608;

constexpr u32 WS_MOD   = 0;
constexpr u32 WS_STAT  = 16384;
constexpr u32 WS_KSC   = 32768;
constexpr u32 WS_FLAG  = 36864;
constexpr u32 WS_EX    = 65536;
constexpr u32 WS_XT    = 1u << 20;
constexpr u32 WS_KR    = WS_XT + 2359296;
constexpr u32 WS_X1F   = WS_KR + 2359296;
constexpr u32 WS_WF    = WS_X1F + 2359296;
constexpr u32 WS_HF    = WS_WF + 10616832;
constexpr u32 WS_PART  = WS_HF + 18874368;
constexpr u32 WS_PH    = WS_PART + 6291456;
constexpr u32 WS_END   = WS_PH + 1048576;

DI float bflo(u32 u) { return __builtin_bit_cast(float, u << 16); }
DI float bfhi(u32 u) { return __builtin_bit_cast(float, u & 0xffff0000u); }
DI float bf2f(u16 v) { return __builtin_bit_cast(float, ((u32)v) << 16); }
DI u32 pkbf(float a, float b) { f32x2 v = {a, b}; return __builtin_bit_cast(u32, __builtin_convertvector(v, bf16x2)); }
DI u32 pkhf(float a, float b) { f32x2 v = {a, b}; return __builtin_bit_cast(u32, __builtin_convertvector(v, f16x2)); }
DI float rbf(float a) { return bflo(pkbf(a, a)); }
template <int CTRL, int RM = 0xf> DI float dppf(float v) {
    return __builtin_bit_cast(float, __builtin_amdgcn_update_dpp(0, __builtin_bit_cast(int, v), CTRL, RM, 0xf, true));
}
DI u32x4 trread2(const char* smem, u32 off0, u32 off1) {
    const u32x2 l = __builtin_bit_cast(u32x2, __builtin_amdgcn_ds_read_tr16_b64_v4i16((LAS s16x4*)(smem + off0)));
    const u32x2 h = __builtin_bit_cast(u32x2, __builtin_amdgcn_ds_read_tr16_b64_v4i16((LAS s16x4*)(smem + off1)));
    return (u32x4){l[0], l[1], h[0], h[1]};
}
DI void st16(char* ws, u32 off, u32x4 v) {
    __builtin_amdgcn_raw_buffer_store_b128(v, __builtin_amdgcn_make_buffer_rsrc(ws, 0, WS_END, 0x00020000), off, 0, 16);
}
DI void st4(char* ws, u32 off, u32 v) {
    __builtin_amdgcn_raw_buffer_store_b32(v, __builtin_amdgcn_make_buffer_rsrc(ws, 0, WS_END, 0x00020000), off, 0, 16);
}
DI float pow2i(int e) { e = e < -126 ? -126 : (e > 127 ? 127 : e); return __builtin_bit_cast(float, (u32)(e + 127) << 23); }
DI int expo(float x) { return (int)((__builtin_bit_cast(u32, x) >> 23) & 255u) - 127; }
DI u32 imgrow(u32 row) { return row * 1184u + (row >> 3) * 128u; }
DI f32x2 cmul(f32x2 z, f32x2 w, f32x2 wp) {
    const f32x2 zx = {z[0], z[0]}, zy = {z[1], z[1]};
    return __builtin_elementwise_fma(zy, wp, zx * w);
}
#define CODE_PREFETCH(sym, part) do { u32 lo_, hi_, len_; \
    asm volatile("s_getpc_b64 s[20:21]\n\ts_add_u32 s20, s20, " #sym "@rel32@lo+4\n\ts_addc_u32 s21, s21, " #sym "@rel32@hi+12\n\t" \
                 "s_mov_b32 %0, s20\n\ts_mov_b32 %1, s21\n\ts_mov_b32 %2, " #sym "_end-" #sym : "=s"(lo_), "=s"(hi_), "=s"(len_) :: "scc", "s20", "s21"); \
    const char* p_ = (const char*)(((unsigned long long)hi_ << 32) | lo_); \
    const u32 o_ = (threadIdx.x + (part) * 256u) * 128u; \
    if (o_ < len_) { u32 t_; asm volatile("global_load_dword %0, %1, off\n\ts_waitcnt vmcnt(0)" : "=v"(t_) : "v"(p_ + o_) : "memory"); } \
    } while (0)

#define DELAY1 20
#define DELAY2 0
#define NAV 26
constexpr int NA = NAV, NB = 36 - NA;
constexpr int NB_PF = 16, NB_LN = 64, NB_GEMV = 108, NB_KGEN = 576, NB_W2 = 144;
constexpr int NB_PREP = NB_PF + NB_LN + NB_GEMV + NB_KGEN + NB_W2;
constexpr int PREP_LDS = 38400;

extern "C" __global__ __launch_bounds__(256, 4) void k_prep(const u16* __restrict__ x, const u16* __restrict__ logA,
                                              const u16* __restrict__ Aim, const u16* __restrict__ Cre,
                                              const u16* __restrict__ Cim, const u16* __restrict__ logdt,
                                              const u16* __restrict__ w2, const u16* __restrict__ cvec,
                                              const u16* __restrict__ wmod, char* __restrict__ ws) {
    extern __shared__ __attribute__((aligned(16))) char smem[];
    RTSTAMP(0);
    const int tid = threadIdx.x, lane = tid & 63;
    const int wv = __builtin_amdgcn_readfirstlane(tid >> 6);
    int b = blockIdx.x;
    if (b >= NB_PF && b < NB_PF + NB_KGEN + NB_LN + NB_GEMV) b = b < NB_PF + NB_KGEN ? b + NB_LN + NB_GEMV : b - NB_KGEN;
    if (b < NB_PF) {
        if (b < 8) CODE_PREFETCH(k_prep, 0); else CODE_PREFETCH(k_conv, 0);
        if (b < 2) st16(ws, WS_FLAG + b * 4096 + tid * 16, (u32x4){0u, 0u, 0u, 0u});
    } else if ((b -= NB_PF) < NB_LN) {
        const int t0 = b * 32, tok = tid >> 3, sub = tid & 7;
        u32x4 v[9];
#pragma unroll
        for (int i = 0; i < 9; ++i) v[i] = *(const u32x4*)(x + (size_t)(t0 + tok) * DM + (sub + 8 * i) * 8);
        MARK(0);
#pragma unroll
        for (int i = 0; i < 9; ++i) *(u32x4*)(smem + imgrow(tok) + (sub + 8 * i) * 16) = v[i];
        __syncthreads();
        const int g = lane >> 4, qq = (lane & 15) >> 2, p = lane & 3;
#pragma unroll
        for (int i = 0; i < 9; ++i) {
            const int cb = wv * 9 + i;
            st16(ws, WS_XT + b * 36864 + cb * 1024 + (lane & 15) * 64 + g * 16,
                 trread2(smem, imgrow(8 * g + qq) + cb * 32 + p * 8, imgrow(8 * g + 4 + qq) + cb * 32 + p * 8));
        }
        MARK(1);
        float s = 0.f;
#pragma unroll
        for (int i = 0; i < 9; ++i)
#pragma unroll
            for (int k = 0; k < 4; ++k) s += bflo(v[i][k]) + bfhi(v[i][k]);
        s += dppf<0xB1>(s); s += dppf<0x4E>(s); s += dppf<0x141>(s);
        const float mean = s * (1.f / DM);
        float q = 0.f;
#pragma unroll
        for (int i = 0; i < 9; ++i)
#pragma unroll
            for (int k = 0; k < 4; ++k) {
                const float a = bflo(v[i][k]) - mean, c = bfhi(v[i][k]) - mean;
                q = fmaf(a, a, q); q = fmaf(c, c, q);
            }
        q += dppf<0xB1>(q); q += dppf<0x4E>(q); q += dppf<0x141>(q);
        if (sub == 0) st4(ws, WS_STAT + (t0 + tok) * 4, pkbf(mean, __builtin_amdgcn_sqrtf(rbf(rbf(q * (1.f / DM)) + 1e-5f))));
    } else if ((b -= NB_LN) < NB_GEMV) {
        const int seg = tid & 3, rg = tid >> 2;
        u32x4 v[9]; u16 cv[9];
#pragma unroll
        for (int i = 0; i < 9; ++i) {
            const int row = rg + 64 * i;
            cv[i] = cvec[row];
            v[i] = *(const u32x4*)(wmod + (size_t)row * (6 * DM) + b * 32 + seg * 8);
        }
        MARK(0);
        float acc[8];
#pragma unroll
        for (int k = 0; k < 8; ++k) acc[k] = 0.f;
#pragma unroll
        for (int i = 0; i < 9; ++i) {
            const float c = bf2f(cv[i]);
            const float sl = rbf(c * __builtin_amdgcn_rcpf(1.f + __builtin_amdgcn_exp2f(c * -1.44269504088896340736f)));
#pragma unroll
            for (int k = 0; k < 4; ++k) {
                acc[2 * k] = fmaf(sl, bflo(v[i][k]), acc[2 * k]);
                acc[2 * k + 1] = fmaf(sl, bfhi(v[i][k]), acc[2 * k + 1]);
            }
        }
#pragma unroll
        for (int k = 0; k < 8; ++k) { acc[k] += dppf<0x114>(acc[k]); acc[k] += dppf<0x118>(acc[k]); }
        float* red = (float*)smem;
        if ((lane & 15) >= 12) {
            *(f32x4*)(red + (tid >> 4) * 32 + seg * 8) = (f32x4){acc[0], acc[1], acc[2], acc[3]};
            *(f32x4*)(red + (tid >> 4) * 32 + seg * 8 + 4) = (f32x4){acc[4], acc[5], acc[6], acc[7]};
        }
        __syncthreads();
        MARK(1);
        if (tid < 32) {
            float t = 0.f;
#pragma unroll
            for (int i = 0; i < 16; ++i) t += red[i * 32 + tid];
            st4(ws, WS_MOD + (b * 32 + tid) * 4, __builtin_bit_cast(u32, rbf(t)));
        }
    } else if ((b -= NB_GEMV) < NB_KGEN) {
        const int d = b, hn = wv & 1, grp = wv >> 1;
        const int n = hn * 64 + lane, n2 = n ^ 64;
        const u16 la_ = logA[d * NS + n], ai_ = Aim[d * NS + n], cr_ = Cre[d * NS + n], ci_ = Cim[d * NS + n];
        const u16 cr2_ = Cre[d * NS + n2], ci2_ = Cim[d * NS + n2], ld_ = logdt[d];
        u32 ta[8];
#pragma unroll
        for (int j = 0; j < 8; j += 2) {
            const float l0 = (float)(2047 - tid * 8 - j);
            const u32 pk = pkbf(l0, l0 - 1.f);
            const float p0 = bflo(pk), p1 = bfhi(pk);
            ta[j] = (u32)fminf(4.f * p0, p0 + 1536.f); ta[j + 1] = (u32)fminf(4.f * p1, p1 + 1536.f);
        }
        asm volatile("" : "+v"(ta[0]), "+v"(ta[1]), "+v"(ta[2]), "+v"(ta[3]), "+v"(ta[4]), "+v"(ta[5]), "+v"(ta[6]), "+v"(ta[7]));
        MARK(0);
        const float cr = bf2f(cr_), ci = bf2f(ci_);
        const float dt = rbf(__builtin_amdgcn_exp2f(bf2f(ld_) * 1.44269504088896340736f));
        const float zr = -rbf(__builtin_amdgcn_exp2f(bf2f(la_) * 1.44269504088896340736f)) * dt, zi = bf2f(ai_) * dt;
        float bs = fabsf(cr) + fabsf(ci) + fabsf(bf2f(cr2_)) + fabsf(bf2f(ci2_));
        bs += dppf<0xB1>(bs); bs += dppf<0x4E>(bs); bs += dppf<0x141>(bs); bs += dppf<0x140>(bs);
        bs += dppf<0x142, 0xa>(bs); bs += dppf<0x143, 0xc>(bs);
        bs = __builtin_bit_cast(float, __builtin_amdgcn_readlane(__builtin_bit_cast(int, bs), 63));
        const int se = 14 - expo(2.f * dt * bs * 1.0001f);
        const float scale = pow2i(se > 100 ? 100 : se);
        const float zr2 = zr * 1.44269504088896340736f;
        auto cexp = [&](float p) -> f32x2 {
            const float mag = __builtin_amdgcn_exp2f(zr2 * p);
            const float ang = zi * p;
            const float rr = __builtin_rintf(ang * 0.15915494309189535f);
            float fr = fmaf(ang, 0.15915494309189535f, -rr);
            fr = fmaf(ang, 6.4206382e-9f, fr);
            return (f32x2){mag * __builtin_amdgcn_cosf(fr), mag * __builtin_amdgcn_sinf(fr)};
        };
        const float p1 = grp ? 4.f : 1.f;
        const f32x2 f1 = cexp(p1), c1 = cexp(16.f * p1), s0 = cexp(grp ? 512.f : 0.f), s1 = cexp(256.f * p1);
        const f32x2 fw = {f1[0], -f1[1]}, fwp = {f1[1], f1[0]};
        const f32x2 cwp = {-c1[1], c1[0]};
        const float es = 2.f * dt * scale;
        f32x2 za = cmul((f32x2){es * cr, es * ci}, s0, (f32x2){-s0[1], s0[0]});
        f32x2 zb = cmul(za, s1, (f32x2){-s1[1], s1[0]});
        f32x2 zf = {1.f, 0.f};
        char* tab = smem + wv * 8704;
        u32 keep[16];
#pragma unroll
        for (int k = 0; k < 16; ++k) {
            *(u32*)(tab + k * 272 + lane * 4) = pkhf(zf[0], zf[1]);
            *(u32*)(tab + (16 + k) * 272 + lane * 4) = pkhf(za[0], za[1]);
            keep[k] = pkhf(zb[0], zb[1]);
            zf = cmul(zf, fw, fwp); za = cmul(za, c1, cwp); zb = cmul(zb, c1, cwp);
        }
        const int r16 = lane & 15, g = lane >> 4;
        f16x8 fb[4];
        f32x4 acc0 = {0.f, 0.f, 0.f, 0.f}, acc1 = {0.f, 0.f, 0.f, 0.f};
        __builtin_amdgcn_fence(__ATOMIC_RELEASE, "wavefront");
        __builtin_amdgcn_wave_barrier();
#pragma unroll
        for (int ks = 0; ks < 4; ++ks) {
            fb[ks] = *(const f16x8*)(tab + r16 * 272 + ks * 64 + g * 16);
            const f16x8 fa = *(const f16x8*)(tab + (16 + r16) * 272 + ks * 64 + g * 16);
            acc0 = __builtin_amdgcn_mfma_f32_16x16x32_f16(fb[ks], fa, acc0, 0, 0, 0);
        }
        __builtin_amdgcn_fence(__ATOMIC_RELEASE, "wavefront");
        __builtin_amdgcn_wave_barrier();
#pragma unroll
        for (int k = 0; k < 16; ++k) *(u32*)(tab + (16 + k) * 272 + lane * 4) = keep[k];
        __builtin_amdgcn_fence(__ATOMIC_RELEASE, "wavefront");
        __builtin_amdgcn_wave_barrier();
#pragma unroll
        for (int ks = 0; ks < 4; ++ks) {
            const f16x8 fa = *(const f16x8*)(tab + (16 + r16) * 272 + ks * 64 + g * 16);
            acc1 = __builtin_amdgcn_mfma_f32_16x16x32_f16(fb[ks], fa, acc1, 0, 0, 0);
        }
        MARK(1);
        __builtin_amdgcn_fence(__ATOMIC_RELEASE, "wavefront");
        __builtin_amdgcn_wave_barrier();
        *(f32x4*)(tab + r16 * 64 + g * 16) = acc0;
        *(f32x4*)(tab + 1024 + r16 * 64 + g * 16) = acc1;
        __syncthreads();
        float kv[8];
#pragma unroll
        for (int j = 0; j < 8; ++j) {
            const u32 o = ta[j] + (ta[j] >= 2048u ? 2u * 8704u - 2048u : 0u);
            kv[j] = *(const float*)(smem + o) + *(const float*)(smem + o + 8704);
        }
        MARK(2);
        st16(ws, WS_KR + d * 4096 + tid * 16, (u32x4){pkhf(kv[0], kv[1]), pkhf(kv[2], kv[3]), pkhf(kv[4], kv[5]), pkhf(kv[6], kv[7])});
        if (tid == 0) st4(ws, WS_KSC + d * 4, __builtin_bit_cast(u32, pow2i(se > 100 ? -100 : -se)));
    } else {
        b -= NB_KGEN;
        const u16* src = w2 + (size_t)b * 32 * DM;
        if (DELAY1 > 0) __builtin_amdgcn_s_sleep(DELAY1);
        const int tok = tid >> 3, sub = tid & 7;
        u32x4 v[9];
#pragma unroll
        for (int i = 0; i < 9; ++i) v[i] = *(const u32x4*)(src + tok * DM + (sub + 8 * i) * 8);
        MARK(0);
#pragma unroll
        for (int i = 0; i < 9; ++i) *(u32x4*)(smem + imgrow(tok) + (sub + 8 * i) * 16) = v[i];
        __syncthreads();
        const int kq = b / 36, kk = b - kq * 36, sp = kk / 9, c = kk - sp * 9;
        const int rel = (sp - wv) & 3;
        const int e = rel == 0 ? c : 9 + c * 3 + (rel - 1);
        const int r0 = 4 * (lane >> 4) + ((lane & 15) >> 2), p = lane & 3;
#pragma unroll
        for (int i = 0; i < 9; ++i) {
            const int sg = (i + 1) >> 1, j = i == 0 ? 0 : (i - 1) & 1;
            const int T = 9 * wv + (sg == 0 ? 8 : 2 * ((kq + sg) & 3) + j);
            const int m = sg == 4 ? 252 + 2 * e + j
                        : e < NA ? 7 * e + i
                        : sg == 0 ? 7 * NA + 2 * NB + (e - NA) : 7 * NA + (sg == 1 ? 0 : (2 * sg - 1) * NB) + (e - NA) * 2 + j;
            const u32 col = T * 32 + p * 8;
            st16(ws, WS_WF + ((kq * 4 + wv) * 648 + 324 + m) * 1024 + lane * 16, trread2(smem, imgrow(r0) + col, imgrow(r0 + 16) + col));
        }
    }
    asm volatile("k_prep_end:");
}

constexpr int NB_CONV = 576, NB_W1 = 144;
constexpr int CONV_LDS = 36864 + 4096;
constexpr int UCP = 4640, KB_OFF = 4 * UCP;

extern "C" __global__ __launch_bounds__(256, 4) void k_conv(const u16* __restrict__ dskip, const u16* __restrict__ w1,
                                              char* __restrict__ ws) {
    extern __shared__ __attribute__((aligned(16))) char smem[];
    const int tid = threadIdx.x, lane = tid & 63;
    const int wv = __builtin_amdgcn_readfirstlane(tid >> 6);
    RTSTAMP(1);
    const float* mod = (const float*)(ws + WS_MOD);
    int b = blockIdx.x;
    if (b >= NB_PF) b = b < NB_PF + NB_CONV ? b + NB_W1 : b - NB_CONV;
    if (b < NB_PF) {
        CODE_PREFETCH(k_mlp, b >> 3);
    } else if ((b -= NB_PF) < NB_W1) {
        const int q = b / 9, c = b - q * 9, f0 = b * 32;
        if (DELAY2 > 0) __builtin_amdgcn_s_sleep(DELAY2);
        const int seg = tid & 3, rg = tid >> 2;
        u32x4 v[9]; float sb[9], cb[9];
#pragma unroll
        for (int i = 0; i < 9; ++i) {
            const int row = rg + 64 * i;
            v[i] = *(const u32x4*)(w1 + (size_t)row * FF + f0 + seg * 8);
            sb[i] = mod[3 * DM + row]; cb[i] = mod[4 * DM + row];
        }
        MARK(0);
        float S[8], B[8];
#pragma unroll
        for (int k = 0; k < 8; ++k) { S[k] = 0.f; B[k] = 0.f; }
#pragma unroll
        for (int i = 0; i < 9; ++i) {
            const float c1 = rbf(1.f + cb[i]);
            u32x4 o;
#pragma unroll
            for (int k = 0; k < 4; ++k) {
                const float lo = bflo(v[i][k]), hi = bfhi(v[i][k]);
                o[k] = pkbf(lo * c1, hi * c1);
                S[2 * k] += bflo(o[k]); S[2 * k + 1] += bfhi(o[k]);
                B[2 * k] = fmaf(sb[i], lo, B[2 * k]); B[2 * k + 1] = fmaf(sb[i], hi, B[2 * k + 1]);
            }
            *(u32x4*)(smem + ((tid ^ ((tid >> 3) & 2)) + 256 * i) * 16) = o;
        }
#pragma unroll
        for (int k = 0; k < 8; ++k) {
            S[k] += dppf<0x114>(S[k]); S[k] += dppf<0x118>(S[k]);
            B[k] += dppf<0x114>(B[k]); B[k] += dppf<0x118>(B[k]);
        }
        float* red = (float*)(smem + 36864);
        if ((lane & 15) >= 12) {
            float* r = red + (tid >> 4) * 64 + seg * 8;
            *(f32x4*)(r) = (f32x4){S[0], S[1], S[2], S[3]}; *(f32x4*)(r + 4) = (f32x4){S[4], S[5], S[6], S[7]};
            *(f32x4*)(r + 32) = (f32x4){B[0], B[1], B[2], B[3]}; *(f32x4*)(r + 36) = (f32x4){B[4], B[5], B[6], B[7]};
        }
        __syncthreads();
        MARK(1);
        const int g = lane >> 4, qq = (lane & 15) >> 2, p = lane & 3;
#pragma unroll
        for (int i = 0; i < 9; ++i) {
            const int s = wv * 9 + i;
            const u32 a = (32 * (s >> 1) + 4 * g + qq) * 64 + (((s & 1) ^ (g & 1)) * 32) + p * 8;
            st16(ws, WS_WF + (q * 648 + c * 36 + s) * 1024 + lane * 16, trread2(smem, a, a + 1024));
        }
        if (wv == 0) {
            float t = 0.f;
#pragma unroll
            for (int i = 0; i < 16; ++i) t += red[i * 64 + lane];
            const float B_ = __builtin_bit_cast(float, __builtin_amdgcn_ds_bpermute((lane ^ 32) * 4, __builtin_bit_cast(int, t)));
            const float S_ = -t;
            const float s1 = rbf(S_), s2 = rbf(S_ - s1), s3 = S_ - s1 - s2;
            const float b1 = rbf(B_), b2 = rbf(B_ - b1), b3 = B_ - b1 - b2;
            if (lane < 32) st16(ws, WS_EX + q * 8192 + (c * 32 + lane) * 16, (u32x4){pkbf(s1, s2), pkbf(s3, b1), pkbf(b2, b3), 0u});
        } else if (wv == 1 && c == 0 && lane < 48) {
            const int d = (q & 3) * 144 + (lane < 32 ? (q >> 2) * 32 + lane : 96 + lane);
            st4(ws, WS_EX + q * 8192 + 4608 + lane * 4, __builtin_bit_cast(u32, mod[5 * DM + d]));
        }
    } else {
        const int d = b - NB_W1;
        const int tp = tid > 0 ? tid * 8 - 4 : 0;
        const char* xb = ws + WS_XT + d * 64;
        const u32x4 x0 = *(const u32x4*)(xb + (tid >> 2) * 36864 + (tid & 3) * 16);
        const u32x2 xm = *(const u32x2*)(xb + (tp >> 5) * 36864 + (tp & 31) * 2);
        u32x4 st[3];
        st[0] = *(const u32x4*)(ws + WS_STAT + tp * 4);
        st[1] = *(const u32x4*)(ws + WS_STAT + tid * 32);
        st[2] = *(const u32x4*)(ws + WS_STAT + tid * 32 + 16);
        const u32x4 kv = *(const u32x4*)(ws + WS_KR + d * 4096 + tid * 16);
        const float sa = mod[d], ca = mod[DM + d], ga = mod[2 * DM + d];
        const float dsk = bf2f(dskip[d]);
        const float kinv = ((const float*)(ws + WS_KSC))[d];
        {
            const u32x4 z = {0u, 0u, 0u, 0u};
            if (tid < 96) *(u32x4*)(smem + (tid / 24) * UCP + (tid % 24) * 16) = z;
            else if (tid < 104) *(u32x4*)(smem + KB_OFF + 4096 + (tid - 96) * 16) = z;
        }
        MARK(0);
        const float c1 = rbf(1.f + ca);
        const int he = expo(64.f * fabsf(c1) + 2.f * fabsf(sa)) - 14;
        const float hs = pow2i(-he), oscale = pow2i(he) * kinv;
        const u32 xw[6] = {xm[0], xm[1], x0[0], x0[1], x0[2], x0[3]};
        u32 P[6], hb[4];
#pragma unroll
        for (int k = 0; k < 6; ++k) {
            const u32 s0 = st[k >> 1][(k & 1) * 2], s1 = st[k >> 1][(k & 1) * 2 + 1];
            const float n0 = (bflo(xw[k]) - bflo(s0)) * __builtin_amdgcn_rcpf(bfhi(s0));
            const float n1 = (bfhi(xw[k]) - bflo(s1)) * __builtin_amdgcn_rcpf(bfhi(s1));
            const u32 t = pkbf(fmaf(n0, c1, sa), fmaf(n1, c1, sa));
            if (k >= 2) hb[k - 2] = t;
            P[k] = pkhf(bflo(t) * hs, bfhi(t) * hs);
        }
        if (tid == 0) { P[0] = 0u; P[1] = 0u; }
        u32 Q[5];
#pragma unroll
        for (int k = 0; k < 5; ++k) Q[k] = __builtin_amdgcn_alignbit(P[k + 1], P[k], 16);
        {
            char* ub = smem + 384 + tid * 16;
            *(u32x4*)(ub) = (u32x4){Q[0], Q[1], Q[2], Q[3]};
            *(u32x4*)(ub + UCP) = (u32x4){P[1], P[2], P[3], P[4]};
            *(u32x4*)(ub + 2 * UCP) = (u32x4){Q[1], Q[2], Q[3], Q[4]};
            *(u32x4*)(ub + 3 * UCP) = (u32x4){P[2], P[3], P[4], P[5]};
            *(u32x4*)(smem + KB_OFF + tid * 16) = kv;
        }
        __syncthreads();
        MARK(1);
        const int c = lane & 15, g = lane >> 4;
        f32x4 acc[8];
#pragma unroll
        for (int j = 0; j < 8; ++j) acc[j] = (f32x4){0.f, 0.f, 0.f, 0.f};
        {
            const u32 lb = (u32)(uintptr_t)(LAS char*)smem;
            const u32 ka = lb + KB_OFF + 2 * (2076 - 4 * c + 8 * g - 64 * wv) - 3584 - 64;
            const u32 ua = lb + (c & 3) * UCP + 2 * (8 * g + 64 * (c >> 2) - 64 * wv + 192);
            u32x2 kl[2][8], kh[2][8]; u32x4 uf[2][8];
            auto rd = [&](int rr, int n) {
                asm volatile("ds_read_b64 %0, %3 offset:%5\n\tds_read_b64 %1, %3 offset:%6\n\tds_read_b128 %2, %4 offset:%7"
                             : "=&v"(kl[rr][n]), "=&v"(kh[rr][n]), "=&v"(uf[rr][n]) : "v"(ka), "v"(ua),
                               "n"((7 - n) * 512 + (1 - rr) * 64), "n"((7 - n) * 512 + (1 - rr) * 64 + 8), "n"(n * 512 + (1 - rr) * 64) : "memory");
            };
            auto mm = [&](int rr, int i, int dl) {
                const u32x4 k = {kl[rr][i][0], kl[rr][i][1], kh[rr][i][0], kh[rr][i][1]};
                acc[i + dl] = __builtin_amdgcn_mfma_f32_16x16x32_f16(__builtin_bit_cast(f16x8, uf[rr][dl]), __builtin_bit_cast(f16x8, k), acc[i + dl], 0, 0, 0);
            };
            auto shell = [&](int rr, int n) {
#pragma unroll
                for (int o = 0; o <= n && o + n < 8; ++o) { mm(rr, n, o); if (o < n) mm(rr, o, n); }
            };
            auto pin = [&](int rr, int n) { asm volatile("" : "+v"(kl[rr][n]), "+v"(kh[rr][n]), "+v"(uf[rr][n])); };
            rd(0, 0); rd(0, 7); rd(0, 1); rd(0, 6); rd(0, 2); rd(0, 5); rd(0, 3); rd(0, 4);
#pragma unroll
            for (int n = 7; n >= 0; --n) {
                if (n >= 4) { asm volatile("s_waitcnt lgkmcnt(%0)" :: "n"(n == 7 ? 15 : 6 * (n - 4) + 3 * (7 - n)) : "memory"); pin(0, n); pin(0, 7 - n); }
                shell(0, n);
                __builtin_amdgcn_sched_barrier(0);
                rd(1, 7 - n);
            }
#pragma unroll
            for (int n = 0; n < 8; ++n) {
                asm volatile("s_waitcnt lgkmcnt(%0)" :: "n"(n < 3 ? 15 : 3 * (7 - n)) : "memory");
                pin(1, n);
                shell(1, n);
                __builtin_amdgcn_sched_barrier(0);
            }
        }
        __syncthreads();
        MARK(2);
#pragma unroll
        for (int j = 0; j < 8; ++j) *(f32x4*)(smem + wv * 8192 + j * 1024 + lane * 16) = acc[j];
        __syncthreads();
        f32x4 y0 = *(const f32x4*)(smem + tid * 32), y1 = *(const f32x4*)(smem + tid * 32 + 16);
#pragma unroll
        for (int w = 1; w < 4; ++w) {
            y0 += *(const f32x4*)(smem + w * 8192 + tid * 32);
            y1 += *(const f32x4*)(smem + w * 8192 + tid * 32 + 16);
        }
        const float y[8] = {y0[0], y0[1], y0[2], y0[3], y1[0], y1[1], y1[2], y1[3]};
        u32x4 o;
#pragma unroll
        for (int k = 0; k < 4; ++k) {
            const u32 yb = pkbf(y[2 * k] * oscale, y[2 * k + 1] * oscale);
            const u32 sk = pkbf(bflo(hb[k]) * dsk, bfhi(hb[k]) * dsk);
            const u32 z = pkbf(bflo(yb) + bflo(sk), bfhi(yb) + bfhi(sk));
            const u32 gz = pkbf(bflo(z) * ga, bfhi(z) * ga);
            o[k] = pkbf(bflo(x0[k]) + bflo(gz), bfhi(x0[k]) + bfhi(gz));
        }
        MARK(3);
        st16(ws, WS_X1F + (tid >> 4) * 147456 + ((d / 48) * 12 + ((tid >> 2) & 3) * 3 + (d >> 4) % 3) * 1024 + (d & 15) * 64 + (tid & 3) * 16, o);
    }
    asm volatile("k_conv_end:");
}
constexpr u32 L_EXF = 147456;
constexpr int MLP_LDS = L_EXF + 12288;
constexpr int GE = 648;

#if defined(__HIP_DEVICE_COMPILE__)
#pragma clang attribute push(__attribute__((target("no-packed-fp32-ops"))), apply_to = function)
#endif
template <int N> struct IC { static constexpr int value = N; };
template <int A, int B, class F> DI void static_for(F&& f) {
    if constexpr (B - A == 1) f(IC<A>{});
    else if constexpr (A < B) { static_for<A, (A + B) / 2>(f); static_for<(A + B) / 2, B>(f); }
}
constexpr int imin(int a, int b) { return a < b ? a : b; }
constexpr int XW = 6;
#define XSP 7
#define XIV 8
constexpr int XI = XIV;
constexpr int x_dmas(int j) { return j <= 0 ? 0 : imin((4 * j + XSP - 1) / XSP, 3 * (19 - XI)); }
constexpr int G1 = 36;
#define PLG 614
constexpr int P1 = 36 * 5 + 7, P2 = 36 * 7 + 7, P3 = 400;
#define PREF 0
#define CD1 34
#define CD3 30
#define QW3 8
constexpr int C1 = P1 + CD1, C2 = P2 + CD1, C3 = P3 + CD3;
constexpr bool s_poll(int G) { return G == P1 || G == P2 || G == P3; }
#define PB2 606
#define PL4 640
constexpr int PUB2 = PB2, P4 = PL4;
constexpr int HS0 = 22, HS1 = 35;
constexpr int s_hstore(int G) { return G < 72 ? (G == 64 ? 0 : G == 71 ? 1 : -1) : G < 324 ? (G % 36 == HS0 ? 0 : G % 36 == HS1 ? 1 : -1) : G == 338 ? 0 : G == 346 ? 1 : -1; }
constexpr int B0 = 324 + 7 * NA;
constexpr int slot_end(int sg) { return B0 - 1 + (sg == 1 ? 2 : sg == 0 ? 3 : 2 * sg + 1) * NB; }
constexpr int s_pstore(int G) {
    for (int sg = 0; sg < 4; ++sg) { if (G == slot_end(sg) + 4) return 2 * sg; if (G == slot_end(sg) + 6 && sg > 0) return 2 * sg + 1; }
    return -1;
}
constexpr bool s_res(int G) { return G == 620 || G == 622 || G == 624; }
constexpr int s_pub(int G) { return G == 36 * 3 + 24 ? 2 : G == 36 * 5 + 24 ? 4 : G == 372 ? 9 : 0; }
constexpr int m_slot(int m) { return m < 7 * NA ? (m % 7 + 1) / 2 : m < 7 * NA + 2 * NB ? 1 : m < 7 * NA + 3 * NB ? 0 : m < 252 ? 2 + (m - 7 * NA - 3 * NB) / (2 * NB) : 4; }
constexpr int m_ent(int m) { return m < 7 * NA ? m / 7 : m < 7 * NA + 2 * NB ? NA + (m - 7 * NA) / 2 : m < 7 * NA + 3 * NB ? NA + (m - 7 * NA - 2 * NB)
                                  : m < 252 ? NA + ((m - 7 * NA - 3 * NB) % (2 * NB)) / 2 : (m - 252) / 2; }
constexpr int m_half(int m) { return m < 7 * NA ? (m % 7 == 0 ? 0 : (m % 7 - 1) & 1) : m < 7 * NA + 2 * NB ? (m - 7 * NA) & 1 : m < 7 * NA + 3 * NB ? 0
                                   : m < 252 ? (m - 7 * NA - 3 * NB) & 1 : m & 1; }
constexpr bool m_first(int m) { return m < 7 || m == 252 || m == 253; }
constexpr bool ent_first(int m) { return m < 7 * NA ? m % 7 == 0 : (m < 7 * NA + 2 * NB && ((m - 7 * NA) & 1) == 0); }
constexpr bool ent_own(int e) { return e < 9; }
constexpr int first_use(int e) { return e < NA ? 324 + 7 * e : B0 + 2 * (e - NA); }
constexpr bool s_ring(int G) { return G + 12 < GE && (G < 100 ? G % 12 == 0 : G < 490 ? G % 36 == 24 : G % 18 == 6); }
constexpr int ring_slot(int grp) { const int u = (grp / 3) & 3; return (u == 0 ? 0 : u == 1 ? 6 : u == 2 ? 9 : 3) + grp % 3; }
constexpr int need_bar(int grp) { int g = 12 * grp - 12; while (g > 0 && !s_ring(g)) --g; return g; }
struct Sched {
    int dn[GE];
    int dgap[162];
    int hl[GE];
    int pre[GE + 1];
    int hgap[36];
    bool ok;
    constexpr Sched() : dn(), dgap(), hl(), pre(), hgap(), ok(true) {
        int nd = 3 * XW, nh = 0, last2 = -99, lastbar = 24;
        for (int n = 0; n < 162; ++n) dgap[n] = -1;
        for (int e = 0; e < 36; ++e) hgap[e] = -1;
        for (int G = 0; G < GE; ++G) {
            dn[G] = -1; hl[G] = -1;
            int n = 0;
            if (G >= G1) {
                if (s_ring(G)) lastbar = G;
                const bool fixed = s_hstore(G) >= 0 || s_pstore(G) >= 0 || s_res(G);
                const bool quiet = (G >= P1 && G < P1 + 17) || (G >= P2 && G < P2 + 17) || (G >= P3 && G < P3 + QW3);
                const int sp2 = G < 324 ? 4 : 5;
                if (fixed) last2 = 2 * G;
                else if (!quiet && 2 * G - last2 >= sp2) {
                    int dd = 1 << 20, dh = 1 << 20;
                    if (nd < 162 && (nd / 3 < 12 || (nd / 3 - 11) * 12 <= lastbar + 6)) dd = need_bar(nd / 3) - 40;
                    const int lim = G > C3 ? 54 : G > C2 ? 24 : G > C1 ? 12 : 0;
                    if (nh < lim) dh = first_use(9 + nh / 2) - 45;
                    if (dd < (1 << 20) || dh < (1 << 20)) {
                        if (dd <= dh) { dn[G] = nd; dgap[nd] = G; if (need_bar(nd / 3) <= G) ok = false; ++nd; }
                        else { hl[G] = 18 + nh; hgap[9 + nh / 2] = G; if (first_use(9 + nh / 2) <= G) ok = false; ++nh; }
                        last2 = last2 + sp2 > 2 * G - 1 ? last2 + sp2 : 2 * G - (G < 324 ? 0 : 1);
                        n = 1;
                    }
                }
                n += fixed + s_poll(G) + (G == P4) + (s_pub(G) > 0) + (G == PUB2);
            }
            pre[G + 1] = pre[G] + n;
        }
        if (nd != 162 || nh != 54) ok = false;
    }
};
constexpr Sched SCH{};
static_assert(SCH.ok, "schedule misses a deadline");
constexpr int younger(int a, int b) { return imin(SCH.pre[b] - SCH.pre[a + 1], 63); }
constexpr bool s_bar(int G) { return G >= G1 && (s_ring(G) || s_pub(G) > 0 || G == PUB2); }
constexpr int bar_wait(int G) {
    int n = 63;
    if (s_ring(G)) { int grp = 53; while (need_bar(grp) > G) --grp; n = G == 36 ? 3 : younger(SCH.dgap[3 * grp + 2], G); }
    if (G == PUB2) n = imin(n, younger(slot_end(3) + 6, G));
    if (s_pub(G) > 0) n = imin(n, younger(G == 372 ? 346 : 36 * s_pub(G) + HS1, G));
    return n;
}
constexpr int frag_lds(int f) { return ring_slot(f / 12) * 12288 + (f % 12) * 1024; }

constexpr u32 RESL = ring_slot(45) * 12288;
static_assert(SCH.dgap[161] < 620, "M0");
#define OAUX 0
#define PG0 60
#define PG1 152
#define PG2 244
#define PG3 336
#define PG4 460
#define PG5 581
#define PADS 1
constexpr int pad_at(int G) { return G == PG0 ? 0 : G == PG1 ? 1 : G == PG2 ? 2 : G == PG3 ? 3 : G == PG4 ? 4 : G == PG5 ? 5 : -1; }
#define SB() __builtin_amdgcn_sched_barrier(0)
#define STAMP(i) do {} while (0)
#define TW(code) do { code; } while (0)
#define MFMA "v_mfma_f32_16x16x32_bf16 "

extern "C" __global__ __launch_bounds__(256, 1) void k_mlp(char* __restrict__ ws, u16* __restrict__ out) {
    extern __shared__ __attribute__((aligned(16))) char smem[];
    const int tid = threadIdx.x, lane = tid & 63;
    const int w = __builtin_amdgcn_readfirstlane(tid >> 6);
    const int b = blockIdx.x, xcd = b & 7, rr = b >> 3;
#define MAPV 1
    const int s = rr & 3;
    const int kq = MAPV == 0 ? xcd >> 1 : MAPV == 1 ? 2 * (xcd >> 2) + ((rr >> 2) & 1) : (rr >> 2) & 3;
    const int it = MAPV == 0 ? 8 * (xcd & 1) + (rr >> 2) : MAPV == 1 ? 4 * (xcd & 3) + (rr >> 3) : 2 * xcd + (rr >> 4);
    const int q = 4 * kq + s;
    const auto rs = __builtin_amdgcn_make_buffer_rsrc(ws, 0, WS_END, 0x00020000);
    const u32x4 srd = {(u32)(uintptr_t)ws, (u32)((uintptr_t)ws >> 32) & 0xffffu, WS_END, 0x00020000u};
    const u32 voff = lane * 16;
    const u32 lbase = (u32)(uintptr_t)(LAS char*)smem;
    const u32 wl = lbase + w * 3072;
    auto set_m0 = [&](u32 v) { asm volatile("s_mov_b32 m0, %0" :: "s"(v) : "memory"); };
    auto dma = [&](auto O, u32 so) {
        const u32 vo = voff; const u32x4 sr = srd;
        asm volatile("buffer_load_dwordx4 %0, %1, %2 offen offset:%3 lds" :: "v"(vo), "s"(sr), "s"(so), "n"(O.value * 1024) : "memory");
    };
    auto dma3 = [&](u32 dst, u32 so) {
        asm volatile("s_mov_b32 m0, %3\n\ts_nop 0\n\tbuffer_load_dwordx4 %0, %1, %2 offen lds\n\t"
                     "buffer_load_dwordx4 %0, %1, %2 offen offset:1024 lds\n\tbuffer_load_dwordx4 %0, %1, %2 offen offset:2048 lds"
                     :: "v"(voff), "s"(srd), "s"(so), "s"(dst) : "memory");
    };
    const u32 xsb = WS_X1F + it * 147456;
    const u32 wfb = WS_WF + q * 663552 + w * 3072;
    auto rq_dst = [&](int p) -> u32 {
        const int x = p <= 2 ? p - 1 : p <= 6 ? p - 2 : p - 3;
        return wl + (p == 0 ? 0 : p == 3 ? 12288 : p == 7 ? 24576 : p == 15 ? L_EXF : p > 15 ? ring_slot(p - 13) * 12288 : (3 + x % 9) * 12288);
    };
    auto rq_src = [&](int p) -> u32 {
        const int x = p <= 2 ? p - 1 : p <= 6 ? p - 2 : p - 3;
        return p == 0 ? wfb : p == 3 ? wfb + 12288 : p == 7 ? wfb + 24576 : p == 15 ? WS_EX + q * 8192 + w * 3072
             : p > 15 ? wfb + (p - 13) * 12288 : xsb + w * 3072 + x * 12288;
    };
#pragma unroll
    for (int p = 0; p < XI; ++p) {
        dma3(rq_dst(p), rq_src(p));
        if (PADS && p == 2) {
            if (w == 0) asm volatile("s_call_b64 s[30:31], k_mlp_pad0\n\ts_call_b64 s[30:31], k_mlp_pad4" ::: "s30", "s31");
            if (w == 1) asm volatile("s_call_b64 s[30:31], k_mlp_pad1\n\ts_call_b64 s[30:31], k_mlp_pad5" ::: "s30", "s31");
            if (w == 2) asm volatile("s_call_b64 s[30:31], k_mlp_pad2" ::: "s30", "s31");
            if (w == 3) asm volatile("s_call_b64 s[30:31], k_mlp_pad3" ::: "s30", "s31");
        }
    }
    set_m0(rq_dst(XI));
    const u32 hfb = WS_HF + (it * 4 + kq) * 36 * 8192 + w * 2048;
    u32x4 Wf[18], Ef[2];
    const u32 rd0 = lbase + voff;
    const u32 rb[3] = {rd0, rd0 + 49152, rd0 + 98304};
    auto rdw = [&](auto F) {
        constexpr int lo = frag_lds(F.value);
        u32x4& d = Wf[F.value % 18]; const u32 base = rb[lo / 49152];
        asm volatile("ds_read_b128 %0, %1 offset:%2" : "=v"(d) : "v"(base), "n"(lo % 49152) : "memory");
    };
    const u32 rex = lbase + L_EXF + (lane & 15) * 16;
    auto rde = [&](auto C) {
        u32x4& d0 = Ef[0]; u32x4& d1 = Ef[1]; const u32 base = rex;
        asm volatile("ds_read_b128 %0, %2 offset:%3\n\tds_read_b128 %1, %2 offset:%4" : "=&v"(d0), "=&v"(d1) : "v"(base), "n"(C.value * 512), "n"(C.value * 512 + 256) : "memory");
    };
    f32x4 acc[2][4];

    u32x4 Xa[36];
    f32x4 gram[2], rsum[2];
    {
        const u32 a0 = wl + (4 * (lane >> 4) + ((lane & 15) >> 2)) * 64 + (lane & 3) * 8;
        u32x4 ones = {0x3f803f80u, 0x3f803f80u, 0x3f803f80u, 0x3f803f80u};
        asm volatile("" : "+v"(ones));
        u32x2 t[3][4];
        const u32 xb[3] = {a0 + 36864, a0 + 73728, a0 + 110592};
        auto xrd = [&](auto KS, auto I) {
            constexpr int hk = 2 * KS.value + (I.value >> 1), sl = (hk / 3) % 9;
            u32x2& d = t[KS.value % 3][I.value]; const u32 base = xb[sl / 3];
            asm volatile("ds_read_b64_tr_b16 %0, %1 offset:%2" : "=v"(d) : "v"(base), "n"((sl % 3) * 12288 + (hk % 3) * 1024 + (I.value & 1) * 32) : "memory");
        };
        auto admit = [&](auto KS, auto J) {
            constexpr int ks = KS.value, x = (2 * ks + 1) / 3, pos = x <= 1 ? x + 1 : x <= 4 ? x + 2 : x + 3;
            constexpr int issued = 3 * XI + x_dmas(J.value);
            static_assert(issued >= 3 * pos + 3, "X order");
            if constexpr (ks % 6 == 0) asm volatile("s_waitcnt vmcnt(%0)\n\ts_barrier" :: "n"(issued - 3 * pos - 3) : "memory");
            else if constexpr (ks % 3 != 2) asm volatile("s_waitcnt vmcnt(%0)" :: "n"(issued - 3 * pos - 3) : "memory");
        };
        static_for<0, 2>([&](auto KS) {
            admit(KS, IC<0>{});
            static_for<0, 4>([&](auto I) { xrd(KS, I); });
            rdw(IC<2 * KS.value>{}); rdw(IC<2 * KS.value + 1>{});
        });
        STAMP(1);
        u32x4 xv[2];
        static_for<0, 72>([&](auto J) {
            constexpr int j = J.value, ks = j / 4, k = j % 4;
            u32x2 (&d)[4] = t[ks % 3];
            u32x4& x0 = xv[0]; u32x4& x1 = xv[1]; u32x4& on = ones;
            if constexpr (k == 0) {
                asm volatile("s_waitcnt lgkmcnt(%4)" : "+v"(d[0]), "+v"(d[1]), "+v"(d[2]), "+v"(d[3]) : "n"(ks < 17 ? 6 : 0) : "memory");
                xv[0] = (u32x4){d[0][0], d[0][1], d[2][0], d[2][1]};
                xv[1] = (u32x4){d[1][0], d[1][1], d[3][0], d[3][1]};
                asm volatile("s_nop 1" : "+v"(x0), "+v"(x1));
            }
            f32x4& c0 = k < 2 ? acc[0][2 * (k & 1)] : k == 2 ? gram[0] : rsum[0];
            f32x4& c1 = k < 2 ? acc[0][2 * (k & 1) + 1] : k == 2 ? gram[1] : rsum[1];
            u32x4& am0 = k < 2 ? Wf[(2 * ks + k) % 18] : k == 2 ? x0 : on;
            u32x4& am1 = k < 2 ? Wf[(2 * ks + k) % 18] : k == 2 ? x1 : on;
            if constexpr (j < 4) asm volatile(MFMA "%0, %1, %2, 0" : "=&v"(c0) : "v"(am0), "v"(x0));
            else asm volatile(MFMA "%0, %1, %2, %0" : "+v"(c0) : "v"(am0), "v"(x0));
            SB();
            if constexpr (ks < 16) {
                if constexpr (k == 0) admit(IC<ks + 2>{}, J);
                if constexpr (k < 2) { xrd(IC<ks + 2>{}, IC<2 * k>{}); xrd(IC<ks + 2>{}, IC<2 * k + 1>{}); }
                else rdw(IC<2 * ks + 2 + k>{});
            }
            SB();
            if constexpr (j < 4) asm volatile(MFMA "%0, %1, %2, 0" : "=&v"(c1) : "v"(am1), "v"(x1));
            else asm volatile(MFMA "%0, %1, %2, %0" : "+v"(c1) : "v"(am1), "v"(x1));
            SB();
            if constexpr (k >= 2) { Xa[2 * ks + k - 2] = xv[k - 2]; asm volatile("" : "+a"(Xa[2 * ks + k - 2])); }
            if constexpr (x_dmas(j + 1) > x_dmas(j)) {
                constexpr int n = x_dmas(j), p = XI + n / 3;
                static_assert(p < 12 || p == 15 || (3 * (p < 15 ? p - 12 : p - 13) + 2) / 2 <= ks, "slot still holds unread x");
                dma(IC<n % 3>{}, rq_src(p));
                if constexpr (n % 3 == 2 && p < 18) set_m0(rq_dst(p + 1));
            }
            SB();
        });
    }
    float ysc[2];
    u32x4 Xe[2];
    {
        asm volatile("s_waitcnt vmcnt(6)\n\ts_barrier" ::: "memory");
        STAMP(2);
        static_for<36, 48>(rdw);
        rde(IC<0>{});
        set_m0(wl + ring_slot(XW) * 12288);
        SB();
    }
    float sq[2], mean[2];
    auto stats = [&](auto ST) {
        constexpr int st = ST.value >> 1, t = ST.value & 1;
        if constexpr (st == 0) {
            const float a = (lane & 1) ? gram[t][1] : gram[t][0], c = (lane & 1) ? gram[t][3] : gram[t][2];
            float v = (lane & 2) ? c : a;
            const u32 src = ((lane & 15) + 4 * (lane & 12)) * 4;
            float& d = sq[t];
            asm volatile("ds_bpermute_b32 %0, %1, %2" : "=v"(d) : "v"(src), "v"(v) : "memory");
            mean[t] = rsum[t][0] * (1.f / DM);
            asm volatile("" : "+v"(mean[t]));
        }
        if constexpr (st == 1) { asm volatile("" : "+v"(sq[t])); sq[t] = rbf(rbf(fmaxf(fmaf(-mean[t], mean[t], sq[t] * (1.f / DM)), 0.f)) + 1e-5f); asm volatile("" : "+v"(sq[t])); }
        if constexpr (st == 2) { sq[t] = rbf(__builtin_amdgcn_sqrtf(sq[t])); ysc[t] = -1.44269504088896340736f * __builtin_amdgcn_rcpf(sq[t]); asm volatile("" : "+v"(sq[t]), "+v"(ysc[t])); }
        if constexpr (st == 3) {
            const bool lo = lane < 16;
            Xe[t] = (u32x4){lo ? pkbf(mean[t], mean[t]) : 0u, lo ? pkbf(mean[t], sq[t]) : 0u, lo ? pkbf(sq[t], sq[t]) : 0u, 0u};
            asm volatile("" : "+v"(Xe[t]));
        }
    };
    auto corr = [&](auto C) {
        f32x4 (&a)[4] = acc[C.value & 1]; u32x4 (&e)[2] = Ef; u32x4 (&x)[2] = Xe;
        asm volatile(MFMA "%0, %4, %6, %0\n\t" MFMA "%1, %4, %7, %1\n\t" MFMA "%2, %5, %6, %2\n\t" MFMA "%3, %5, %7, %3"
                     : "+v"(a[0]), "+v"(a[1]), "+v"(a[2]), "+v"(a[3]) : "v"(e[0]), "v"(e[1]), "v"(x[0]), "v"(x[1]));
    };

    f32x4 acc2[5][4];
    float yv[16], ev[16];
    u32x4 H[36][2], pollv;
    auto flag_store = [&](u32 level, u32 so) {
        asm volatile("s_mov_b64 exec, 1\n\tbuffer_store_dword %0, off, %1, %2 sc1\n\ts_mov_b64 exec, -1" :: "v"(level), "s"(srd), "s"(so) : "memory");
    };
    auto act = [&](auto C, auto HS, auto PT) {
        constexpr int t = HS.value / 2, e = C.value, pt = PT.value;
        if constexpr (HS.value % 2 == 0) {
            if constexpr (pt != 1 && t < 16) { yv[t] = acc[e & 1][2 * ((t >> 2) & 1) + (t >> 3)][t & 3] * ysc[t >> 3]; asm volatile("" :: "v"(yv[t])); }
            if constexpr (pt != 0 && t >= 2) { yv[t - 2] = yv[t - 2] * ev[t - 2]; asm volatile("" :: "v"(yv[t - 2])); }
            if constexpr (pt != 0 && t < 16) { ev[t] = __builtin_amdgcn_exp2f(yv[t]); asm volatile("" :: "v"(ev[t])); }
        } else {
            if constexpr (pt != 1 && t >= 1 && t < 17) { ev[t - 1] = 1.f + ev[t - 1]; asm volatile("" :: "v"(ev[t - 1])); }
            if constexpr (pt != 0 && t >= 3 && (t & 1)) {
                u32 pk = pkbf(yv[t - 3], yv[t - 2]);
                asm volatile("" :: "v"(pk));
                H[e][(t - 3) / 8][((t - 3) / 2) & 3] = pk;
            }
            if constexpr (pt != 0 && t >= 1 && t < 17) { ev[t - 1] = __builtin_amdgcn_rcpf(ev[t - 1]); asm volatile("" :: "v"(ev[t - 1])); }
        }
    };
    const u32 hown = hfb + 9 * s * 8192;
    const u32 hpeer[3] = {hfb + 9 * ((s + 1) & 3) * 8192, hfb + 9 * ((s + 2) & 3) * 8192, hfb + 9 * ((s + 3) & 3) * 8192};
    auto hload = [&](auto E, auto HF) {
        constexpr int e = E.value, hf = HF.value, k = e - 9;
        const u32 so = hpeer[k % 3] + (k / 3) * 8192, vo = voff; const u32x4 sr = srd;
        u32x4& d = H[e][hf];
        asm volatile("buffer_load_dwordx4 %0, %1, %2, %3 offen offset:%4 sc1" : "=a"(d) : "v"(vo), "s"(sr), "s"(so), "n"(hf * 1024) : "memory");
    };
    const u32 vzero = 0;
    const u32 fso1 = WS_FLAG + (it * 4 + kq) * 64;
    auto poll = [&]() {
        asm volatile("buffer_load_dwordx4 %0, %1, %2, %3 offen sc1" : "=v"(pollv) : "v"(vzero), "s"(srd), "s"(fso1) : "memory");
    };
    auto check = [&](u32 level) {
        asm volatile("" : "+v"(pollv));
#pragma nounroll
        for (u32 spins = 0; spins < (1u << 20); ++spins) {
            const bool bad = pollv[0] < level || pollv[1] < level || pollv[2] < level || pollv[3] < level;
            if (__builtin_amdgcn_ballot_w64(bad) == 0ull) break;
            __builtin_amdgcn_s_sleep(1);
            asm volatile("buffer_load_dwordx4 %0, %1, %2, %3 offen sc1\n\ts_waitcnt vmcnt(0)"
                         : "=v"(pollv) : "v"(vzero), "s"(srd), "s"(fso1) : "memory");
        }
    };
    const u32 pgb = WS_PART + (it * 4 + s) * 98304 + w * 2048;
    const u32 phb = WS_PH + (it * 4 + s) * 16384 + w * 4096;
    auto xfrag = [&](int ks) -> u32 { return xsb + ((ks / 3) * 12 + 3 * w + ks % 3) * 1024; };
    const u32 fso2 = WS_FLAG + 4096 + (it * 4 + s) * 64;
    u32 fv;
    const u32 vfl = (lane & 3) * 4;
    auto poll2 = [&]() {
        asm volatile("buffer_load_dword %0, %1, %2, %3 offen sc1" : "=v"(fv) : "v"(vfl), "s"(srd), "s"(fso2) : "memory");
    };
    auto check2 = [&]() {
        asm volatile("" : "+v"(fv));
#pragma nounroll
        for (u32 spins = 0; spins < (1u << 20); ++spins) {
            if (__builtin_amdgcn_ballot_w64(fv == 0u) == 0ull) break;
            __builtin_amdgcn_s_sleep(1);
            asm volatile("buffer_load_dword %0, %1, %2, %3 offen sc1\n\ts_waitcnt vmcnt(0)" : "=v"(fv) : "v"(vfl), "s"(srd), "s"(fso2) : "memory");
        }
    };
    static_for<G1, GE>([&](auto GG) {
        constexpr int G = GG.value, c = G / 36, g = G % 36, m = G - 324;
        if constexpr (g == 0 && G > 0) STAMP(2 + G / 36);
        if constexpr (PADS && pad_at(G) >= 0) asm volatile("s_branch 1f\n\t.p2align 12\nk_mlp_pad%c0:\n\ts_setpc_b64 s[30:31]\n1:" :: "n"(pad_at(G)));
        if constexpr (s_bar(G)) {
            if constexpr (G % 6 == 0) TW(asm volatile("s_waitcnt vmcnt(%0) lgkmcnt(%1)\n\ts_barrier" :: "n"(bar_wait(G)), "n"(G == G1 ? 8 : 6) : "memory"));
            else TW(asm volatile("s_waitcnt vmcnt(%0)\n\ts_barrier" :: "n"(bar_wait(G)) : "memory"));
        } else if constexpr (G % 6 == 0) asm volatile("s_waitcnt lgkmcnt(%0)" :: "n"(G == 42 ? 4 : G + 6 < GE ? 6 : 0) : "memory");
        u32x4& wf = Wf[G % 18];
        constexpr int sl = G < 324 ? 0 : m_slot(m), e = G < 324 ? 0 : m_ent(m), tl = G < 324 ? (g & 1) : m_half(m);
        constexpr bool first = G < 324 ? g < 2 : m_first(m), own = G >= 324 && ent_own(e);
        if constexpr (G >= 324) {
            if constexpr (ent_first(m) && !own) TW(asm volatile("s_waitcnt vmcnt(%0)" :: "n"(younger(SCH.hgap[e], G)) : "memory"));
        }
        auto mm = [&](auto TT) {
            constexpr int tt = TT.value;
            f32x4& ac = G < 324 ? acc[c & 1][2 * tl + tt] : acc2[sl][2 * tl + tt];
            u32x4& a = wf; u32x4& bo = G < 324 ? Xa[(g & ~1) + tt] : H[e][tt];
            if constexpr (first && own) asm volatile(MFMA "%0, %1, %2, 0" : "=&v"(ac) : "v"(a), "v"(bo));
            else if constexpr (first) asm volatile(MFMA "%0, %1, %2, 0" : "=&v"(ac) : "v"(a), "a"(bo));
            else if constexpr (own) asm volatile(MFMA "%0, %1, %2, %0" : "+v"(ac) : "v"(a), "v"(bo));
            else asm volatile(MFMA "%0, %1, %2, %0" : "+v"(ac) : "v"(a), "a"(bo));
            SB();
        };
        constexpr int hs = G >= 72 && G < 324 && g >= 1 ? (g < 12 ? g - 1 : g) : -1;
        mm(IC<0>{});
        if constexpr (G + 12 < GE) rdw(IC<G + 12>{});
        if constexpr (hs >= 0) act(IC<c - 1>{}, IC<hs>{}, IC<0>{});
        if constexpr (G >= 52 && G < 70) act(IC<0>{}, IC<2 * (G - 52)>{}, IC<2>{});
        if constexpr (G >= 326 && G < 344) act(IC<8>{}, IC<2 * (G - 326)>{}, IC<2>{});
        SB();
        mm(IC<1>{});
        if constexpr (G == 50) { corr(IC<0>{}); SB(); rde(IC<1>{}); }
        if constexpr (G < 324 && g == (c == 1 ? 30 : 11)) {
            if constexpr (c > 1) { act(IC<c - 1>{}, IC<10>{}, IC<1>{}); SB(); }
            corr(IC<c>{}); SB();
            if constexpr (c > 1) act(IC<c - 1>{}, IC<11>{}, IC<2>{});
        } else if constexpr (hs >= 0) act(IC<c - 1>{}, IC<hs>{}, IC<1>{});
        if constexpr (g == (c == 1 ? 32 : 14) && c < 8) rde(IC<c + 1>{});
        if constexpr (SCH.dn[G] >= 0) {
            dma(IC<SCH.dn[G] % 3>{}, wfb + (SCH.dn[G] / 3) * 12288);
            if constexpr (SCH.dn[G] % 3 == 2 && SCH.dn[G] < 161) set_m0(wl + ring_slot(SCH.dn[G] / 3 + 1) * 12288);
        }
        if constexpr (G == 36 || G == 37) stats(IC<G - 36>{});
        if constexpr (G >= 43 && G < 49) stats(IC<G - 41>{});
        if constexpr (G >= 52 && G < 70) act(IC<0>{}, IC<2 * (G - 52) + 1>{}, IC<2>{});
        if constexpr (G >= 326 && G < 344) act(IC<8>{}, IC<2 * (G - 326) + 1>{}, IC<2>{});
        if constexpr (s_hstore(G) >= 0) {
            constexpr int hf = s_hstore(G), cc = c - 1;
            __builtin_amdgcn_raw_buffer_store_b128(H[cc][hf], rs, voff + hf * 1024, hown + cc * 8192, 16);
        }
        if constexpr (s_pub(G) > 0) flag_store(s_pub(G), fso1 + s * 4);
        if constexpr (G == PUB2) flag_store(1u, fso2 + kq * 4);
        if constexpr (G == C1 || G == C2 || G == C3) {
            TW(asm volatile("s_waitcnt vmcnt(%0)" :: "n"(younger(G == C1 ? P1 : G == C2 ? P2 : P3, G)) : "memory"));
            check(G == C1 ? 2 : G == C2 ? 4 : 9);
        }
        if constexpr (G == P1 || G == P2 || G == P3) poll();
        if constexpr (G == P4) poll2();
        if constexpr (SCH.hl[G] >= 0) hload(IC<SCH.hl[G] / 2>{}, IC<SCH.hl[G] % 2>{});
        if constexpr (s_pstore(G) >= 0) {
            constexpr int sg = s_pstore(G) / 2, pt = s_pstore(G) % 2;
            const f32x4 a0 = acc2[sg][2 * pt], a1 = acc2[sg][2 * pt + 1];
            const u32x4 v = {pkbf(a0[0], a0[1]), pkbf(a0[2], a0[3]), pkbf(a1[0], a1[1]), pkbf(a1[2], a1[3])};
            if constexpr (sg == 0) __builtin_amdgcn_raw_buffer_store_b128(v, rs, voff, phb + kq * 1024, 16);
            else __builtin_amdgcn_raw_buffer_store_b128(v, rs, voff + pt * 1024, pgb + ((kq + sg) & 3) * 24576 + (sg - 1) * 8192, 16);
        }
        if constexpr (s_res(G)) {
            constexpr int i = (G - 620) / 2;
            set_m0(wl + RESL + i * 1024);
            const u32 so = xfrag(9 * s + (i < 2 ? 2 * kq + i : 8));
            asm volatile("s_nop 0" ::: "memory");
            dma(IC<0>{}, so);
        }
        SB();
    });
    STAMP(20);

    {
        u32x2 xr[5];
        f32x4 gb[2];
        f32x2 gh;
        const u32 ra = wl + RESL + (4 * (lane >> 4) + ((lane & 15) >> 2)) * 64 + (lane & 3) * 8;
        const u32 ga = lbase + L_EXF + 4608 + (lane >> 4) * 16;
        asm volatile("s_waitcnt vmcnt(1)" ::: "memory");
        asm volatile("ds_read_b64_tr_b16 %0, %5\n\tds_read_b64_tr_b16 %1, %5 offset:32\n\tds_read_b64_tr_b16 %2, %5 offset:1024\n\t"
                     "ds_read_b64_tr_b16 %3, %5 offset:1056\n\tds_read_b64_tr_b16 %4, %6 offset:2048"
                     : "=&v"(xr[0]), "=&v"(xr[1]), "=&v"(xr[2]), "=&v"(xr[3]), "=&v"(xr[4]) : "v"(ra), "v"(ra + (kq >> 1) * 32) : "memory");
        asm volatile("ds_read_b128 %0, %3\n\tds_read_b128 %1, %3 offset:64\n\tds_read_b64 %2, %4 offset:128\n\ts_waitcnt vmcnt(0) lgkmcnt(0)"
                     : "=&v"(gb[0]), "=&v"(gb[1]), "=&v"(gh) : "v"(ga), "v"(ga + (kq & 1) * 8) : "memory");
        asm volatile("" : "+v"(xr[0]), "+v"(xr[1]), "+v"(xr[2]), "+v"(xr[3]), "+v"(xr[4]));
        check2();
        STAMP(21);
        u32x4 P[6];
        u32 Q[4];
        const u32 pin = pgb + kq * 24576;
        asm volatile("buffer_load_dwordx4 %4, %10, %11, %14 offen sc1\n\tbuffer_load_dwordx4 %5, %10, %11, %14 offen offset:1024 sc1\n\t"
                     "buffer_load_dwordx4 %2, %10, %11, %13 offen sc1\n\tbuffer_load_dwordx4 %3, %10, %11, %13 offen offset:1024 sc1\n\t"
                     "buffer_load_dwordx4 %0, %10, %11, %12 offen sc1\n\tbuffer_load_dwordx4 %1, %10, %11, %12 offen offset:1024 sc1\n\t"
                     "buffer_load_dword %6, %15, %11, %16 offen sc1\n\tbuffer_load_dword %7, %15, %11, %16 offen offset:1024 sc1\n\t"
                     "buffer_load_dword %8, %15, %11, %16 offen offset:2048 sc1\n\tbuffer_load_dword %9, %15, %11, %16 offen offset:3072 sc1\n\t"
                     "s_waitcnt vmcnt(0)"
                     : "=&v"(P[0]), "=&v"(P[1]), "=&v"(P[2]), "=&v"(P[3]), "=&v"(P[4]), "=&v"(P[5]), "=&v"(Q[0]), "=&v"(Q[1]), "=&v"(Q[2]), "=&v"(Q[3])
                     : "v"(voff), "s"(srd), "s"(pin), "s"(pin + 8192), "s"(pin + 16384), "v"(voff + kq * 4), "s"(phb) : "memory");
        STAMP(22);
        const auto ro = __builtin_amdgcn_make_buffer_rsrc(out, 0, SEQ * DM * 2, 0x00020000);
        const u32 ov = ((it * 128 + w * 32 + (lane & 15)) * DM + s * 144 + 4 * (lane >> 4)) * 2;
        auto fin2 = [&](float a0, float a1, float g0, float g1, u32 xk) -> u32 {
            const u32 t = pkbf(a0 * -0.69314718055994530942f, a1 * -0.69314718055994530942f);
            const u32 gz = pkbf(bflo(t) * g0, bfhi(t) * g1);
            return pkbf(bflo(xk) + bflo(gz), bfhi(xk) + bfhi(gz));
        };
#pragma unroll
        for (int k = 0; k < 4; ++k) {
            u32x2 o;
#pragma unroll
            for (int j = 0; j < 2; ++j) {
                const int d = 2 * (k & 1) + j;
                const u32 p0 = P[k >> 1][d], p1 = P[2 + (k >> 1)][d], p2 = P[4 + (k >> 1)][d], xk = xr[k][j];
                o[j] = fin2(acc2[4][k][2 * j] + bflo(p0) + bflo(p1) + bflo(p2), acc2[4][k][2 * j + 1] + bfhi(p0) + bfhi(p1) + bfhi(p2),
                            gb[k >> 1][2 * j], gb[k >> 1][2 * j + 1], xk);
            }
            __builtin_amdgcn_raw_buffer_store_b64(o, ro, ov + (k >> 1) * 32, kq * 64 + (k & 1) * (16 * DM * 2), OAUX);
        }
        const u32 oh = fin2(bflo(Q[0]) + bflo(Q[1]) + bflo(Q[2]) + bflo(Q[3]), bfhi(Q[0]) + bfhi(Q[1]) + bfhi(Q[2]) + bfhi(Q[3]),
                            gh[0], gh[1], (kq & 1) ? xr[4][1] : xr[4][0]);
        __builtin_amdgcn_raw_buffer_store_b32(oh, ro, ov + 256, (kq & 1) * 4 + (kq >> 1) * (16 * DM * 2), OAUX);
    }
    asm volatile("k_mlp_end:");
}
#if defined(__HIP_DEVICE_COMPILE__)
#pragma clang attribute pop
#endif

extern "C" void kernel_launch(void* const* d_in, const int* in_sizes, int n_in, void* d_out, int out_size,
                              void* d_ws, size_t ws_size, hipStream_t stream) {
    const u16* x = (const u16*)d_in[0];
    char* ws = (char*)d_ws;
    (void)hipFuncSetAttribute((const void*)k_mlp, hipFuncAttributeMaxDynamicSharedMemorySize, MLP_LDS);
    k_prep<<<NB_PREP, 256, PREP_LDS, stream>>>(x, (const u16*)d_in[1], (const u16*)d_in[2], (const u16*)d_in[3],
                                               (const u16*)d_in[4], (const u16*)d_in[5], (const u16*)d_in[9],
                                               (const u16*)d_in[10], (const u16*)d_in[11], ws);
    k_conv<<<NB_PF + NB_CONV + NB_W1, 256, CONV_LDS, stream>>>((const u16*)d_in[6], (const u16*)d_in[8], ws);
    k_mlp<<<256, 256, MLP_LDS, stream>>>(ws, (u16*)d_out);
}
```

```cpp
#include <hip/hip_runtime.h>
#include <stdint.h>

typedef unsigned short u16;
typedef unsigned int u32;
typedef float f32x2 __attribute__((ext_vector_type(2)));
typedef float f32x4 __attribute__((ext_vector_type(4)));
typedef float f32x16 __attribute__((ext_vector_type(16)));
typedef _Float16 f16x2 __attribute__((ext_vector_type(2)));
typedef _Float16 f16x8 __attribute__((ext_vector_type(8)));
typedef __bf16 bf16x2 __attribute__((ext_vector_type(2)));
typedef __bf16 bf16x8 __attribute__((ext_vector_type(8)));
typedef short s16x4 __attribute__((ext_vector_type(4)));
typedef u32 u32x2 __attribute__((ext_vector_type(2)));
typedef u32 u32x4 __attribute__((ext_vector_type(4)));
#define LAS __attribute__((address_space(3)))
#define DI __device__ __forceinline__
#define RTSTAMP(k)
#define MARK(i)

constexpr int SEQ = 2048, DM = 576, NS = 128, FF = 4608;

constexpr u32 WS_MOD   = 0;
constexpr u32 WS_STAT  = 16384;
constexpr u32 WS_KSC   = 32768;
constexpr u32 WS_FLAG  = 36864;
constexpr u32 WS_EX    = 65536;
constexpr u32 WS_XT    = 1u << 20;
constexpr u32 WS_KR    = WS_XT + 2359296;
constexpr u32 WS_X1F   = WS_KR + 2359296;
constexpr u32 WS_WF    = WS_X1F + 2359296;
constexpr u32 WS_HF    = WS_WF + 10616832;
constexpr u32 WS_PART  = WS_HF + 18874368;
constexpr u32 WS_PH    = WS_PART + 6291456;
constexpr u32 WS_END   = WS_PH + 1048576;

DI float bflo(u32 u) { return __builtin_bit_cast(float, u << 16); }
DI float bfhi(u32 u) { return __builtin_bit_cast(float, u & 0xffff0000u); }
DI float bf2f(u16 v) { return __builtin_bit_cast(float, ((u32)v) << 16); }
DI u32 pkbf(float a, float b) { f32x2 v = {a, b}; return __builtin_bit_cast(u32, __builtin_convertvector(v, bf16x2)); }
DI u32 pkhf(float a, float b) { f32x2 v = {a, b}; return __builtin_bit_cast(u32, __builtin_convertvector(v, f16x2)); }
DI float rbf(float a) { return bflo(pkbf(a, a)); }
template <int CTRL, int RM = 0xf> DI float dppf(float v) {
    return __builtin_bit_cast(float, __builtin_amdgcn_update_dpp(0, __builtin_bit_cast(int, v), CTRL, RM, 0xf, true));
}
DI u32x4 trread2(const char* smem, u32 off0, u32 off1) {
    const u32x2 l = __builtin_bit_cast(u32x2, __builtin_amdgcn_ds_read_tr16_b64_v4i16((LAS s16x4*)(smem + off0)));
    const u32x2 h = __builtin_bit_cast(u32x2, __builtin_amdgcn_ds_read_tr16_b64_v4i16((LAS s16x4*)(smem + off1)));
    return (u32x4){l[0], l[1], h[0], h[1]};
}
DI void st16(char* ws, u32 off, u32x4 v) {
    __builtin_amdgcn_raw_buffer_store_b128(v, __builtin_amdgcn_make_buffer_rsrc(ws, 0, WS_END, 0x00020000), off, 0, 16);
}
DI void st4(char* ws, u32 off, u32 v) {
    __builtin_amdgcn_raw_buffer_store_b32(v, __builtin_amdgcn_make_buffer_rsrc(ws, 0, WS_END, 0x00020000), off, 0, 16);
}
DI float pow2i(int e) { e = e < -126 ? -126 : (e > 127 ? 127 : e); return __builtin_bit_cast(float, (u32)(e + 127) << 23); }
DI int expo(float x) { return (int)((__builtin_bit_cast(u32, x) >> 23) & 255u) - 127; }
DI u32 imgrow(u32 row) { return row * 1184u + (row >> 3) * 128u; }
DI f32x2 cmul(f32x2 z, f32x2 w, f32x2 wp) {
    const f32x2 zx = {z[0], z[0]}, zy = {z[1], z[1]};
    return __builtin_elementwise_fma(zy, wp, zx * w);
}
#define CODE_PREFETCH(sym, part) do { u32 lo_, hi_, len_; \
    asm volatile("s_getpc_b64 s[20:21]\n\ts_add_u32 s20, s20, " #sym "@rel32@lo+4\n\ts_addc_u32 s21, s21, " #sym "@rel32@hi+12\n\t" \
                 "s_mov_b32 %0, s20\n\ts_mov_b32 %1, s21\n\ts_mov_b32 %2, " #sym "_end-" #sym : "=s"(lo_), "=s"(hi_), "=s"(len_) :: "scc", "s20", "s21"); \
    const char* p_ = (const char*)(((unsigned long long)hi_ << 32) | lo_); \
    const u32 o_ = (threadIdx.x + (part) * 256u) * 128u; \
    if (o_ < len_) { u32 t_; asm volatile("global_load_dword %0, %1, off\n\ts_waitcnt vmcnt(0)" : "=v"(t_) : "v"(p_ + o_) : "memory"); } \
    } while (0)

#define DELAY1 20
#define DELAY2 0
#define TLBW 0
#define NAV 26
constexpr int NA = NAV, NB = 36 - NA;
constexpr int NB_PF = 16, NB_LN = 64, NB_GEMV = 108, NB_KGEN = 576, NB_W2 = 144;
constexpr int NB_PREP = NB_PF + NB_LN + NB_GEMV + NB_KGEN + NB_W2;
constexpr int PREP_LDS = 38400;

extern "C" __global__ __launch_bounds__(256, 4) void k_prep(const u16* __restrict__ x, const u16* __restrict__ logA,
                                              const u16* __restrict__ Aim, const u16* __restrict__ Cre,
                                              const u16* __restrict__ Cim, const u16* __restrict__ logdt,
                                              const u16* __restrict__ w2, const u16* __restrict__ cvec,
                                              const u16* __restrict__ wmod, char* __restrict__ ws, const u16* w1p) {
    extern __shared__ __attribute__((aligned(16))) char smem[];
    RTSTAMP(0);
    const int tid = threadIdx.x, lane = tid & 63;
    const int wv = __builtin_amdgcn_readfirstlane(tid >> 6);
    int b = blockIdx.x;
    if (b >= NB_PF && b < NB_PF + NB_KGEN + NB_LN + NB_GEMV) b = b < NB_PF + NB_KGEN ? b + NB_LN + NB_GEMV : b - NB_KGEN;
    if (b < NB_PF) {
        if (b < 8) CODE_PREFETCH(k_prep, 0); else CODE_PREFETCH(k_conv, 0);
        if (TLBW & 2) {
            const u32 o = ((b >> 3) * 256 + tid) * 40960u;
            if (o < WS_HF) { u32 t_; asm volatile("global_load_dword %0, %1, off\n\ts_waitcnt vmcnt(0)" : "=v"(t_) : "v"(ws + o) : "memory"); }
            if (tid < 81) { u32 t_; asm volatile("global_load_dword %0, %1, off\n\ts_waitcnt vmcnt(0)" : "=v"(t_) : "v"((const char*)w1p + tid * 65536) : "memory"); }
        }
        if (b < 2) st16(ws, WS_FLAG + b * 4096 + tid * 16, (u32x4){0u, 0u, 0u, 0u});
    } else if ((b -= NB_PF) < NB_LN) {
        const int t0 = b * 32, tok = tid >> 3, sub = tid & 7;
        u32x4 v[9];
#pragma unroll
        for (int i = 0; i < 9; ++i) v[i] = *(const u32x4*)(x + (size_t)(t0 + tok) * DM + (sub + 8 * i) * 8);
        MARK(0);
#pragma unroll
        for (int i = 0; i < 9; ++i) *(u32x4*)(smem + imgrow(tok) + (sub + 8 * i) * 16) = v[i];
        __syncthreads();
        const int g = lane >> 4, qq = (lane & 15) >> 2, p = lane & 3;
#pragma unroll
        for (int i = 0; i < 9; ++i) {
            const int cb = wv * 9 + i;
            st16(ws, WS_XT + b * 36864 + cb * 1024 + (lane & 15) * 64 + g * 16,
                 trread2(smem, imgrow(8 * g + qq) + cb * 32 + p * 8, imgrow(8 * g + 4 + qq) + cb * 32 + p * 8));
        }
        MARK(1);
        float s = 0.f;
#pragma unroll
        for (int i = 0; i < 9; ++i)
#pragma unroll
            for (int k = 0; k < 4; ++k) s += bflo(v[i][k]) + bfhi(v[i][k]);
        s += dppf<0xB1>(s); s += dppf<0x4E>(s); s += dppf<0x141>(s);
        const float mean = s * (1.f / DM);
        float q = 0.f;
#pragma unroll
        for (int i = 0; i < 9; ++i)
#pragma unroll
            for (int k = 0; k < 4; ++k) {
                const float a = bflo(v[i][k]) - mean, c = bfhi(v[i][k]) - mean;
                q = fmaf(a, a, q); q = fmaf(c, c, q);
            }
        q += dppf<0xB1>(q); q += dppf<0x4E>(q); q += dppf<0x141>(q);
        if (sub == 0) st4(ws, WS_STAT + (t0 + tok) * 4, pkbf(mean, __builtin_amdgcn_sqrtf(rbf(rbf(q * (1.f / DM)) + 1e-5f))));
    } else if ((b -= NB_LN) < NB_GEMV) {
        const int seg = tid & 3, rg = tid >> 2;
        u32x4 v[9]; u16 cv[9];
#pragma unroll
        for (int i = 0; i < 9; ++i) {
            const int row = rg + 64 * i;
            cv[i] = cvec[row];
            v[i] = *(const u32x4*)(wmod + (size_t)row * (6 * DM) + b * 32 + seg * 8);
        }
        MARK(0);
        float acc[8];
#pragma unroll
        for (int k = 0; k < 8; ++k) acc[k] = 0.f;
#pragma unroll
        for (int i = 0; i < 9; ++i) {
            const float c = bf2f(cv[i]);
            const float sl = rbf(c * __builtin_amdgcn_rcpf(1.f + __builtin_amdgcn_exp2f(c * -1.44269504088896340736f)));
#pragma unroll
            for (int k = 0; k < 4; ++k) {
                acc[2 * k] = fmaf(sl, bflo(v[i][k]), acc[2 * k]);
                acc[2 * k + 1] = fmaf(sl, bfhi(v[i][k]), acc[2 * k + 1]);
            }
        }
#pragma unroll
        for (int k = 0; k < 8; ++k) { acc[k] += dppf<0x114>(acc[k]); acc[k] += dppf<0x118>(acc[k]); }
        float* red = (float*)smem;
        if ((lane & 15) >= 12) {
            *(f32x4*)(red + (tid >> 4) * 32 + seg * 8) = (f32x4){acc[0], acc[1], acc[2], acc[3]};
            *(f32x4*)(red + (tid >> 4) * 32 + seg * 8 + 4) = (f32x4){acc[4], acc[5], acc[6], acc[7]};
        }
        __syncthreads();
        MARK(1);
        if (tid < 32) {
            float t = 0.f;
#pragma unroll
            for (int i = 0; i < 16; ++i) t += red[i * 32 + tid];
            st4(ws, WS_MOD + (b * 32 + tid) * 4, __builtin_bit_cast(u32, rbf(t)));
        }
    } else if ((b -= NB_GEMV) < NB_KGEN) {
        const int d = b, hn = wv & 1, grp = wv >> 1;
        const int n = hn * 64 + lane, n2 = n ^ 64;
        const u16 la_ = logA[d * NS + n], ai_ = Aim[d * NS + n], cr_ = Cre[d * NS + n], ci_ = Cim[d * NS + n];
        const u16 cr2_ = Cre[d * NS + n2], ci2_ = Cim[d * NS + n2], ld_ = logdt[d];
        u32 ta[8];
#pragma unroll
        for (int j = 0; j < 8; j += 2) {
            const float l0 = (float)(2047 - tid * 8 - j);
            const u32 pk = pkbf(l0, l0 - 1.f);
            const float p0 = bflo(pk), p1 = bfhi(pk);
            ta[j] = (u32)fminf(4.f * p0, p0 + 1536.f); ta[j + 1] = (u32)fminf(4.f * p1, p1 + 1536.f);
        }
        asm volatile("" : "+v"(ta[0]), "+v"(ta[1]), "+v"(ta[2]), "+v"(ta[3]), "+v"(ta[4]), "+v"(ta[5]), "+v"(ta[6]), "+v"(ta[7]));
        MARK(0);
        const float cr = bf2f(cr_), ci = bf2f(ci_);
        const float dt = rbf(__builtin_amdgcn_exp2f(bf2f(ld_) * 1.44269504088896340736f));
        const float zr = -rbf(__builtin_amdgcn_exp2f(bf2f(la_) * 1.44269504088896340736f)) * dt, zi = bf2f(ai_) * dt;
        float bs = fabsf(cr) + fabsf(ci) + fabsf(bf2f(cr2_)) + fabsf(bf2f(ci2_));
        bs += dppf<0xB1>(bs); bs += dppf<0x4E>(bs); bs += dppf<0x141>(bs); bs += dppf<0x140>(bs);
        bs += dppf<0x142, 0xa>(bs); bs += dppf<0x143, 0xc>(bs);
        bs = __builtin_bit_cast(float, __builtin_amdgcn_readlane(__builtin_bit_cast(int, bs), 63));
        const int se = 14 - expo(2.f * dt * bs * 1.0001f);
        const float scale = pow2i(se > 100 ? 100 : se);
        const float zr2 = zr * 1.44269504088896340736f;
        auto cexp = [&](float p) -> f32x2 {
            const float mag = __builtin_amdgcn_exp2f(zr2 * p);
            const float ang = zi * p;
            const float rr = __builtin_rintf(ang * 0.15915494309189535f);
            float fr = fmaf(ang, 0.15915494309189535f, -rr);
            fr = fmaf(ang, 6.4206382e-9f, fr);
            return (f32x2){mag * __builtin_amdgcn_cosf(fr), mag * __builtin_amdgcn_sinf(fr)};
        };
        const float p1 = grp ? 4.f : 1.f;
        const f32x2 f1 = cexp(p1), c1 = cexp(16.f * p1), s0 = cexp(grp ? 512.f : 0.f), s1 = cexp(256.f * p1);
        const f32x2 fw = {f1[0], -f1[1]}, fwp = {f1[1], f1[0]};
        const f32x2 cwp = {-c1[1], c1[0]};
        const float es = 2.f * dt * scale;
        f32x2 za = cmul((f32x2){es * cr, es * ci}, s0, (f32x2){-s0[1], s0[0]});
        f32x2 zb = cmul(za, s1, (f32x2){-s1[1], s1[0]});
        f32x2 zf = {1.f, 0.f};
        char* tab = smem + wv * 8704;
        u32 keep[16];
#pragma unroll
        for (int k = 0; k < 16; ++k) {
            *(u32*)(tab + k * 272 + lane * 4) = pkhf(zf[0], zf[1]);
            *(u32*)(tab + (16 + k) * 272 + lane * 4) = pkhf(za[0], za[1]);
            keep[k] = pkhf(zb[0], zb[1]);
            zf = cmul(zf, fw, fwp); za = cmul(za, c1, cwp); zb = cmul(zb, c1, cwp);
        }
        const int r16 = lane & 15, g = lane >> 4;
        f16x8 fb[4];
        f32x4 acc0 = {0.f, 0.f, 0.f, 0.f}, acc1 = {0.f, 0.f, 0.f, 0.f};
        __builtin_amdgcn_fence(__ATOMIC_RELEASE, "wavefront");
        __builtin_amdgcn_wave_barrier();
#pragma unroll
        for (int ks = 0; ks < 4; ++ks) {
            fb[ks] = *(const f16x8*)(tab + r16 * 272 + ks * 64 + g * 16);
            const f16x8 fa = *(const f16x8*)(tab + (16 + r16) * 272 + ks * 64 + g * 16);
            acc0 = __builtin_amdgcn_mfma_f32_16x16x32_f16(fb[ks], fa, acc0, 0, 0, 0);
        }
        __builtin_amdgcn_fence(__ATOMIC_RELEASE, "wavefront");
        __builtin_amdgcn_wave_barrier();
#pragma unroll
        for (int k = 0; k < 16; ++k) *(u32*)(tab + (16 + k) * 272 + lane * 4) = keep[k];
        __builtin_amdgcn_fence(__ATOMIC_RELEASE, "wavefront");
        __builtin_amdgcn_wave_barrier();
#pragma unroll
        for (int ks = 0; ks < 4; ++ks) {
            const f16x8 fa = *(const f16x8*)(tab + (16 + r16) * 272 + ks * 64 + g * 16);
            acc1 = __builtin_amdgcn_mfma_f32_16x16x32_f16(fb[ks], fa, acc1, 0, 0, 0);
        }
        MARK(1);
        __builtin_amdgcn_fence(__ATOMIC_RELEASE, "wavefront");
        __builtin_amdgcn_wave_barrier();
        *(f32x4*)(tab + r16 * 64 + g * 16) = acc0;
        *(f32x4*)(tab + 1024 + r16 * 64 + g * 16) = acc1;
        __syncthreads();
        float kv[8];
#pragma unroll
        for (int j = 0; j < 8; ++j) {
            const u32 o = ta[j] + (ta[j] >= 2048u ? 2u * 8704u - 2048u : 0u);
            kv[j] = *(const float*)(smem + o) + *(const float*)(smem + o + 8704);
        }
        MARK(2);
        st16(ws, WS_KR + d * 4096 + tid * 16, (u32x4){pkhf(kv[0], kv[1]), pkhf(kv[2], kv[3]), pkhf(kv[4], kv[5]), pkhf(kv[6], kv[7])});
        if (tid == 0) st4(ws, WS_KSC + d * 4, __builtin_bit_cast(u32, pow2i(se > 100 ? -100 : -se)));
    } else {
        b -= NB_KGEN;
        const u16* src = w2 + (size_t)b * 32 * DM;
        if (DELAY1 > 0) __builtin_amdgcn_s_sleep(DELAY1);
        const int tok = tid >> 3, sub = tid & 7;
        u32x4 v[9];
#pragma unroll
        for (int i = 0; i < 9; ++i) v[i] = *(const u32x4*)(src + tok * DM + (sub + 8 * i) * 8);
        MARK(0);
#pragma unroll
        for (int i = 0; i < 9; ++i) *(u32x4*)(smem + imgrow(tok) + (sub + 8 * i) * 16) = v[i];
        __syncthreads();
        const int kq = b / 36, kk = b - kq * 36, sp = kk / 9, c = kk - sp * 9;
        const int rel = (sp - wv) & 3;
        const int e = rel == 0 ? c : 9 + c * 3 + (rel - 1);
        const int r0 = 4 * (lane >> 4) + ((lane & 15) >> 2), p = lane & 3;
#pragma unroll
        for (int i = 0; i < 9; ++i) {
            const int sg = (i + 1) >> 1, j = i == 0 ? 0 : (i - 1) & 1;
            const int T = 9 * wv + (sg == 0 ? 8 : 2 * ((kq + sg) & 3) + j);
            const int m = sg == 4 ? 252 + 2 * e + j
                        : e < NA ? 7 * e + i
                        : sg == 0 ? 7 * NA + 2 * NB + (e - NA) : 7 * NA + (sg == 1 ? 0 : (2 * sg - 1) * NB) + (e - NA) * 2 + j;
            const u32 col = T * 32 + p * 8;
            st16(ws, WS_WF + ((kq * 4 + wv) * 648 + 324 + m) * 1024 + lane * 16, trread2(smem, imgrow(r0) + col, imgrow(r0 + 16) + col));
        }
    }
    asm volatile("k_prep_end:");
}

constexpr int NB_CONV = 576, NB_W1 = 144;
constexpr int CONV_LDS = 36864 + 4096;
constexpr int UCP = 4640, KB_OFF = 4 * UCP;

extern "C" __global__ __launch_bounds__(256, 4) void k_conv(const u16* __restrict__ dskip, const u16* __restrict__ w1,
                                              char* __restrict__ ws, const u16* outp) {
    extern __shared__ __attribute__((aligned(16))) char smem[];
    const int tid = threadIdx.x, lane = tid & 63;
    const int wv = __builtin_amdgcn_readfirstlane(tid >> 6);
    RTSTAMP(1);
    const float* mod = (const float*)(ws + WS_MOD);
    int b = blockIdx.x;
    if (b >= NB_PF) b = b < NB_PF + NB_CONV ? b + NB_W1 : b - NB_CONV;
    if (b < NB_PF) {
        CODE_PREFETCH(k_mlp, b >> 3);
        if (TLBW & 1) {
            const u32 o = WS_X1F + ((b >> 3) * 256 + tid) * 81920u;
            if (o < WS_END) { u32 t_; asm volatile("global_load_dword %0, %1, off\n\ts_waitcnt vmcnt(0)" : "=v"(t_) : "v"(ws + o) : "memory"); }
            if (tid < 36) { u32 t_; asm volatile("global_load_dword %0, %1, off\n\ts_waitcnt vmcnt(0)" : "=v"(t_) : "v"((const char*)outp + tid * 65536) : "memory"); }
        }
    } else if ((b -= NB_PF) < NB_W1) {
        const int q = b / 9, c = b - q * 9, f0 = b * 32;
        if (DELAY2 > 0) __builtin_amdgcn_s_sleep(DELAY2);
        const int seg = tid & 3, rg = tid >> 2;
        u32x4 v[9]; float sb[9], cb[9];
#pragma unroll
        for (int i = 0; i < 9; ++i) {
            const int row = rg + 64 * i;
            v[i] = *(const u32x4*)(w1 + (size_t)row * FF + f0 + seg * 8);
            sb[i] = mod[3 * DM + row]; cb[i] = mod[4 * DM + row];
        }
        MARK(0);
        float S[8], B[8];
#pragma unroll
        for (int k = 0; k < 8; ++k) { S[k] = 0.f; B[k] = 0.f; }
#pragma unroll
        for (int i = 0; i < 9; ++i) {
            const float c1 = rbf(1.f + cb[i]);
            u32x4 o;
#pragma unroll
            for (int k = 0; k < 4; ++k) {
                const float lo = bflo(v[i][k]), hi = bfhi(v[i][k]);
                o[k] = pkbf(lo * c1, hi * c1);
                S[2 * k] += bflo(o[k]); S[2 * k + 1] += bfhi(o[k]);
                B[2 * k] = fmaf(sb[i], lo, B[2 * k]); B[2 * k + 1] = fmaf(sb[i], hi, B[2 * k + 1]);
            }
            *(u32x4*)(smem + ((tid ^ ((tid >> 3) & 2)) + 256 * i) * 16) = o;
        }
#pragma unroll
        for (int k = 0; k < 8; ++k) {
            S[k] += dppf<0x114>(S[k]); S[k] += dppf<0x118>(S[k]);
            B[k] += dppf<0x114>(B[k]); B[k] += dppf<0x118>(B[k]);
        }
        float* red = (float*)(smem + 36864);
        if ((lane & 15) >= 12) {
            float* r = red + (tid >> 4) * 64 + seg * 8;
            *(f32x4*)(r) = (f32x4){S[0], S[1], S[2], S[3]}; *(f32x4*)(r + 4) = (f32x4){S[4], S[5], S[6], S[7]};
            *(f32x4*)(r + 32) = (f32x4){B[0], B[1], B[2], B[3]}; *(f32x4*)(r + 36) = (f32x4){B[4], B[5], B[6], B[7]};
        }
        __syncthreads();
        MARK(1);
        const int g = lane >> 4, qq = (lane & 15) >> 2, p = lane & 3;
#pragma unroll
        for (int i = 0; i < 9; ++i) {
            const int s = wv * 9 + i;
            const u32 a = (32 * (s >> 1) + 4 * g + qq) * 64 + (((s & 1) ^ (g & 1)) * 32) + p * 8;
            st16(ws, WS_WF + (q * 648 + c * 36 + s) * 1024 + lane * 16, trread2(smem, a, a + 1024));
        }
        if (wv == 0) {
            float t = 0.f;
#pragma unroll
            for (int i = 0; i < 16; ++i) t += red[i * 64 + lane];
            const float B_ = __builtin_bit_cast(float, __builtin_amdgcn_ds_bpermute((lane ^ 32) * 4, __builtin_bit_cast(int, t)));
            const float S_ = -t;
            const float s1 = rbf(S_), s2 = rbf(S_ - s1), s3 = S_ - s1 - s2;
            const float b1 = rbf(B_), b2 = rbf(B_ - b1), b3 = B_ - b1 - b2;
            if (lane < 32) st16(ws, WS_EX + q * 8192 + (c * 32 + lane) * 16, (u32x4){pkbf(s1, s2), pkbf(s3, b1), pkbf(b2, b3), 0u});
        } else if (wv == 1 && c == 0 && lane < 48) {
            const int d = (q & 3) * 144 + (lane < 32 ? (q >> 2) * 32 + lane : 96 + lane);
            st4(ws, WS_EX + q * 8192 + 4608 + lane * 4, __builtin_bit_cast(u32, mod[5 * DM + d]));
        }
    } else {
        const int d = b - NB_W1;
        const int tp = tid > 0 ? tid * 8 - 4 : 0;
        const char* xb = ws + WS_XT + d * 64;
        const u32x4 x0 = *(const u32x4*)(xb + (tid >> 2) * 36864 + (tid & 3) * 16);
        const u32x2 xm = *(const u32x2*)(xb + (tp >> 5) * 36864 + (tp & 31) * 2);
        u32x4 st[3];
        st[0] = *(const u32x4*)(ws + WS_STAT + tp * 4);
        st[1] = *(const u32x4*)(ws + WS_STAT + tid * 32);
        st[2] = *(const u32x4*)(ws + WS_STAT + tid * 32 + 16);
        const u32x4 kv = *(const u32x4*)(ws + WS_KR + d * 4096 + tid * 16);
        const float sa = mod[d], ca = mod[DM + d], ga = mod[2 * DM + d];
        const float dsk = bf2f(dskip[d]);
        const float kinv = ((const float*)(ws + WS_KSC))[d];
        {
            const u32x4 z = {0u, 0u, 0u, 0u};
            if (tid < 96) *(u32x4*)(smem + (tid / 24) * UCP + (tid % 24) * 16) = z;
            else if (tid < 104) *(u32x4*)(smem + KB_OFF + 4096 + (tid - 96) * 16) = z;
        }
        MARK(0);
        const float c1 = rbf(1.f + ca);
        const int he = expo(64.f * fabsf(c1) + 2.f * fabsf(sa)) - 14;
        const float hs = pow2i(-he), oscale = pow2i(he) * kinv;
        const u32 xw[6] = {xm[0], xm[1], x0[0], x0[1], x0[2], x0[3]};
        u32 P[6], hb[4];
#pragma unroll
        for (int k = 0; k < 6; ++k) {
            const u32 s0 = st[k >> 1][(k & 1) * 2], s1 = st[k >> 1][(k & 1) * 2 + 1];
            const float n0 = (bflo(xw[k]) - bflo(s0)) * __builtin_amdgcn_rcpf(bfhi(s0));
            const float n1 = (bfhi(xw[k]) - bflo(s1)) * __builtin_amdgcn_rcpf(bfhi(s1));
            const u32 t = pkbf(fmaf(n0, c1, sa), fmaf(n1, c1, sa));
            if (k >= 2) hb[k - 2] = t;
            P[k] = pkhf(bflo(t) * hs, bfhi(t) * hs);
        }
        if (tid == 0) { P[0] = 0u; P[1] = 0u; }
        u32 Q[5];
#pragma unroll
        for (int k = 0; k < 5; ++k) Q[k] = __builtin_amdgcn_alignbit(P[k + 1], P[k], 16);
        {
            char* ub = smem + 384 + tid * 16;
            *(u32x4*)(ub) = (u32x4){Q[0], Q[1], Q[2], Q[3]};
            *(u32x4*)(ub + UCP) = (u32x4){P[1], P[2], P[3], P[4]};
            *(u32x4*)(ub + 2 * UCP) = (u32x4){Q[1], Q[2], Q[3], Q[4]};
            *(u32x4*)(ub + 3 * UCP) = (u32x4){P[2], P[3], P[4], P[5]};
            *(u32x4*)(smem + KB_OFF + tid * 16) = kv;
        }
        __syncthreads();
        MARK(1);
        const int c = lane & 15, g = lane >> 4;
        f32x4 acc[8];
#pragma unroll
        for (int j = 0; j < 8; ++j) acc[j] = (f32x4){0.f, 0.f, 0.f, 0.f};
        {
            const u32 lb = (u32)(uintptr_t)(LAS char*)smem;
            const u32 ka = lb + KB_OFF + 2 * (2076 - 4 * c + 8 * g - 64 * wv) - 3584 - 64;
            const u32 ua = lb + (c & 3) * UCP + 2 * (8 * g + 64 * (c >> 2) - 64 * wv + 192);
            u32x2 kl[2][8], kh[2][8]; u32x4 uf[2][8];
            auto rd = [&](int rr, int n) {
                asm volatile("ds_read_b64 %0, %3 offset:%5\n\tds_read_b64 %1, %3 offset:%6\n\tds_read_b128 %2, %4 offset:%7"
                             : "=&v"(kl[rr][n]), "=&v"(kh[rr][n]), "=&v"(uf[rr][n]) : "v"(ka), "v"(ua),
                               "n"((7 - n) * 512 + (1 - rr) * 64), "n"((7 - n) * 512 + (1 - rr) * 64 + 8), "n"(n * 512 + (1 - rr) * 64) : "memory");
            };
            auto mm = [&](int rr, int i, int dl) {
                const u32x4 k = {kl[rr][i][0], kl[rr][i][1], kh[rr][i][0], kh[rr][i][1]};
                acc[i + dl] = __builtin_amdgcn_mfma_f32_16x16x32_f16(__builtin_bit_cast(f16x8, uf[rr][dl]), __builtin_bit_cast(f16x8, k), acc[i + dl], 0, 0, 0);
            };
            auto shell = [&](int rr, int n) {
#pragma unroll
                for (int o = 0; o <= n && o + n < 8; ++o) { mm(rr, n, o); if (o < n) mm(rr, o, n); }
            };
            auto pin = [&](int rr, int n) { asm volatile("" : "+v"(kl[rr][n]), "+v"(kh[rr][n]), "+v"(uf[rr][n])); };
            rd(0, 0); rd(0, 7); rd(0, 1); rd(0, 6); rd(0, 2); rd(0, 5); rd(0, 3); rd(0, 4);
#pragma unroll
            for (int n = 7; n >= 0; --n) {
                if (n >= 4) { asm volatile("s_waitcnt lgkmcnt(%0)" :: "n"(n == 7 ? 15 : 6 * (n - 4) + 3 * (7 - n)) : "memory"); pin(0, n); pin(0, 7 - n); }
                shell(0, n);
                __builtin_amdgcn_sched_barrier(0);
                rd(1, 7 - n);
            }
#pragma unroll
            for (int n = 0; n < 8; ++n) {
                asm volatile("s_waitcnt lgkmcnt(%0)" :: "n"(n < 3 ? 15 : 3 * (7 - n)) : "memory");
                pin(1, n);
                shell(1, n);
                __builtin_amdgcn_sched_barrier(0);
            }
        }
        __syncthreads();
        MARK(2);
#pragma unroll
        for (int j = 0; j < 8; ++j) *(f32x4*)(smem + wv * 8192 + j * 1024 + lane * 16) = acc[j];
        __syncthreads();
        f32x4 y0 = *(const f32x4*)(smem + tid * 32), y1 = *(const f32x4*)(smem + tid * 32 + 16);
#pragma unroll
        for (int w = 1; w < 4; ++w) {
            y0 += *(const f32x4*)(smem + w * 8192 + tid * 32);
            y1 += *(const f32x4*)(smem + w * 8192 + tid * 32 + 16);
        }
        const float y[8] = {y0[0], y0[1], y0[2], y0[3], y1[0], y1[1], y1[2], y1[3]};
        u32x4 o;
#pragma unroll
        for (int k = 0; k < 4; ++k) {
            const u32 yb = pkbf(y[2 * k] * oscale, y[2 * k + 1] * oscale);
            const u32 sk = pkbf(bflo(hb[k]) * dsk, bfhi(hb[k]) * dsk);
            const u32 z = pkbf(bflo(yb) + bflo(sk), bfhi(yb) + bfhi(sk));
            const u32 gz = pkbf(bflo(z) * ga, bfhi(z) * ga);
            o[k] = pkbf(bflo(x0[k]) + bflo(gz), bfhi(x0[k]) + bfhi(gz));
        }
        MARK(3);
        st16(ws, WS_X1F + (tid >> 4) * 147456 + ((d / 48) * 12 + ((tid >> 2) & 3) * 3 + (d >> 4) % 3) * 1024 + (d & 15) * 64 + (tid & 3) * 16, o);
    }
    asm volatile("k_conv_end:");
}
constexpr u32 L_EXF = 147456;
constexpr int MLP_LDS = L_EXF + 12288;
constexpr int GE = 648;

#if defined(__HIP_DEVICE_COMPILE__)
#pragma clang attribute push(__attribute__((target("no-packed-fp32-ops"))), apply_to = function)
#endif
template <int N> struct IC { static constexpr int value = N; };
template <int A, int B, class F> DI void static_for(F&& f) {
    if constexpr (B - A == 1) f(IC<A>{});
    else if constexpr (A < B) { static_for<A, (A + B) / 2>(f); static_for<(A + B) / 2, B>(f); }
}
constexpr int imin(int a, int b) { return a < b ? a : b; }
constexpr int XW = 6;
#define XSP 7
#define XIV 8
constexpr int XI = XIV;
constexpr int x_dmas(int j) { return j <= 0 ? 0 : imin((4 * j + XSP - 1) / XSP, 3 * (19 - XI)); }
constexpr int G1 = 36;
#define PLG 614
constexpr int P1 = 36 * 5 + 7, P2 = 36 * 7 + 7, P3 = 400;
#define PREF 0
#define CD1 34
#define CD3 30
#define QW3 8
constexpr int C1 = P1 + CD1, C2 = P2 + CD1, C3 = P3 + CD3;
constexpr bool s_poll(int G) { return G == P1 || G == P2 || G == P3; }
#define PB2 606
#define PL4 640
constexpr int PUB2 = PB2, P4 = PL4;
constexpr int HS0 = 22, HS1 = 35;
constexpr int s_hstore(int G) { return G < 72 ? (G == 64 ? 0 : G == 71 ? 1 : -1) : G < 324 ? (G % 36 == HS0 ? 0 : G % 36 == HS1 ? 1 : -1) : G == 338 ? 0 : G == 346 ? 1 : -1; }
constexpr int B0 = 324 + 7 * NA;
constexpr int slot_end(int sg) { return B0 - 1 + (sg == 1 ? 2 : sg == 0 ? 3 : 2 * sg + 1) * NB; }
constexpr int s_pstore(int G) {
    for (int sg = 0; sg < 4; ++sg) { if (G == slot_end(sg) + 4) return 2 * sg; if (G == slot_end(sg) + 6 && sg > 0) return 2 * sg + 1; }
    return -1;
}
constexpr bool s_res(int G) { return G == 620 || G == 622 || G == 624; }
constexpr int s_pub(int G) { return G == 36 * 3 + 24 ? 2 : G == 36 * 5 + 24 ? 4 : G == 372 ? 9 : 0; }
constexpr int m_slot(int m) { return m < 7 * NA ? (m % 7 + 1) / 2 : m < 7 * NA + 2 * NB ? 1 : m < 7 * NA + 3 * NB ? 0 : m < 252 ? 2 + (m - 7 * NA - 3 * NB) / (2 * NB) : 4; }
constexpr int m_ent(int m) { return m < 7 * NA ? m / 7 : m < 7 * NA + 2 * NB ? NA + (m - 7 * NA) / 2 : m < 7 * NA + 3 * NB ? NA + (m - 7 * NA - 2 * NB)
                                  : m < 252 ? NA + ((m - 7 * NA - 3 * NB) % (2 * NB)) / 2 : (m - 252) / 2; }
constexpr int m_half(int m) { return m < 7 * NA ? (m % 7 == 0 ? 0 : (m % 7 - 1) & 1) : m < 7 * NA + 2 * NB ? (m - 7 * NA) & 1 : m < 7 * NA + 3 * NB ? 0
                                   : m < 252 ? (m - 7 * NA - 3 * NB) & 1 : m & 1; }
constexpr bool m_first(int m) { return m < 7 || m == 252 || m == 253; }
constexpr bool ent_first(int m) { return m < 7 * NA ? m % 7 == 0 : (m < 7 * NA + 2 * NB && ((m - 7 * NA) & 1) == 0); }
constexpr bool ent_own(int e) { return e < 9; }
constexpr int first_use(int e) { return e < NA ? 324 + 7 * e : B0 + 2 * (e - NA); }
constexpr bool s_ring(int G) { return G + 12 < GE && (G < 100 ? G % 12 == 0 : G < 490 ? G % 36 == 24 : G % 18 == 6); }
constexpr int ring_slot(int grp) { const int u = (grp / 3) & 3; return (u == 0 ? 0 : u == 1 ? 6 : u == 2 ? 9 : 3) + grp % 3; }
constexpr int need_bar(int grp) { int g = 12 * grp - 12; while (g > 0 && !s_ring(g)) --g; return g; }
struct Sched {
    int dn[GE];
    int dgap[162];
    int hl[GE];
    int pre[GE + 1];
    int hgap[36];
    bool ok;
    constexpr Sched() : dn(), dgap(), hl(), pre(), hgap(), ok(true) {
        int nd = 3 * XW, nh = 0, last2 = -99, lastbar = 24;
        for (int n = 0; n < 162; ++n) dgap[n] = -1;
        for (int e = 0; e < 36; ++e) hgap[e] = -1;
        for (int G = 0; G < GE; ++G) {
            dn[G] = -1; hl[G] = -1;
            int n = 0;
            if (G >= G1) {
                if (s_ring(G)) lastbar = G;
                const bool fixed = s_hstore(G) >= 0 || s_pstore(G) >= 0 || s_res(G);
                const bool quiet = (G >= P1 && G < P1 + 17) || (G >= P2 && G < P2 + 17) || (G >= P3 && G < P3 + QW3);
                const int sp2 = G < 324 ? 4 : 5;
                if (fixed) last2 = 2 * G;
                else if (!quiet && 2 * G - last2 >= sp2) {
                    int dd = 1 << 20, dh = 1 << 20;
                    if (nd < 162 && (nd / 3 < 12 || (nd / 3 - 11) * 12 <= lastbar + 6)) dd = need_bar(nd / 3) - 40;
                    const int lim = G > C3 ? 54 : G > C2 ? 24 : G > C1 ? 12 : 0;
                    if (nh < lim) dh = first_use(9 + nh / 2) - 45;
                    if (dd < (1 << 20) || dh < (1 << 20)) {
                        if (dd <= dh) { dn[G] = nd; dgap[nd] = G; if (need_bar(nd / 3) <= G) ok = false; ++nd; }
                        else { hl[G] = 18 + nh; hgap[9 + nh / 2] = G; if (first_use(9 + nh / 2) <= G) ok = false; ++nh; }
                        last2 = last2 + sp2 > 2 * G - 1 ? last2 + sp2 : 2 * G - (G < 324 ? 0 : 1);
                        n = 1;
                    }
                }
                n += fixed + s_poll(G) + (G == P4) + (s_pub(G) > 0) + (G == PUB2);
            }
            pre[G + 1] = pre[G] + n;
        }
        if (nd != 162 || nh != 54) ok = false;
    }
};
constexpr Sched SCH{};
static_assert(SCH.ok, "schedule misses a deadline");
constexpr int younger(int a, int b) { return imin(SCH.pre[b] - SCH.pre[a + 1], 63); }
constexpr bool s_bar(int G) { return G >= G1 && (s_ring(G) || s_pub(G) > 0 || G == PUB2); }
constexpr int bar_wait(int G) {
    int n = 63;
    if (s_ring(G)) { int grp = 53; while (need_bar(grp) > G) --grp; n = G == 36 ? 3 : younger(SCH.dgap[3 * grp + 2], G); }
    if (G == PUB2) n = imin(n, younger(slot_end(3) + 6, G));
    if (s_pub(G) > 0) n = imin(n, younger(G == 372 ? 346 : 36 * s_pub(G) + HS1, G));
    return n;
}
constexpr int frag_lds(int f) { return ring_slot(f / 12) * 12288 + (f % 12) * 1024; }

constexpr u32 RESL = ring_slot(45) * 12288;
static_assert(SCH.dgap[161] < 620, "M0");
#define OAUX 0
#define PGX 55
#define PG0 95
#define PG1 192
#define PG2 285
#define PG3 388
#define PG4 517
#define PG5 641
#define PADS 1
constexpr int pad_at(int G) { return G == PG0 ? 0 : G == PG1 ? 1 : G == PG2 ? 2 : G == PG3 ? 3 : G == PG4 ? 4 : G == PG5 ? 5 : -1; }
#define SB() __builtin_amdgcn_sched_barrier(0)
#define STAMP(i) do {} while (0)
#define TW(code) do { code; } while (0)
#define MFMA "v_mfma_f32_16x16x32_bf16 "

extern "C" __global__ __attribute__((aligned(4096))) __launch_bounds__(256, 1) void k_mlp(char* __restrict__ ws, u16* __restrict__ out) {
    extern __shared__ __attribute__((aligned(16))) char smem[];
    const int tid = threadIdx.x, lane = tid & 63;
    const int w = __builtin_amdgcn_readfirstlane(tid >> 6);
    const int b = blockIdx.x, xcd = b & 7, rr = b >> 3;
#define MAPV 1
    const int s = rr & 3;
    const int kq = MAPV == 0 ? xcd >> 1 : MAPV == 1 ? 2 * (xcd >> 2) + ((rr >> 2) & 1) : (rr >> 2) & 3;
    const int it = MAPV == 0 ? 8 * (xcd & 1) + (rr >> 2) : MAPV == 1 ? 4 * (xcd & 3) + (rr >> 3) : 2 * xcd + (rr >> 4);
    const int q = 4 * kq + s;
    const auto rs = __builtin_amdgcn_make_buffer_rsrc(ws, 0, WS_END, 0x00020000);
    const u32x4 srd = {(u32)(uintptr_t)ws, (u32)((uintptr_t)ws >> 32) & 0xffffu, WS_END, 0x00020000u};
    const u32 voff = lane * 16;
    const u32 lbase = (u32)(uintptr_t)(LAS char*)smem;
    const u32 wl = lbase + w * 3072;
    auto set_m0 = [&](u32 v) { asm volatile("s_mov_b32 m0, %0" :: "s"(v) : "memory"); };
    auto dma = [&](auto O, u32 so) {
        const u32 vo = voff; const u32x4 sr = srd;
        asm volatile("buffer_load_dwordx4 %0, %1, %2 offen offset:%3 lds" :: "v"(vo), "s"(sr), "s"(so), "n"(O.value * 1024) : "memory");
    };
    auto dma3 = [&](u32 dst, u32 so) {
        asm volatile("s_mov_b32 m0, %3\n\ts_nop 0\n\tbuffer_load_dwordx4 %0, %1, %2 offen lds\n\t"
                     "buffer_load_dwordx4 %0, %1, %2 offen offset:1024 lds\n\tbuffer_load_dwordx4 %0, %1, %2 offen offset:2048 lds"
                     :: "v"(voff), "s"(srd), "s"(so), "s"(dst) : "memory");
    };
    const u32 xsb = WS_X1F + it * 147456;
    const u32 wfb = WS_WF + q * 663552 + w * 3072;
    auto rq_dst = [&](int p) -> u32 {
        const int x = p <= 2 ? p - 1 : p <= 6 ? p - 2 : p - 3;
        return wl + (p == 0 ? 0 : p == 3 ? 12288 : p == 7 ? 24576 : p == 15 ? L_EXF : p > 15 ? ring_slot(p - 13) * 12288 : (3 + x % 9) * 12288);
    };
    auto rq_src = [&](int p) -> u32 {
        const int x = p <= 2 ? p - 1 : p <= 6 ? p - 2 : p - 3;
        return p == 0 ? wfb : p == 3 ? wfb + 12288 : p == 7 ? wfb + 24576 : p == 15 ? WS_EX + q * 8192 + w * 3072
             : p > 15 ? wfb + (p - 13) * 12288 : xsb + w * 3072 + x * 12288;
    };
#pragma unroll
    for (int p = 0; p < XI; ++p) {
        dma3(rq_dst(p), rq_src(p));
        if (PADS && p == 2) {
            if (w == 0) asm volatile("s_call_b64 s[30:31], k_mlp_pad0\n\ts_call_b64 s[30:31], k_mlp_pad4" ::: "s30", "s31");
            if (w == 1) asm volatile("s_call_b64 s[30:31], k_mlp_pad1\n\ts_call_b64 s[30:31], k_mlp_pad5" ::: "s30", "s31");
            if (w == 2) asm volatile("s_call_b64 s[30:31], k_mlp_pad6\n\ts_call_b64 s[30:31], k_mlp_pad2" ::: "s30", "s31");
            if (w == 3) asm volatile("s_call_b64 s[30:31], k_mlp_pad3" ::: "s30", "s31");
        }
    }
    set_m0(rq_dst(XI));
    const u32 hfb = WS_HF + (it * 4 + kq) * 36 * 8192 + w * 2048;
    u32x4 Wf[18], Ef[2];
    const u32 rd0 = lbase + voff;
    const u32 rb[3] = {rd0, rd0 + 49152, rd0 + 98304};
    auto rdw = [&](auto F) {
        constexpr int lo = frag_lds(F.value);
        u32x4& d = Wf[F.value % 18]; const u32 base = rb[lo / 49152];
        asm volatile("ds_read_b128 %0, %1 offset:%2" : "=v"(d) : "v"(base), "n"(lo % 49152) : "memory");
    };
    const u32 rex = lbase + L_EXF + (lane & 15) * 16;
    auto rde = [&](auto C) {
        u32x4& d0 = Ef[0]; u32x4& d1 = Ef[1]; const u32 base = rex;
        asm volatile("ds_read_b128 %0, %2 offset:%3\n\tds_read_b128 %1, %2 offset:%4" : "=&v"(d0), "=&v"(d1) : "v"(base), "n"(C.value * 512), "n"(C.value * 512 + 256) : "memory");
    };
    f32x4 acc[2][4];

    u32x4 Xa[36];
    f32x4 gram[2], rsum[2];
    {
        const u32 a0 = wl + (4 * (lane >> 4) + ((lane & 15) >> 2)) * 64 + (lane & 3) * 8;
        u32x4 ones = {0x3f803f80u, 0x3f803f80u, 0x3f803f80u, 0x3f803f80u};
        asm volatile("" : "+v"(ones));
        u32x2 t[3][4];
        const u32 xb[3] = {a0 + 36864, a0 + 73728, a0 + 110592};
        auto xrd = [&](auto KS, auto I) {
            constexpr int hk = 2 * KS.value + (I.value >> 1), sl = (hk / 3) % 9;
            u32x2& d = t[KS.value % 3][I.value]; const u32 base = xb[sl / 3];
            asm volatile("ds_read_b64_tr_b16 %0, %1 offset:%2" : "=v"(d) : "v"(base), "n"((sl % 3) * 12288 + (hk % 3) * 1024 + (I.value & 1) * 32) : "memory");
        };
        auto admit = [&](auto KS, auto J) {
            constexpr int ks = KS.value, x = (2 * ks + 1) / 3, pos = x <= 1 ? x + 1 : x <= 4 ? x + 2 : x + 3;
            constexpr int issued = 3 * XI + x_dmas(J.value);
            static_assert(issued >= 3 * pos + 3, "X order");
            if constexpr (ks % 6 == 0) asm volatile("s_waitcnt vmcnt(%0)\n\ts_barrier" :: "n"(issued - 3 * pos - 3) : "memory");
            else if constexpr (ks % 3 != 2) asm volatile("s_waitcnt vmcnt(%0)" :: "n"(issued - 3 * pos - 3) : "memory");
        };
        static_for<0, 2>([&](auto KS) {
            admit(KS, IC<0>{});
            static_for<0, 4>([&](auto I) { xrd(KS, I); });
            rdw(IC<2 * KS.value>{}); rdw(IC<2 * KS.value + 1>{});
        });
        STAMP(1);
        u32x4 xv[2];
        static_for<0, 72>([&](auto J) {
            constexpr int j = J.value, ks = j / 4, k = j % 4;
            if constexpr (PADS && j == PGX) asm volatile("s_branch 1f\n\t.p2align 12\nk_mlp_pad6:\n\ts_setpc_b64 s[30:31]\n1:");
            u32x2 (&d)[4] = t[ks % 3];
            u32x4& x0 = xv[0]; u32x4& x1 = xv[1]; u32x4& on = ones;
            if constexpr (k == 0) {
                asm volatile("s_waitcnt lgkmcnt(%4)" : "+v"(d[0]), "+v"(d[1]), "+v"(d[2]), "+v"(d[3]) : "n"(ks < 17 ? 6 : 0) : "memory");
                xv[0] = (u32x4){d[0][0], d[0][1], d[2][0], d[2][1]};
                xv[1] = (u32x4){d[1][0], d[1][1], d[3][0], d[3][1]};
                asm volatile("s_nop 1" : "+v"(x0), "+v"(x1));
            }
            f32x4& c0 = k < 2 ? acc[0][2 * (k & 1)] : k == 2 ? gram[0] : rsum[0];
            f32x4& c1 = k < 2 ? acc[0][2 * (k & 1) + 1] : k == 2 ? gram[1] : rsum[1];
            u32x4& am0 = k < 2 ? Wf[(2 * ks + k) % 18] : k == 2 ? x0 : on;
            u32x4& am1 = k < 2 ? Wf[(2 * ks + k) % 18] : k == 2 ? x1 : on;
            if constexpr (j < 4) asm volatile(MFMA "%0, %1, %2, 0" : "=&v"(c0) : "v"(am0), "v"(x0));
            else asm volatile(MFMA "%0, %1, %2, %0" : "+v"(c0) : "v"(am0), "v"(x0));
            SB();
            if constexpr (ks < 16) {
                if constexpr (k == 0) admit(IC<ks + 2>{}, J);
                if constexpr (k < 2) { xrd(IC<ks + 2>{}, IC<2 * k>{}); xrd(IC<ks + 2>{}, IC<2 * k + 1>{}); }
                else rdw(IC<2 * ks + 2 + k>{});
            }
            SB();
            if constexpr (j < 4) asm volatile(MFMA "%0, %1, %2, 0" : "=&v"(c1) : "v"(am1), "v"(x1));
            else asm volatile(MFMA "%0, %1, %2, %0" : "+v"(c1) : "v"(am1), "v"(x1));
            SB();
            if constexpr (k >= 2) { Xa[2 * ks + k - 2] = xv[k - 2]; asm volatile("" : "+a"(Xa[2 * ks + k - 2])); }
            if constexpr (x_dmas(j + 1) > x_dmas(j)) {
                constexpr int n = x_dmas(j), p = XI + n / 3;
                static_assert(p < 12 || p == 15 || (3 * (p < 15 ? p - 12 : p - 13) + 2) / 2 <= ks, "slot still holds unread x");
                dma(IC<n % 3>{}, rq_src(p));
                if constexpr (n % 3 == 2 && p < 18) set_m0(rq_dst(p + 1));
            }
            SB();
        });
    }
    float ysc[2];
    u32x4 Xe[2];
    {
        asm volatile("s_waitcnt vmcnt(6)\n\ts_barrier" ::: "memory");
        STAMP(2);
        static_for<36, 48>(rdw);
        rde(IC<0>{});
        set_m0(wl + ring_slot(XW) * 12288);
        SB();
    }
    float sq[2], mean[2];
    auto stats = [&](auto ST) {
        constexpr int st = ST.value >> 1, t = ST.value & 1;
        if constexpr (st == 0) {
            const float a = (lane & 1) ? gram[t][1] : gram[t][0], c = (lane & 1) ? gram[t][3] : gram[t][2];
            float v = (lane & 2) ? c : a;
            const u32 src = ((lane & 15) + 4 * (lane & 12)) * 4;
            float& d = sq[t];
            asm volatile("ds_bpermute_b32 %0, %1, %2" : "=v"(d) : "v"(src), "v"(v) : "memory");
            mean[t] = rsum[t][0] * (1.f / DM);
            asm volatile("" : "+v"(mean[t]));
        }
        if constexpr (st == 1) { asm volatile("" : "+v"(sq[t])); sq[t] = rbf(rbf(fmaxf(fmaf(-mean[t], mean[t], sq[t] * (1.f / DM)), 0.f)) + 1e-5f); asm volatile("" : "+v"(sq[t])); }
        if constexpr (st == 2) { sq[t] = rbf(__builtin_amdgcn_sqrtf(sq[t])); ysc[t] = -1.44269504088896340736f * __builtin_amdgcn_rcpf(sq[t]); asm volatile("" : "+v"(sq[t]), "+v"(ysc[t])); }
        if constexpr (st == 3) {
            const bool lo = lane < 16;
            Xe[t] = (u32x4){lo ? pkbf(mean[t], mean[t]) : 0u, lo ? pkbf(mean[t], sq[t]) : 0u, lo ? pkbf(sq[t], sq[t]) : 0u, 0u};
            asm volatile("" : "+v"(Xe[t]));
        }
    };
    auto corr = [&](auto C) {
        f32x4 (&a)[4] = acc[C.value & 1]; u32x4 (&e)[2] = Ef; u32x4 (&x)[2] = Xe;
        asm volatile(MFMA "%0, %4, %6, %0\n\t" MFMA "%1, %4, %7, %1\n\t" MFMA "%2, %5, %6, %2\n\t" MFMA "%3, %5, %7, %3"
                     : "+v"(a[0]), "+v"(a[1]), "+v"(a[2]), "+v"(a[3]) : "v"(e[0]), "v"(e[1]), "v"(x[0]), "v"(x[1]));
    };

    f32x4 acc2[5][4];
    float yv[16], ev[16];
    u32x4 H[36][2], pollv;
    auto flag_store = [&](u32 level, u32 so) {
        asm volatile("s_mov_b64 exec, 1\n\tbuffer_store_dword %0, off, %1, %2 sc1\n\ts_mov_b64 exec, -1" :: "v"(level), "s"(srd), "s"(so) : "memory");
    };
    auto act = [&](auto C, auto HS, auto PT) {
        constexpr int t = HS.value / 2, e = C.value, pt = PT.value;
        if constexpr (HS.value % 2 == 0) {
            if constexpr (pt != 1 && t < 16) { yv[t] = acc[e & 1][2 * ((t >> 2) & 1) + (t >> 3)][t & 3] * ysc[t >> 3]; asm volatile("" :: "v"(yv[t])); }
            if constexpr (pt != 0 && t >= 2) { yv[t - 2] = yv[t - 2] * ev[t - 2]; asm volatile("" :: "v"(yv[t - 2])); }
            if constexpr (pt != 0 && t < 16) { ev[t] = __builtin_amdgcn_exp2f(yv[t]); asm volatile("" :: "v"(ev[t])); }
        } else {
            if constexpr (pt != 1 && t >= 1 && t < 17) { ev[t - 1] = 1.f + ev[t - 1]; asm volatile("" :: "v"(ev[t - 1])); }
            if constexpr (pt != 0 && t >= 3 && (t & 1)) {
                u32 pk = pkbf(yv[t - 3], yv[t - 2]);
                asm volatile("" :: "v"(pk));
                H[e][(t - 3) / 8][((t - 3) / 2) & 3] = pk;
            }
            if constexpr (pt != 0 && t >= 1 && t < 17) { ev[t - 1] = __builtin_amdgcn_rcpf(ev[t - 1]); asm volatile("" :: "v"(ev[t - 1])); }
        }
    };
    const u32 hown = hfb + 9 * s * 8192;
    const u32 hpeer[3] = {hfb + 9 * ((s + 1) & 3) * 8192, hfb + 9 * ((s + 2) & 3) * 8192, hfb + 9 * ((s + 3) & 3) * 8192};
    auto hload = [&](auto E, auto HF) {
        constexpr int e = E.value, hf = HF.value, k = e - 9;
        const u32 so = hpeer[k % 3] + (k / 3) * 8192, vo = voff; const u32x4 sr = srd;
        u32x4& d = H[e][hf];
        asm volatile("buffer_load_dwordx4 %0, %1, %2, %3 offen offset:%4 sc1" : "=a"(d) : "v"(vo), "s"(sr), "s"(so), "n"(hf * 1024) : "memory");
    };
    const u32 vzero = 0;
    const u32 fso1 = WS_FLAG + (it * 4 + kq) * 64;
    auto poll = [&]() {
        asm volatile("buffer_load_dwordx4 %0, %1, %2, %3 offen sc1" : "=v"(pollv) : "v"(vzero), "s"(srd), "s"(fso1) : "memory");
    };
    auto check = [&](u32 level) {
        asm volatile("" : "+v"(pollv));
#pragma nounroll
        for (u32 spins = 0; spins < (1u << 20); ++spins) {
            const bool bad = pollv[0] < level || pollv[1] < level || pollv[2] < level || pollv[3] < level;
            if (__builtin_amdgcn_ballot_w64(bad) == 0ull) break;
            __builtin_amdgcn_s_sleep(1);
            asm volatile("buffer_load_dwordx4 %0, %1, %2, %3 offen sc1\n\ts_waitcnt vmcnt(0)"
                         : "=v"(pollv) : "v"(vzero), "s"(srd), "s"(fso1) : "memory");
        }
    };
    const u32 pgb = WS_PART + (it * 4 + s) * 98304 + w * 2048;
    const u32 phb = WS_PH + (it * 4 + s) * 16384 + w * 4096;
    auto xfrag = [&](int ks) -> u32 { return xsb + ((ks / 3) * 12 + 3 * w + ks % 3) * 1024; };
    const u32 fso2 = WS_FLAG + 4096 + (it * 4 + s) * 64;
    u32 fv;
    const u32 vfl = (lane & 3) * 4;
    auto poll2 = [&]() {
        asm volatile("buffer_load_dword %0, %1, %2, %3 offen sc1" : "=v"(fv) : "v"(vfl), "s"(srd), "s"(fso2) : "memory");
    };
    auto check2 = [&]() {
        asm volatile("" : "+v"(fv));
#pragma nounroll
        for (u32 spins = 0; spins < (1u << 20); ++spins) {
            if (__builtin_amdgcn_ballot_w64(fv == 0u) == 0ull) break;
            __builtin_amdgcn_s_sleep(1);
            asm volatile("buffer_load_dword %0, %1, %2, %3 offen sc1\n\ts_waitcnt vmcnt(0)" : "=v"(fv) : "v"(vfl), "s"(srd), "s"(fso2) : "memory");
        }
    };
    static_for<G1, GE>([&](auto GG) {
        constexpr int G = GG.value, c = G / 36, g = G % 36, m = G - 324;
        if constexpr (g == 0 && G > 0) STAMP(2 + G / 36);
        if constexpr (PADS && pad_at(G) >= 0) asm volatile("s_branch 1f\n\t.p2align 12\nk_mlp_pad%c0:\n\ts_setpc_b64 s[30:31]\n1:" :: "n"(pad_at(G)));
        if constexpr (s_bar(G)) {
            if constexpr (G % 6 == 0) TW(asm volatile("s_waitcnt vmcnt(%0) lgkmcnt(%1)\n\ts_barrier" :: "n"(bar_wait(G)), "n"(G == G1 ? 8 : 6) : "memory"));
            else TW(asm volatile("s_waitcnt vmcnt(%0)\n\ts_barrier" :: "n"(bar_wait(G)) : "memory"));
        } else if constexpr (G % 6 == 0) asm volatile("s_waitcnt lgkmcnt(%0)" :: "n"(G == 42 ? 4 : G + 6 < GE ? 6 : 0) : "memory");
        u32x4& wf = Wf[G % 18];
        constexpr int sl = G < 324 ? 0 : m_slot(m), e = G < 324 ? 0 : m_ent(m), tl = G < 324 ? (g & 1) : m_half(m);
        constexpr bool first = G < 324 ? g < 2 : m_first(m), own = G >= 324 && ent_own(e);
        if constexpr (G >= 324) {
            if constexpr (ent_first(m) && !own) TW(asm volatile("s_waitcnt vmcnt(%0)" :: "n"(younger(SCH.hgap[e], G)) : "memory"));
        }
        auto mm = [&](auto TT) {
            constexpr int tt = TT.value;
            f32x4& ac = G < 324 ? acc[c & 1][2 * tl + tt] : acc2[sl][2 * tl + tt];
            u32x4& a = wf; u32x4& bo = G < 324 ? Xa[(g & ~1) + tt] : H[e][tt];
            if constexpr (first && own) asm volatile(MFMA "%0, %1, %2, 0" : "=&v"(ac) : "v"(a), "v"(bo));
            else if constexpr (first) asm volatile(MFMA "%0, %1, %2, 0" : "=&v"(ac) : "v"(a), "a"(bo));
            else if constexpr (own) asm volatile(MFMA "%0, %1, %2, %0" : "+v"(ac) : "v"(a), "v"(bo));
            else asm volatile(MFMA "%0, %1, %2, %0" : "+v"(ac) : "v"(a), "a"(bo));
            SB();
        };
        constexpr int hs = G >= 72 && G < 324 && g >= 1 ? (g < 12 ? g - 1 : g) : -1;
        mm(IC<0>{});
        if constexpr (G + 12 < GE) rdw(IC<G + 12>{});
        if constexpr (hs >= 0) act(IC<c - 1>{}, IC<hs>{}, IC<0>{});
        if constexpr (G >= 52 && G < 70) act(IC<0>{}, IC<2 * (G - 52)>{}, IC<2>{});
        if constexpr (G >= 326 && G < 344) act(IC<8>{}, IC<2 * (G - 326)>{}, IC<2>{});
        SB();
        mm(IC<1>{});
        if constexpr (G == 50) { corr(IC<0>{}); SB(); rde(IC<1>{}); }
        if constexpr (G < 324 && g == (c == 1 ? 30 : 11)) {
            if constexpr (c > 1) { act(IC<c - 1>{}, IC<10>{}, IC<1>{}); SB(); }
            corr(IC<c>{}); SB();
            if constexpr (c > 1) act(IC<c - 1>{}, IC<11>{}, IC<2>{});
        } else if constexpr (hs >= 0) act(IC<c - 1>{}, IC<hs>{}, IC<1>{});
        if constexpr (g == (c == 1 ? 32 : 14) && c < 8) rde(IC<c + 1>{});
        if constexpr (SCH.dn[G] >= 0) {
            dma(IC<SCH.dn[G] % 3>{}, wfb + (SCH.dn[G] / 3) * 12288);
            if constexpr (SCH.dn[G] % 3 == 2 && SCH.dn[G] < 161) set_m0(wl + ring_slot(SCH.dn[G] / 3 + 1) * 12288);
        }
        if constexpr (G == 36 || G == 37) stats(IC<G - 36>{});
        if constexpr (G >= 43 && G < 49) stats(IC<G - 41>{});
        if constexpr (G >= 52 && G < 70) act(IC<0>{}, IC<2 * (G - 52) + 1>{}, IC<2>{});
        if constexpr (G >= 326 && G < 344) act(IC<8>{}, IC<2 * (G - 326) + 1>{}, IC<2>{});
        if constexpr (s_hstore(G) >= 0) {
            constexpr int hf = s_hstore(G), cc = c - 1;
            __builtin_amdgcn_raw_buffer_store_b128(H[cc][hf], rs, voff + hf * 1024, hown + cc * 8192, 16);
        }
        if constexpr (s_pub(G) > 0) flag_store(s_pub(G), fso1 + s * 4);
        if constexpr (G == PUB2) flag_store(1u, fso2 + kq * 4);
        if constexpr (G == C1 || G == C2 || G == C3) {
            TW(asm volatile("s_waitcnt vmcnt(%0)" :: "n"(younger(G == C1 ? P1 : G == C2 ? P2 : P3, G)) : "memory"));
            check(G == C1 ? 2 : G == C2 ? 4 : 9);
        }
        if constexpr (G == P1 || G == P2 || G == P3) poll();
        if constexpr (G == P4) poll2();
        if constexpr (SCH.hl[G] >= 0) hload(IC<SCH.hl[G] / 2>{}, IC<SCH.hl[G] % 2>{});
        if constexpr (s_pstore(G) >= 0) {
            constexpr int sg = s_pstore(G) / 2, pt = s_pstore(G) % 2;
            const f32x4 a0 = acc2[sg][2 * pt], a1 = acc2[sg][2 * pt + 1];
            const u32x4 v = {pkbf(a0[0], a0[1]), pkbf(a0[2], a0[3]), pkbf(a1[0], a1[1]), pkbf(a1[2], a1[3])};
            if constexpr (sg == 0) __builtin_amdgcn_raw_buffer_store_b128(v, rs, voff, phb + kq * 1024, 16);
            else __builtin_amdgcn_raw_buffer_store_b128(v, rs, voff + pt * 1024, pgb + ((kq + sg) & 3) * 24576 + (sg - 1) * 8192, 16);
        }
        if constexpr (s_res(G)) {
            constexpr int i = (G - 620) / 2;
            set_m0(wl + RESL + i * 1024);
            const u32 so = xfrag(9 * s + (i < 2 ? 2 * kq + i : 8));
            asm volatile("s_nop 0" ::: "memory");
            dma(IC<0>{}, so);
        }
        SB();
    });
    STAMP(20);

    {
        u32x2 xr[5];
        f32x4 gb[2];
        f32x2 gh;
        const u32 ra = wl + RESL + (4 * (lane >> 4) + ((lane & 15) >> 2)) * 64 + (lane & 3) * 8;
        const u32 ga = lbase + L_EXF + 4608 + (lane >> 4) * 16;
        asm volatile("s_waitcnt vmcnt(1)" ::: "memory");
        asm volatile("ds_read_b64_tr_b16 %0, %5\n\tds_read_b64_tr_b16 %1, %5 offset:32\n\tds_read_b64_tr_b16 %2, %5 offset:1024\n\t"
                     "ds_read_b64_tr_b16 %3, %5 offset:1056\n\tds_read_b64_tr_b16 %4, %6 offset:2048"
                     : "=&v"(xr[0]), "=&v"(xr[1]), "=&v"(xr[2]), "=&v"(xr[3]), "=&v"(xr[4]) : "v"(ra), "v"(ra + (kq >> 1) * 32) : "memory");
        asm volatile("ds_read_b128 %0, %3\n\tds_read_b128 %1, %3 offset:64\n\tds_read_b64 %2, %4 offset:128\n\ts_waitcnt vmcnt(0) lgkmcnt(0)"
                     : "=&v"(gb[0]), "=&v"(gb[1]), "=&v"(gh) : "v"(ga), "v"(ga + (kq & 1) * 8) : "memory");
        asm volatile("" : "+v"(xr[0]), "+v"(xr[1]), "+v"(xr[2]), "+v"(xr[3]), "+v"(xr[4]));
        check2();
        STAMP(21);
        u32x4 P[6];
        u32 Q[4];
        const u32 pin = pgb + kq * 24576;
        asm volatile("buffer_load_dwordx4 %4, %10, %11, %14 offen sc1\n\tbuffer_load_dwordx4 %5, %10, %11, %14 offen offset:1024 sc1\n\t"
                     "buffer_load_dwordx4 %2, %10, %11, %13 offen sc1\n\tbuffer_load_dwordx4 %3, %10, %11, %13 offen offset:1024 sc1\n\t"
                     "buffer_load_dwordx4 %0, %10, %11, %12 offen sc1\n\tbuffer_load_dwordx4 %1, %10, %11, %12 offen offset:1024 sc1\n\t"
                     "buffer_load_dword %6, %15, %11, %16 offen sc1\n\tbuffer_load_dword %7, %15, %11, %16 offen offset:1024 sc1\n\t"
                     "buffer_load_dword %8, %15, %11, %16 offen offset:2048 sc1\n\tbuffer_load_dword %9, %15, %11, %16 offen offset:3072 sc1\n\t"
                     "s_waitcnt vmcnt(0)"
                     : "=&v"(P[0]), "=&v"(P[1]), "=&v"(P[2]), "=&v"(P[3]), "=&v"(P[4]), "=&v"(P[5]), "=&v"(Q[0]), "=&v"(Q[1]), "=&v"(Q[2]), "=&v"(Q[3])
                     : "v"(voff), "s"(srd), "s"(pin), "s"(pin + 8192), "s"(pin + 16384), "v"(voff + kq * 4), "s"(phb) : "memory");
        STAMP(22);
        const auto ro = __builtin_amdgcn_make_buffer_rsrc(out, 0, SEQ * DM * 2, 0x00020000);
        const u32 ov = ((it * 128 + w * 32 + (lane & 15)) * DM + s * 144 + 4 * (lane >> 4)) * 2;
        auto fin2 = [&](float a0, float a1, float g0, float g1, u32 xk) -> u32 {
            const u32 t = pkbf(a0 * -0.69314718055994530942f, a1 * -0.69314718055994530942f);
            const u32 gz = pkbf(bflo(t) * g0, bfhi(t) * g1);
            return pkbf(bflo(xk) + bflo(gz), bfhi(xk) + bfhi(gz));
        };
#pragma unroll
        for (int k = 0; k < 4; ++k) {
            u32x2 o;
#pragma unroll
            for (int j = 0; j < 2; ++j) {
                const int d = 2 * (k & 1) + j;
                const u32 p0 = P[k >> 1][d], p1 = P[2 + (k >> 1)][d], p2 = P[4 + (k >> 1)][d], xk = xr[k][j];
                o[j] = fin2(acc2[4][k][2 * j] + bflo(p0) + bflo(p1) + bflo(p2), acc2[4][k][2 * j + 1] + bfhi(p0) + bfhi(p1) + bfhi(p2),
                            gb[k >> 1][2 * j], gb[k >> 1][2 * j + 1], xk);
            }
            __builtin_amdgcn_raw_buffer_store_b64(o, ro, ov + (k >> 1) * 32, kq * 64 + (k & 1) * (16 * DM * 2), OAUX);
        }
        const u32 oh = fin2(bflo(Q[0]) + bflo(Q[1]) + bflo(Q[2]) + bflo(Q[3]), bfhi(Q[0]) + bfhi(Q[1]) + bfhi(Q[2]) + bfhi(Q[3]),
                            gh[0], gh[1], (kq & 1) ? xr[4][1] : xr[4][0]);
        __builtin_amdgcn_raw_buffer_store_b32(oh, ro, ov + 256, (kq & 1) * 4 + (kq >> 1) * (16 * DM * 2), OAUX);
    }
    asm volatile("k_mlp_end:");
}
#if defined(__HIP_DEVICE_COMPILE__)
#pragma clang attribute pop
#endif

extern "C" void kernel_launch(void* const* d_in, const int* in_sizes, int n_in, void* d_out, int out_size,
                              void* d_ws, size_t ws_size, hipStream_t stream) {
    const u16* x = (const u16*)d_in[0];
    char* ws = (char*)d_ws;
    (void)hipFuncSetAttribute((const void*)k_mlp, hipFuncAttributeMaxDynamicSharedMemorySize, MLP_LDS);
    k_prep<<<NB_PREP, 256, PREP_LDS, stream>>>(x, (const u16*)d_in[1], (const u16*)d_in[2], (const u16*)d_in[3],
                                               (const u16*)d_in[4], (const u16*)d_in[5], (const u16*)d_in[9],
                                               (const u16*)d_in[10], (const u16*)d_in[11], ws, (const u16*)d_in[8]);
    k_conv<<<NB_PF + NB_CONV + NB_W1, 256, CONV_LDS, stream>>>((const u16*)d_in[6], (const u16*)d_in[8], ws, (const u16*)d_out);
    k_mlp<<<256, 256, MLP_LDS, stream>>>(ws, (u16*)d_out);
}
```

```cpp
#include <hip/hip_runtime.h>
#include <stdint.h>

typedef unsigned short u16;
typedef unsigned int u32;
typedef float f32x2 __attribute__((ext_vector_type(2)));
typedef float f32x4 __attribute__((ext_vector_type(4)));
typedef float f32x16 __attribute__((ext_vector_type(16)));
typedef _Float16 f16x2 __attribute__((ext_vector_type(2)));
typedef _Float16 f16x8 __attribute__((ext_vector_type(8)));
typedef __bf16 bf16x2 __attribute__((ext_vector_type(2)));
typedef __bf16 bf16x8 __attribute__((ext_vector_type(8)));
typedef short s16x4 __attribute__((ext_vector_type(4)));
typedef u32 u32x2 __attribute__((ext_vector_type(2)));
typedef u32 u32x4 __attribute__((ext_vector_type(4)));
#define LAS __attribute__((address_space(3)))
#define DI __device__ __forceinline__
#define RTSTAMP(k)
#define MARK(i)

constexpr int SEQ = 2048, DM = 576, NS = 128, FF = 4608;

constexpr u32 WS_MOD   = 0;
constexpr u32 WS_STAT  = 16384;
constexpr u32 WS_KSC   = 32768;
constexpr u32 WS_FLAG  = 36864;
constexpr u32 WS_EX    = 65536;
constexpr u32 WS_XT    = 1u << 20;
constexpr u32 WS_KR    = WS_XT + 2359296;
constexpr u32 WS_X1F   = WS_KR + 2359296;
constexpr u32 WS_WF    = WS_X1F + 2359296;
constexpr u32 WS_HF    = WS_WF + 10616832;
constexpr u32 WS_PART  = WS_HF + 18874368;
constexpr u32 WS_PH    = WS_PART + 6291456;
constexpr u32 WS_END   = WS_PH + 1048576;

DI float bflo(u32 u) { return __builtin_bit_cast(float, u << 16); }
DI float bfhi(u32 u) { return __builtin_bit_cast(float, u & 0xffff0000u); }
DI float bf2f(u16 v) { return __builtin_bit_cast(float, ((u32)v) << 16); }
DI u32 pkbf(float a, float b) { f32x2 v = {a, b}; return __builtin_bit_cast(u32, __builtin_convertvector(v, bf16x2)); }
DI u32 pkhf(float a, float b) { f32x2 v = {a, b}; return __builtin_bit_cast(u32, __builtin_convertvector(v, f16x2)); }
DI float rbf(float a) { return bflo(pkbf(a, a)); }
template <int CTRL, int RM = 0xf> DI float dppf(float v) {
    return __builtin_bit_cast(float, __builtin_amdgcn_update_dpp(0, __builtin_bit_cast(int, v), CTRL, RM, 0xf, true));
}
DI u32x4 trread2(const char* smem, u32 off0, u32 off1) {
    const u32x2 l = __builtin_bit_cast(u32x2, __builtin_amdgcn_ds_read_tr16_b64_v4i16((LAS s16x4*)(smem + off0)));
    const u32x2 h = __builtin_bit_cast(u32x2, __builtin_amdgcn_ds_read_tr16_b64_v4i16((LAS s16x4*)(smem + off1)));
    return (u32x4){l[0], l[1], h[0], h[1]};
}
DI void st16(char* ws, u32 off, u32x4 v) {
    __builtin_amdgcn_raw_buffer_store_b128(v, __builtin_amdgcn_make_buffer_rsrc(ws, 0, WS_END, 0x00020000), off, 0, 16);
}
DI void st4(char* ws, u32 off, u32 v) {
    __builtin_amdgcn_raw_buffer_store_b32(v, __builtin_amdgcn_make_buffer_rsrc(ws, 0, WS_END, 0x00020000), off, 0, 16);
}
DI float pow2i(int e) { e = e < -126 ? -126 : (e > 127 ? 127 : e); return __builtin_bit_cast(float, (u32)(e + 127) << 23); }
DI int expo(float x) { return (int)((__builtin_bit_cast(u32, x) >> 23) & 255u) - 127; }
DI u32 imgrow(u32 row) { return row * 1184u + (row >> 3) * 128u; }
DI f32x2 cmul(f32x2 z, f32x2 w, f32x2 wp) {
    const f32x2 zx = {z[0], z[0]}, zy = {z[1], z[1]};
    return __builtin_elementwise_fma(zy, wp, zx * w);
}
#define CODE_PREFETCH(sym, part) do { u32 lo_, hi_, len_; \
    asm volatile("s_getpc_b64 s[20:21]\n\ts_add_u32 s20, s20, " #sym "@rel32@lo+4\n\ts_addc_u32 s21, s21, " #sym "@rel32@hi+12\n\t" \
                 "s_mov_b32 %0, s20\n\ts_mov_b32 %1, s21\n\ts_mov_b32 %2, " #sym "_end-" #sym : "=s"(lo_), "=s"(hi_), "=s"(len_) :: "scc", "s20", "s21"); \
    const char* p_ = (const char*)(((unsigned long long)hi_ << 32) | lo_); \
    const u32 o_ = (threadIdx.x + (part) * 256u) * 128u; \
    if (o_ < len_) { u32 t_; asm volatile("global_load_dword %0, %1, off\n\ts_waitcnt vmcnt(0)" : "=v"(t_) : "v"(p_ + o_) : "memory"); } \
    } while (0)

#define DELAY1 20
#define DELAY2 0
#define NAV 26
constexpr int NA = NAV, NB = 36 - NA;
constexpr int NB_PF = 16, NB_LN = 64, NB_GEMV = 108, NB_KGEN = 576, NB_W2 = 144;
constexpr int NB_PREP = NB_PF + NB_LN + NB_GEMV + NB_KGEN + NB_W2;
constexpr int PREP_LDS = 38400;

extern "C" __global__ __launch_bounds__(256, 4) void k_prep(const u16* __restrict__ x, const u16* __restrict__ logA,
                                              const u16* __restrict__ Aim, const u16* __restrict__ Cre,
                                              const u16* __restrict__ Cim, const u16* __restrict__ logdt,
                                              const u16* __restrict__ w2, const u16* __restrict__ cvec,
                                              const u16* __restrict__ wmod, char* __restrict__ ws) {
    extern __shared__ __attribute__((aligned(16))) char smem[];
    RTSTAMP(0);
    const int tid = threadIdx.x, lane = tid & 63;
    const int wv = __builtin_amdgcn_readfirstlane(tid >> 6);
    int b = blockIdx.x;
    if (b >= NB_PF && b < NB_PF + NB_KGEN + NB_LN + NB_GEMV) b = b < NB_PF + NB_KGEN ? b + NB_LN + NB_GEMV : b - NB_KGEN;
    if (b < NB_PF) {
        if (b < 8) CODE_PREFETCH(k_prep, 0); else CODE_PREFETCH(k_conv, 0);
        if (b < 2) st16(ws, WS_FLAG + b * 4096 + tid * 16, (u32x4){0u, 0u, 0u, 0u});
    } else if ((b -= NB_PF) < NB_LN) {
        const int t0 = b * 32, tok = tid >> 3, sub = tid & 7;
        u32x4 v[9];
#pragma unroll
        for (int i = 0; i < 9; ++i) v[i] = *(const u32x4*)(x + (size_t)(t0 + tok) * DM + (sub + 8 * i) * 8);
        MARK(0);
#pragma unroll
        for (int i = 0; i < 9; ++i) *(u32x4*)(smem + imgrow(tok) + (sub + 8 * i) * 16) = v[i];
        __syncthreads();
        const int g = lane >> 4, qq = (lane & 15) >> 2, p = lane & 3;
#pragma unroll
        for (int i = 0; i < 9; ++i) {
            const int cb = wv * 9 + i;
            st16(ws, WS_XT + b * 36864 + cb * 1024 + (lane & 15) * 64 + g * 16,
                 trread2(smem, imgrow(8 * g + qq) + cb * 32 + p * 8, imgrow(8 * g + 4 + qq) + cb * 32 + p * 8));
        }
        MARK(1);
        float s = 0.f;
#pragma unroll
        for (int i = 0; i < 9; ++i)
#pragma unroll
            for (int k = 0; k < 4; ++k) s += bflo(v[i][k]) + bfhi(v[i][k]);
        s += dppf<0xB1>(s); s += dppf<0x4E>(s); s += dppf<0x141>(s);
        const float mean = s * (1.f / DM);
        float q = 0.f;
#pragma unroll
        for (int i = 0; i < 9; ++i)
#pragma unroll
            for (int k = 0; k < 4; ++k) {
                const float a = bflo(v[i][k]) - mean, c = bfhi(v[i][k]) - mean;
                q = fmaf(a, a, q); q = fmaf(c, c, q);
            }
        q += dppf<0xB1>(q); q += dppf<0x4E>(q); q += dppf<0x141>(q);
        if (sub == 0) st4(ws, WS_STAT + (t0 + tok) * 4, pkbf(mean, __builtin_amdgcn_sqrtf(rbf(rbf(q * (1.f / DM)) + 1e-5f))));
    } else if ((b -= NB_LN) < NB_GEMV) {
        const int seg = tid & 3, rg = tid >> 2;
        u32x4 v[9]; u16 cv[9];
#pragma unroll
        for (int i = 0; i < 9; ++i) {
            const int row = rg + 64 * i;
            cv[i] = cvec[row];
            v[i] = *(const u32x4*)(wmod + (size_t)row * (6 * DM) + b * 32 + seg * 8);
        }
        MARK(0);
        float acc[8];
#pragma unroll
        for (int k = 0; k < 8; ++k) acc[k] = 0.f;
#pragma unroll
        for (int i = 0; i < 9; ++i) {
            const float c = bf2f(cv[i]);
            const float sl = rbf(c * __builtin_amdgcn_rcpf(1.f + __builtin_amdgcn_exp2f(c * -1.44269504088896340736f)));
#pragma unroll
            for (int k = 0; k < 4; ++k) {
                acc[2 * k] = fmaf(sl, bflo(v[i][k]), acc[2 * k]);
                acc[2 * k + 1] = fmaf(sl, bfhi(v[i][k]), acc[2 * k + 1]);
            }
        }
#pragma unroll
        for (int k = 0; k < 8; ++k) { acc[k] += dppf<0x114>(acc[k]); acc[k] += dppf<0x118>(acc[k]); }
        float* red = (float*)smem;
        if ((lane & 15) >= 12) {
            *(f32x4*)(red + (tid >> 4) * 32 + seg * 8) = (f32x4){acc[0], acc[1], acc[2], acc[3]};
            *(f32x4*)(red + (tid >> 4) * 32 + seg * 8 + 4) = (f32x4){acc[4], acc[5], acc[6], acc[7]};
        }
        __syncthreads();
        MARK(1);
        if (tid < 32) {
            float t = 0.f;
#pragma unroll
            for (int i = 0; i < 16; ++i) t += red[i * 32 + tid];
            st4(ws, WS_MOD + (b * 32 + tid) * 4, __builtin_bit_cast(u32, rbf(t)));
        }
    } else if ((b -= NB_GEMV) < NB_KGEN) {
        const int d = b, hn = wv & 1, grp = wv >> 1;
        const int n = hn * 64 + lane, n2 = n ^ 64;
        const u16 la_ = logA[d * NS + n], ai_ = Aim[d * NS + n], cr_ = Cre[d * NS + n], ci_ = Cim[d * NS + n];
        const u16 cr2_ = Cre[d * NS + n2], ci2_ = Cim[d * NS + n2], ld_ = logdt[d];
        u32 ta[8];
#pragma unroll
        for (int j = 0; j < 8; j += 2) {
            const float l0 = (float)(2047 - tid * 8 - j);
            const u32 pk = pkbf(l0, l0 - 1.f);
            const float p0 = bflo(pk), p1 = bfhi(pk);
            ta[j] = (u32)fminf(4.f * p0, p0 + 1536.f); ta[j + 1] = (u32)fminf(4.f * p1, p1 + 1536.f);
        }
        asm volatile("" : "+v"(ta[0]), "+v"(ta[1]), "+v"(ta[2]), "+v"(ta[3]), "+v"(ta[4]), "+v"(ta[5]), "+v"(ta[6]), "+v"(ta[7]));
        MARK(0);
        const float cr = bf2f(cr_), ci = bf2f(ci_);
        const float dt = rbf(__builtin_amdgcn_exp2f(bf2f(ld_) * 1.44269504088896340736f));
        const float zr = -rbf(__builtin_amdgcn_exp2f(bf2f(la_) * 1.44269504088896340736f)) * dt, zi = bf2f(ai_) * dt;
        float bs = fabsf(cr) + fabsf(ci) + fabsf(bf2f(cr2_)) + fabsf(bf2f(ci2_));
        bs += dppf<0xB1>(bs); bs += dppf<0x4E>(bs); bs += dppf<0x141>(bs); bs += dppf<0x140>(bs);
        bs += dppf<0x142, 0xa>(bs); bs += dppf<0x143, 0xc>(bs);
        bs = __builtin_bit_cast(float, __builtin_amdgcn_readlane(__builtin_bit_cast(int, bs), 63));
        const int se = 14 - expo(2.f * dt * bs * 1.0001f);
        const float scale = pow2i(se > 100 ? 100 : se);
        const float zr2 = zr * 1.44269504088896340736f;
        auto cexp = [&](float p) -> f32x2 {
            const float mag = __builtin_amdgcn_exp2f(zr2 * p);
            const float ang = zi * p;
            const float rr = __builtin_rintf(ang * 0.15915494309189535f);
            float fr = fmaf(ang, 0.15915494309189535f, -rr);
            fr = fmaf(ang, 6.4206382e-9f, fr);
            return (f32x2){mag * __builtin_amdgcn_cosf(fr), mag * __builtin_amdgcn_sinf(fr)};
        };
        const float p1 = grp ? 4.f : 1.f;
        const f32x2 f1 = cexp(p1), c1 = cexp(16.f * p1), s0 = cexp(grp ? 512.f : 0.f), s1 = cexp(256.f * p1);
        const f32x2 fw = {f1[0], -f1[1]}, fwp = {f1[1], f1[0]};
        const f32x2 cwp = {-c1[1], c1[0]};
        const float es = 2.f * dt * scale;
        f32x2 za = cmul((f32x2){es * cr, es * ci}, s0, (f32x2){-s0[1], s0[0]});
        f32x2 zb = cmul(za, s1, (f32x2){-s1[1], s1[0]});
        f32x2 zf = {1.f, 0.f};
        char* tab = smem + wv * 8704;
        u32 keep[16];
#pragma unroll
        for (int k = 0; k < 16; ++k) {
            *(u32*)(tab + k * 272 + lane * 4) = pkhf(zf[0], zf[1]);
            *(u32*)(tab + (16 + k) * 272 + lane * 4) = pkhf(za[0], za[1]);
            keep[k] = pkhf(zb[0], zb[1]);
            zf = cmul(zf, fw, fwp); za = cmul(za, c1, cwp); zb = cmul(zb, c1, cwp);
        }
        const int r16 = lane & 15, g = lane >> 4;
        f16x8 fb[4];
        f32x4 acc0 = {0.f, 0.f, 0.f, 0.f}, acc1 = {0.f, 0.f, 0.f, 0.f};
        __builtin_amdgcn_fence(__ATOMIC_RELEASE, "wavefront");
        __builtin_amdgcn_wave_barrier();
#pragma unroll
        for (int ks = 0; ks < 4; ++ks) {
            fb[ks] = *(const f16x8*)(tab + r16 * 272 + ks * 64 + g * 16);
            const f16x8 fa = *(const f16x8*)(tab + (16 + r16) * 272 + ks * 64 + g * 16);
            acc0 = __builtin_amdgcn_mfma_f32_16x16x32_f16(fb[ks], fa, acc0, 0, 0, 0);
        }
        __builtin_amdgcn_fence(__ATOMIC_RELEASE, "wavefront");
        __builtin_amdgcn_wave_barrier();
#pragma unroll
        for (int k = 0; k < 16; ++k) *(u32*)(tab + (16 + k) * 272 + lane * 4) = keep[k];
        __builtin_amdgcn_fence(__ATOMIC_RELEASE, "wavefront");
        __builtin_amdgcn_wave_barrier();
#pragma unroll
        for (int ks = 0; ks < 4; ++ks) {
            const f16x8 fa = *(const f16x8*)(tab + (16 + r16) * 272 + ks * 64 + g * 16);
            acc1 = __builtin_amdgcn_mfma_f32_16x16x32_f16(fb[ks], fa, acc1, 0, 0, 0);
        }
        MARK(1);
        __builtin_amdgcn_fence(__ATOMIC_RELEASE, "wavefront");
        __builtin_amdgcn_wave_barrier();
        *(f32x4*)(tab + r16 * 64 + g * 16) = acc0;
        *(f32x4*)(tab + 1024 + r16 * 64 + g * 16) = acc1;
        __syncthreads();
        float kv[8];
#pragma unroll
        for (int j = 0; j < 8; ++j) {
            const u32 o = ta[j] + (ta[j] >= 2048u ? 2u * 8704u - 2048u : 0u);
            kv[j] = *(const float*)(smem + o) + *(const float*)(smem + o + 8704);
        }
        MARK(2);
        st16(ws, WS_KR + d * 4096 + tid * 16, (u32x4){pkhf(kv[0], kv[1]), pkhf(kv[2], kv[3]), pkhf(kv[4], kv[5]), pkhf(kv[6], kv[7])});
        if (tid == 0) st4(ws, WS_KSC + d * 4, __builtin_bit_cast(u32, pow2i(se > 100 ? -100 : -se)));
    } else {
        b -= NB_KGEN;
        const u16* src = w2 + (size_t)b * 32 * DM;
        if (DELAY1 > 0) __builtin_amdgcn_s_sleep(DELAY1);
        const int tok = tid >> 3, sub = tid & 7;
        u32x4 v[9];
#pragma unroll
        for (int i = 0; i < 9; ++i) v[i] = *(const u32x4*)(src + tok * DM + (sub + 8 * i) * 8);
        MARK(0);
#pragma unroll
        for (int i = 0; i < 9; ++i) *(u32x4*)(smem + imgrow(tok) + (sub + 8 * i) * 16) = v[i];
        __syncthreads();
        const int kq = b / 36, kk = b - kq * 36, sp = kk / 9, c = kk - sp * 9;
        const int rel = (sp - wv) & 3;
        const int e = rel == 0 ? c : 9 + c * 3 + (rel - 1);
        const int r0 = 4 * (lane >> 4) + ((lane & 15) >> 2), p = lane & 3;
#pragma unroll
        for (int i = 0; i < 9; ++i) {
            const int sg = (i + 1) >> 1, j = i == 0 ? 0 : (i - 1) & 1;
            const int T = 9 * wv + (sg == 0 ? 8 : 2 * ((kq + sg) & 3) + j);
            const int m = sg == 4 ? 252 + 2 * e + j
                        : e < NA ? 7 * e + i
                        : sg == 0 ? 7 * NA + 2 * NB + (e - NA) : 7 * NA + (sg == 1 ? 0 : (2 * sg - 1) * NB) + (e - NA) * 2 + j;
            const u32 col = T * 32 + p * 8;
            st16(ws, WS_WF + ((kq * 4 + wv) * 648 + 324 + m) * 1024 + lane * 16, trread2(smem, imgrow(r0) + col, imgrow(r0 + 16) + col));
        }
    }
    asm volatile("k_prep_end:");
}

constexpr int NB_CONV = 576, NB_W1 = 144;
constexpr int CONV_LDS = 36864 + 4096;
constexpr int UCP = 4640, KB_OFF = 4 * UCP;

extern "C" __global__ __launch_bounds__(256, 4) void k_conv(const u16* __restrict__ dskip, const u16* __restrict__ w1,
                                              char* __restrict__ ws) {
    extern __shared__ __attribute__((aligned(16))) char smem[];
    const int tid = threadIdx.x, lane = tid & 63;
    const int wv = __builtin_amdgcn_readfirstlane(tid >> 6);
    RTSTAMP(1);
    const float* mod = (const float*)(ws + WS_MOD);
    int b = blockIdx.x;
    if (b >= NB_PF) b = b < NB_PF + NB_CONV ? b + NB_W1 : b - NB_CONV;
    if (b < NB_PF) {
        CODE_PREFETCH(k_mlp, b >> 3);
    } else if ((b -= NB_PF) < NB_W1) {
        const int q = b / 9, c = b - q * 9, f0 = b * 32;
        if (DELAY2 > 0) __builtin_amdgcn_s_sleep(DELAY2);
        const int seg = tid & 3, rg = tid >> 2;
        u32x4 v[9]; float sb[9], cb[9];
#pragma unroll
        for (int i = 0; i < 9; ++i) {
            const int row = rg + 64 * i;
            v[i] = *(const u32x4*)(w1 + (size_t)row * FF + f0 + seg * 8);
            sb[i] = mod[3 * DM + row]; cb[i] = mod[4 * DM + row];
        }
        MARK(0);
        float S[8], B[8];
#pragma unroll
        for (int k = 0; k < 8; ++k) { S[k] = 0.f; B[k] = 0.f; }
#pragma unroll
        for (int i = 0; i < 9; ++i) {
            const float c1 = rbf(1.f + cb[i]);
            u32x4 o;
#pragma unroll
            for (int k = 0; k < 4; ++k) {
                const float lo = bflo(v[i][k]), hi = bfhi(v[i][k]);
                o[k] = pkbf(lo * c1, hi * c1);
                S[2 * k] += bflo(o[k]); S[2 * k + 1] += bfhi(o[k]);
                B[2 * k] = fmaf(sb[i], lo, B[2 * k]); B[2 * k + 1] = fmaf(sb[i], hi, B[2 * k + 1]);
            }
            *(u32x4*)(smem + ((tid ^ ((tid >> 3) & 2)) + 256 * i) * 16) = o;
        }
#pragma unroll
        for (int k = 0; k < 8; ++k) {
            S[k] += dppf<0x114>(S[k]); S[k] += dppf<0x118>(S[k]);
            B[k] += dppf<0x114>(B[k]); B[k] += dppf<0x118>(B[k]);
        }
        float* red = (float*)(smem + 36864);
        if ((lane & 15) >= 12) {
            float* r = red + (tid >> 4) * 64 + seg * 8;
            *(f32x4*)(r) = (f32x4){S[0], S[1], S[2], S[3]}; *(f32x4*)(r + 4) = (f32x4){S[4], S[5], S[6], S[7]};
            *(f32x4*)(r + 32) = (f32x4){B[0], B[1], B[2], B[3]}; *(f32x4*)(r + 36) = (f32x4){B[4], B[5], B[6], B[7]};
        }
        __syncthreads();
        MARK(1);
        const int g = lane >> 4, qq = (lane & 15) >> 2, p = lane & 3;
#pragma unroll
        for (int i = 0; i < 9; ++i) {
            const int s = wv * 9 + i;
            const u32 a = (32 * (s >> 1) + 4 * g + qq) * 64 + (((s & 1) ^ (g & 1)) * 32) + p * 8;
            st16(ws, WS_WF + (q * 648 + c * 36 + s) * 1024 + lane * 16, trread2(smem, a, a + 1024));
        }
        if (wv == 0) {
            float t = 0.f;
#pragma unroll
            for (int i = 0; i < 16; ++i) t += red[i * 64 + lane];
            const float B_ = __builtin_bit_cast(float, __builtin_amdgcn_ds_bpermute((lane ^ 32) * 4, __builtin_bit_cast(int, t)));
            const float S_ = -t;
            const float s1 = rbf(S_), s2 = rbf(S_ - s1), s3 = S_ - s1 - s2;
            const float b1 = rbf(B_), b2 = rbf(B_ - b1), b3 = B_ - b1 - b2;
            if (lane < 32) st16(ws, WS_EX + q * 8192 + (c * 32 + lane) * 16, (u32x4){pkbf(s1, s2), pkbf(s3, b1), pkbf(b2, b3), 0u});
        } else if (wv == 1 && c == 0 && lane < 48) {
            const int d = (q & 3) * 144 + (lane < 32 ? (q >> 2) * 32 + lane : 96 + lane);
            st4(ws, WS_EX + q * 8192 + 4608 + lane * 4, __builtin_bit_cast(u32, mod[5 * DM + d]));
        }
    } else {
        const int d = b - NB_W1;
        const int tp = tid > 0 ? tid * 8 - 4 : 0;
        const char* xb = ws + WS_XT + d * 64;
        const u32x4 x0 = *(const u32x4*)(xb + (tid >> 2) * 36864 + (tid & 3) * 16);
        const u32x2 xm = *(const u32x2*)(xb + (tp >> 5) * 36864 + (tp & 31) * 2);
        u32x4 st[3];
        st[0] = *(const u32x4*)(ws + WS_STAT + tp * 4);
        st[1] = *(const u32x4*)(ws + WS_STAT + tid * 32);
        st[2] = *(const u32x4*)(ws + WS_STAT + tid * 32 + 16);
        const u32x4 kv = *(const u32x4*)(ws + WS_KR + d * 4096 + tid * 16);
        const float sa = mod[d], ca = mod[DM + d], ga = mod[2 * DM + d];
        const float dsk = bf2f(dskip[d]);
        const float kinv = ((const float*)(ws + WS_KSC))[d];
        {
            const u32x4 z = {0u, 0u, 0u, 0u};
            if (tid < 96) *(u32x4*)(smem + (tid / 24) * UCP + (tid % 24) * 16) = z;
            else if (tid < 104) *(u32x4*)(smem + KB_OFF + 4096 + (tid - 96) * 16) = z;
        }
        MARK(0);
        const float c1 = rbf(1.f + ca);
        const int he = expo(64.f * fabsf(c1) + 2.f * fabsf(sa)) - 14;
        const float hs = pow2i(-he), oscale = pow2i(he) * kinv;
        const u32 xw[6] = {xm[0], xm[1], x0[0], x0[1], x0[2], x0[3]};
        u32 P[6], hb[4];
#pragma unroll
        for (int k = 0; k < 6; ++k) {
            const u32 s0 = st[k >> 1][(k & 1) * 2], s1 = st[k >> 1][(k & 1) * 2 + 1];
            const float n0 = (bflo(xw[k]) - bflo(s0)) * __builtin_amdgcn_rcpf(bfhi(s0));
            const float n1 = (bfhi(xw[k]) - bflo(s1)) * __builtin_amdgcn_rcpf(bfhi(s1));
            const u32 t = pkbf(fmaf(n0, c1, sa), fmaf(n1, c1, sa));
            if (k >= 2) hb[k - 2] = t;
            P[k] = pkhf(bflo(t) * hs, bfhi(t) * hs);
        }
        if (tid == 0) { P[0] = 0u; P[1] = 0u; }
        u32 Q[5];
#pragma unroll
        for (int k = 0; k < 5; ++k) Q[k] = __builtin_amdgcn_alignbit(P[k + 1], P[k], 16);
        {
            char* ub = smem + 384 + tid * 16;
            *(u32x4*)(ub) = (u32x4){Q[0], Q[1], Q[2], Q[3]};
            *(u32x4*)(ub + UCP) = (u32x4){P[1], P[2], P[3], P[4]};
            *(u32x4*)(ub + 2 * UCP) = (u32x4){Q[1], Q[2], Q[3], Q[4]};
            *(u32x4*)(ub + 3 * UCP) = (u32x4){P[2], P[3], P[4], P[5]};
            *(u32x4*)(smem + KB_OFF + tid * 16) = kv;
        }
        __syncthreads();
        MARK(1);
        const int c = lane & 15, g = lane >> 4;
        f32x4 acc[8];
#pragma unroll
        for (int j = 0; j < 8; ++j) acc[j] = (f32x4){0.f, 0.f, 0.f, 0.f};
        {
            const u32 lb = (u32)(uintptr_t)(LAS char*)smem;
            const u32 ka = lb + KB_OFF + 2 * (2076 - 4 * c + 8 * g - 64 * wv) - 3584 - 64;
            const u32 ua = lb + (c & 3) * UCP + 2 * (8 * g + 64 * (c >> 2) - 64 * wv + 192);
            u32x2 kl[2][8], kh[2][8]; u32x4 uf[2][8];
            auto rd = [&](int rr, int n) {
                asm volatile("ds_read_b64 %0, %3 offset:%5\n\tds_read_b64 %1, %3 offset:%6\n\tds_read_b128 %2, %4 offset:%7"
                             : "=&v"(kl[rr][n]), "=&v"(kh[rr][n]), "=&v"(uf[rr][n]) : "v"(ka), "v"(ua),
                               "n"((7 - n) * 512 + (1 - rr) * 64), "n"((7 - n) * 512 + (1 - rr) * 64 + 8), "n"(n * 512 + (1 - rr) * 64) : "memory");
            };
            auto mm = [&](int rr, int i, int dl) {
                const u32x4 k = {kl[rr][i][0], kl[rr][i][1], kh[rr][i][0], kh[rr][i][1]};
                acc[i + dl] = __builtin_amdgcn_mfma_f32_16x16x32_f16(__builtin_bit_cast(f16x8, uf[rr][dl]), __builtin_bit_cast(f16x8, k), acc[i + dl], 0, 0, 0);
            };
            auto shell = [&](int rr, int n) {
#pragma unroll
                for (int o = 0; o <= n && o + n < 8; ++o) { mm(rr, n, o); if (o < n) mm(rr, o, n); }
            };
            auto pin = [&](int rr, int n) { asm volatile("" : "+v"(kl[rr][n]), "+v"(kh[rr][n]), "+v"(uf[rr][n])); };
            rd(0, 0); rd(0, 7); rd(0, 1); rd(0, 6); rd(0, 2); rd(0, 5); rd(0, 3); rd(0, 4);
#pragma unroll
            for (int n = 7; n >= 0; --n) {
                if (n >= 4) { asm volatile("s_waitcnt lgkmcnt(%0)" :: "n"(n == 7 ? 15 : 6 * (n - 4) + 3 * (7 - n)) : "memory"); pin(0, n); pin(0, 7 - n); }
                shell(0, n);
                __builtin_amdgcn_sched_barrier(0);
                rd(1, 7 - n);
            }
#pragma unroll
            for (int n = 0; n < 8; ++n) {
                asm volatile("s_waitcnt lgkmcnt(%0)" :: "n"(n < 3 ? 15 : 3 * (7 - n)) : "memory");
                pin(1, n);
                shell(1, n);
                __builtin_amdgcn_sched_barrier(0);
            }
        }
        __syncthreads();
        MARK(2);
#pragma unroll
        for (int j = 0; j < 8; ++j) *(f32x4*)(smem + wv * 8192 + j * 1024 + lane * 16) = acc[j];
        __syncthreads();
        f32x4 y0 = *(const f32x4*)(smem + tid * 32), y1 = *(const f32x4*)(smem + tid * 32 + 16);
#pragma unroll
        for (int w = 1; w < 4; ++w) {
            y0 += *(const f32x4*)(smem + w * 8192 + tid * 32);
            y1 += *(const f32x4*)(smem + w * 8192 + tid * 32 + 16);
        }
        const float y[8] = {y0[0], y0[1], y0[2], y0[3], y1[0], y1[1], y1[2], y1[3]};
        u32x4 o;
#pragma unroll
        for (int k = 0; k < 4; ++k) {
            const u32 yb = pkbf(y[2 * k] * oscale, y[2 * k + 1] * oscale);
            const u32 sk = pkbf(bflo(hb[k]) * dsk, bfhi(hb[k]) * dsk);
            const u32 z = pkbf(bflo(yb) + bflo(sk), bfhi(yb) + bfhi(sk));
            const u32 gz = pkbf(bflo(z) * ga, bfhi(z) * ga);
            o[k] = pkbf(bflo(x0[k]) + bflo(gz), bfhi(x0[k]) + bfhi(gz));
        }
        MARK(3);
        st16(ws, WS_X1F + (tid >> 4) * 147456 + ((d / 48) * 12 + ((tid >> 2) & 3) * 3 + (d >> 4) % 3) * 1024 + (d & 15) * 64 + (tid & 3) * 16, o);
    }
    asm volatile("k_conv_end:");
}
constexpr u32 L_EXF = 147456;
constexpr int MLP_LDS = L_EXF + 12288;
constexpr int GE = 648;

#if defined(__HIP_DEVICE_COMPILE__)
#pragma clang attribute push(__attribute__((target("no-packed-fp32-ops"))), apply_to = function)
#endif
template <int N> struct IC { static constexpr int value = N; };
template <int A, int B, class F> DI void static_for(F&& f) {
    if constexpr (B - A == 1) f(IC<A>{});
    else if constexpr (A < B) { static_for<A, (A + B) / 2>(f); static_for<(A + B) / 2, B>(f); }
}
constexpr int imin(int a, int b) { return a < b ? a : b; }
constexpr int XW = 6;
#define XSP 7
#define XIV 8
constexpr int XI = XIV;
constexpr int x_dmas(int j) { return j <= 0 ? 0 : imin((4 * j + XSP - 1) / XSP, 3 * (19 - XI)); }
constexpr int G1 = 36;
#define PLG 614
constexpr int P1 = 36 * 5 + 7, P2 = 36 * 7 + 7, P3 = 400;
#define PREF 0
#define CD1 34
#define CD3 30
#define QW3 8
constexpr int C1 = P1 + CD1, C2 = P2 + CD1, C3 = P3 + CD3;
constexpr bool s_poll(int G) { return G == P1 || G == P2 || G == P3; }
#define PB2 606
#define PL4 640
constexpr int PUB2 = PB2, P4 = PL4;
constexpr int HS0 = 22, HS1 = 35;
constexpr int s_hstore(int G) { return G < 72 ? (G == 64 ? 0 : G == 71 ? 1 : -1) : G < 324 ? (G % 36 == HS0 ? 0 : G % 36 == HS1 ? 1 : -1) : G == 338 ? 0 : G == 346 ? 1 : -1; }
constexpr int B0 = 324 + 7 * NA;
constexpr int slot_end(int sg) { return B0 - 1 + (sg == 1 ? 2 : sg == 0 ? 3 : 2 * sg + 1) * NB; }
constexpr int s_pstore(int G) {
    for (int sg = 0; sg < 4; ++sg) { if (G == slot_end(sg) + 4) return 2 * sg; if (G == slot_end(sg) + 6 && sg > 0) return 2 * sg + 1; }
    return -1;
}
constexpr bool s_res(int G) { return G == 620 || G == 622 || G == 624; }
constexpr int s_pub(int G) { return G == 36 * 3 + 24 ? 2 : G == 36 * 5 + 24 ? 4 : G == 372 ? 9 : 0; }
constexpr int m_slot(int m) { return m < 7 * NA ? (m % 7 + 1) / 2 : m < 7 * NA + 2 * NB ? 1 : m < 7 * NA + 3 * NB ? 0 : m < 252 ? 2 + (m - 7 * NA - 3 * NB) / (2 * NB) : 4; }
constexpr int m_ent(int m) { return m < 7 * NA ? m / 7 : m < 7 * NA + 2 * NB ? NA + (m - 7 * NA) / 2 : m < 7 * NA + 3 * NB ? NA + (m - 7 * NA - 2 * NB)
                                  : m < 252 ? NA + ((m - 7 * NA - 3 * NB) % (2 * NB)) / 2 : (m - 252) / 2; }
constexpr int m_half(int m) { return m < 7 * NA ? (m % 7 == 0 ? 0 : (m % 7 - 1) & 1) : m < 7 * NA + 2 * NB ? (m - 7 * NA) & 1 : m < 7 * NA + 3 * NB ? 0
                                   : m < 252 ? (m - 7 * NA - 3 * NB) & 1 : m & 1; }
constexpr bool m_first(int m) { return m < 7 || m == 252 || m == 253; }
constexpr bool ent_first(int m) { return m < 7 * NA ? m % 7 == 0 : (m < 7 * NA + 2 * NB && ((m - 7 * NA) & 1) == 0); }
constexpr bool ent_own(int e) { return e < 9; }
constexpr int first_use(int e) { return e < NA ? 324 + 7 * e : B0 + 2 * (e - NA); }
constexpr bool s_ring(int G) { return G + 12 < GE && (G < 100 ? G % 12 == 0 : G < 490 ? G % 36 == 24 : G % 18 == 6); }
constexpr int ring_slot(int grp) { const int u = (grp / 3) & 3; return (u == 0 ? 0 : u == 1 ? 6 : u == 2 ? 9 : 3) + grp % 3; }
constexpr int need_bar(int grp) { int g = 12 * grp - 12; while (g > 0 && !s_ring(g)) --g; return g; }
struct Sched {
    int dn[GE];
    int dgap[162];
    int hl[GE];
    int pre[GE + 1];
    int hgap[36];
    bool ok;
    constexpr Sched() : dn(), dgap(), hl(), pre(), hgap(), ok(true) {
        int nd = 3 * XW, nh = 0, last2 = -99, lastbar = 24;
        for (int n = 0; n < 162; ++n) dgap[n] = -1;
        for (int e = 0; e < 36; ++e) hgap[e] = -1;
        for (int G = 0; G < GE; ++G) {
            dn[G] = -1; hl[G] = -1;
            int n = 0;
            if (G >= G1) {
                if (s_ring(G)) lastbar = G;
                const bool fixed = s_hstore(G) >= 0 || s_pstore(G) >= 0 || s_res(G);
                const bool quiet = (G >= P1 && G < P1 + 17) || (G >= P2 && G < P2 + 17) || (G >= P3 && G < P3 + QW3);
                const int sp2 = G < 324 ? 4 : 5;
                if (fixed) last2 = 2 * G;
                else if (!quiet && 2 * G - last2 >= sp2) {
                    int dd = 1 << 20, dh = 1 << 20;
                    if (nd < 162 && (nd / 3 < 12 || (nd / 3 - 11) * 12 <= lastbar + 6)) dd = need_bar(nd / 3) - 40;
                    const int lim = G > C3 ? 54 : G > C2 ? 24 : G > C1 ? 12 : 0;
                    if (nh < lim) dh = first_use(9 + nh / 2) - 45;
                    if (dd < (1 << 20) || dh < (1 << 20)) {
                        if (dd <= dh) { dn[G] = nd; dgap[nd] = G; if (need_bar(nd / 3) <= G) ok = false; ++nd; }
                        else { hl[G] = 18 + nh; hgap[9 + nh / 2] = G; if (first_use(9 + nh / 2) <= G) ok = false; ++nh; }
                        last2 = last2 + sp2 > 2 * G - 1 ? last2 + sp2 : 2 * G - (G < 324 ? 0 : 1);
                        n = 1;
                    }
                }
                n += fixed + s_poll(G) + (G == P4) + (s_pub(G) > 0) + (G == PUB2);
            }
            pre[G + 1] = pre[G] + n;
        }
        if (nd != 162 || nh != 54) ok = false;
    }
};
constexpr Sched SCH{};
static_assert(SCH.ok, "schedule misses a deadline");
constexpr int younger(int a, int b) { return imin(SCH.pre[b] - SCH.pre[a + 1], 63); }
constexpr bool s_bar(int G) { return G >= G1 && (s_ring(G) || s_pub(G) > 0 || G == PUB2); }
constexpr int bar_wait(int G) {
    int n = 63;
    if (s_ring(G)) { int grp = 53; while (need_bar(grp) > G) --grp; n = G == 36 ? 3 : younger(SCH.dgap[3 * grp + 2], G); }
    if (G == PUB2) n = imin(n, younger(slot_end(3) + 6, G));
    if (s_pub(G) > 0) n = imin(n, younger(G == 372 ? 346 : 36 * s_pub(G) + HS1, G));
    return n;
}
constexpr int frag_lds(int f) { return ring_slot(f / 12) * 12288 + (f % 12) * 1024; }

constexpr u32 RESL = ring_slot(45) * 12288;
static_assert(SCH.dgap[161] < 620, "M0");
#define OAUX 0
#define PGX 55
#define PG0 95
#define PG1 192
#define PG2 285
#define PG3 388
#define PG4 517
#define PG5 641
#define PADS 1
constexpr int pad_at(int G) { return G == PG0 ? 0 : G == PG1 ? 1 : G == PG2 ? 2 : G == PG3 ? 3 : G == PG4 ? 4 : G == PG5 ? 5 : -1; }
#define SB() __builtin_amdgcn_sched_barrier(0)
#define STAMP(i) do {} while (0)
#define TW(code) do { code; } while (0)
#define MFMA "v_mfma_f32_16x16x32_bf16 "

extern "C" __global__ __attribute__((aligned(4096))) __launch_bounds__(256, 1) void k_mlp(char* __restrict__ ws, u16* __restrict__ out) {
    extern __shared__ __attribute__((aligned(16))) char smem[];
    const int tid = threadIdx.x, lane = tid & 63;
    const int w = __builtin_amdgcn_readfirstlane(tid >> 6);
    const int b = blockIdx.x, xcd = b & 7, rr = b >> 3;
#define MAPV 1
    const int s = rr & 3;
    const int kq = MAPV == 0 ? xcd >> 1 : MAPV == 1 ? 2 * (xcd >> 2) + ((rr >> 2) & 1) : (rr >> 2) & 3;
    const int it = MAPV == 0 ? 8 * (xcd & 1) + (rr >> 2) : MAPV == 1 ? 4 * (xcd & 3) + (rr >> 3) : 2 * xcd + (rr >> 4);
    const int q = 4 * kq + s;
    const auto rs = __builtin_amdgcn_make_buffer_rsrc(ws, 0, WS_END, 0x00020000);
    const u32x4 srd = {(u32)(uintptr_t)ws, (u32)((uintptr_t)ws >> 32) & 0xffffu, WS_END, 0x00020000u};
    const u32 voff = lane * 16;
    const u32 lbase = (u32)(uintptr_t)(LAS char*)smem;
    const u32 wl = lbase + w * 3072;
    auto set_m0 = [&](u32 v) { asm volatile("s_mov_b32 m0, %0" :: "s"(v) : "memory"); };
    auto dma = [&](auto O, u32 so) {
        const u32 vo = voff; const u32x4 sr = srd;
        asm volatile("buffer_load_dwordx4 %0, %1, %2 offen offset:%3 lds" :: "v"(vo), "s"(sr), "s"(so), "n"(O.value * 1024) : "memory");
    };
    auto dma3 = [&](u32 dst, u32 so) {
        asm volatile("s_mov_b32 m0, %3\n\ts_nop 0\n\tbuffer_load_dwordx4 %0, %1, %2 offen lds\n\t"
                     "buffer_load_dwordx4 %0, %1, %2 offen offset:1024 lds\n\tbuffer_load_dwordx4 %0, %1, %2 offen offset:2048 lds"
                     :: "v"(voff), "s"(srd), "s"(so), "s"(dst) : "memory");
    };
    const u32 xsb = WS_X1F + it * 147456;
    const u32 wfb = WS_WF + q * 663552 + w * 3072;
    auto rq_dst = [&](int p) -> u32 {
        const int x = p <= 2 ? p - 1 : p <= 6 ? p - 2 : p - 3;
        return wl + (p == 0 ? 0 : p == 3 ? 12288 : p == 7 ? 24576 : p == 15 ? L_EXF : p > 15 ? ring_slot(p - 13) * 12288 : (3 + x % 9) * 12288);
    };
    auto rq_src = [&](int p) -> u32 {
        const int x = p <= 2 ? p - 1 : p <= 6 ? p - 2 : p - 3;
        return p == 0 ? wfb : p == 3 ? wfb + 12288 : p == 7 ? wfb + 24576 : p == 15 ? WS_EX + q * 8192 + w * 3072
             : p > 15 ? wfb + (p - 13) * 12288 : xsb + w * 3072 + x * 12288;
    };
#pragma unroll
    for (int p = 0; p < XI; ++p) {
        dma3(rq_dst(p), rq_src(p));
        if (PADS && p == 2) {
            if (w == 0) asm volatile("s_call_b64 s[30:31], k_mlp_pad0\n\ts_call_b64 s[30:31], k_mlp_pad4" ::: "s30", "s31");
            if (w == 1) asm volatile("s_call_b64 s[30:31], k_mlp_pad1\n\ts_call_b64 s[30:31], k_mlp_pad5" ::: "s30", "s31");
            if (w == 2) asm volatile("s_call_b64 s[30:31], k_mlp_pad6\n\ts_call_b64 s[30:31], k_mlp_pad2" ::: "s30", "s31");
            if (w == 3) asm volatile("s_call_b64 s[30:31], k_mlp_pad3" ::: "s30", "s31");
        }
    }
    set_m0(rq_dst(XI));
    const u32 hfb = WS_HF + (it * 4 + kq) * 36 * 8192 + w * 2048;
    u32x4 Wf[18], Ef[2];
    const u32 rd0 = lbase + voff;
    const u32 rb[3] = {rd0, rd0 + 49152, rd0 + 98304};
    auto rdw = [&](auto F) {
        constexpr int lo = frag_lds(F.value);
        u32x4& d = Wf[F.value % 18]; const u32 base = rb[lo / 49152];
        asm volatile("ds_read_b128 %0, %1 offset:%2" : "=v"(d) : "v"(base), "n"(lo % 49152) : "memory");
    };
    const u32 rex = lbase + L_EXF + (lane & 15) * 16;
    auto rde = [&](auto C) {
        u32x4& d0 = Ef[0]; u32x4& d1 = Ef[1]; const u32 base = rex;
        asm volatile("ds_read_b128 %0, %2 offset:%3\n\tds_read_b128 %1, %2 offset:%4" : "=&v"(d0), "=&v"(d1) : "v"(base), "n"(C.value * 512), "n"(C.value * 512 + 256) : "memory");
    };
    f32x4 acc[2][4];

    u32x4 Xa[36];
    f32x4 gram[2], rsum[2];
    {
        const u32 a0 = wl + (4 * (lane >> 4) + ((lane & 15) >> 2)) * 64 + (lane & 3) * 8;
        u32x4 ones = {0x3f803f80u, 0x3f803f80u, 0x3f803f80u, 0x3f803f80u};
        asm volatile("" : "+v"(ones));
        u32x2 t[3][4];
        const u32 xb[3] = {a0 + 36864, a0 + 73728, a0 + 110592};
        auto xrd = [&](auto KS, auto I) {
            constexpr int hk = 2 * KS.value + (I.value >> 1), sl = (hk / 3) % 9;
            u32x2& d = t[KS.value % 3][I.value]; const u32 base = xb[sl / 3];
            asm volatile("ds_read_b64_tr_b16 %0, %1 offset:%2" : "=v"(d) : "v"(base), "n"((sl % 3) * 12288 + (hk % 3) * 1024 + (I.value & 1) * 32) : "memory");
        };
        auto admit = [&](auto KS, auto J) {
            constexpr int ks = KS.value, x = (2 * ks + 1) / 3, pos = x <= 1 ? x + 1 : x <= 4 ? x + 2 : x + 3;
            constexpr int issued = 3 * XI + x_dmas(J.value);
            static_assert(issued >= 3 * pos + 3, "X order");
            if constexpr (ks % 6 == 0) asm volatile("s_waitcnt vmcnt(%0)\n\ts_barrier" :: "n"(issued - 3 * pos - 3) : "memory");
            else if constexpr (ks % 3 != 2) asm volatile("s_waitcnt vmcnt(%0)" :: "n"(issued - 3 * pos - 3) : "memory");
        };
        static_for<0, 2>([&](auto KS) {
            admit(KS, IC<0>{});
            static_for<0, 4>([&](auto I) { xrd(KS, I); });
            rdw(IC<2 * KS.value>{}); rdw(IC<2 * KS.value + 1>{});
        });
        STAMP(1);
        u32x4 xv[2];
        static_for<0, 72>([&](auto J) {
            constexpr int j = J.value, ks = j / 4, k = j % 4;
            if constexpr (PADS && j == PGX) asm volatile("s_branch 1f\n\t.p2align 12\nk_mlp_pad6:\n\ts_setpc_b64 s[30:31]\n1:");
            u32x2 (&d)[4] = t[ks % 3];
            u32x4& x0 = xv[0]; u32x4& x1 = xv[1]; u32x4& on = ones;
            if constexpr (k == 0) {
                asm volatile("s_waitcnt lgkmcnt(%4)" : "+v"(d[0]), "+v"(d[1]), "+v"(d[2]), "+v"(d[3]) : "n"(ks < 17 ? 6 : 0) : "memory");
                xv[0] = (u32x4){d[0][0], d[0][1], d[2][0], d[2][1]};
                xv[1] = (u32x4){d[1][0], d[1][1], d[3][0], d[3][1]};
                asm volatile("s_nop 1" : "+v"(x0), "+v"(x1));
            }
            f32x4& c0 = k < 2 ? acc[0][2 * (k & 1)] : k == 2 ? gram[0] : rsum[0];
            f32x4& c1 = k < 2 ? acc[0][2 * (k & 1) + 1] : k == 2 ? gram[1] : rsum[1];
            u32x4& am0 = k < 2 ? Wf[(2 * ks + k) % 18] : k == 2 ? x0 : on;
            u32x4& am1 = k < 2 ? Wf[(2 * ks + k) % 18] : k == 2 ? x1 : on;
            if constexpr (j < 4) asm volatile(MFMA "%0, %1, %2, 0" : "=&v"(c0) : "v"(am0), "v"(x0));
            else asm volatile(MFMA "%0, %1, %2, %0" : "+v"(c0) : "v"(am0), "v"(x0));
            SB();
            if constexpr (ks < 16) {
                if constexpr (k == 0) admit(IC<ks + 2>{}, J);
                if constexpr (k < 2) { xrd(IC<ks + 2>{}, IC<2 * k>{}); xrd(IC<ks + 2>{}, IC<2 * k + 1>{}); }
                else rdw(IC<2 * ks + 2 + k>{});
            }
            SB();
            if constexpr (j < 4) asm volatile(MFMA "%0, %1, %2, 0" : "=&v"(c1) : "v"(am1), "v"(x1));
            else asm volatile(MFMA "%0, %1, %2, %0" : "+v"(c1) : "v"(am1), "v"(x1));
            SB();
            if constexpr (k >= 2) { Xa[2 * ks + k - 2] = xv[k - 2]; asm volatile("" : "+a"(Xa[2 * ks + k - 2])); }
            if constexpr (x_dmas(j + 1) > x_dmas(j)) {
                constexpr int n = x_dmas(j), p = XI + n / 3;
                static_assert(p < 12 || p == 15 || (3 * (p < 15 ? p - 12 : p - 13) + 2) / 2 <= ks, "slot still holds unread x");
                dma(IC<n % 3>{}, rq_src(p));
                if constexpr (n % 3 == 2 && p < 18) set_m0(rq_dst(p + 1));
            }
            SB();
        });
    }
    float ysc[2];
    u32x4 Xe[2];
    {
        asm volatile("s_waitcnt vmcnt(6)\n\ts_barrier" ::: "memory");
        STAMP(2);
        static_for<36, 48>(rdw);
        rde(IC<0>{});
        set_m0(wl + ring_slot(XW) * 12288);
        SB();
    }
    float sq[2], mean[2];
    auto stats = [&](auto ST) {
        constexpr int st = ST.value >> 1, t = ST.value & 1;
        if constexpr (st == 0) {
            const float a = (lane & 1) ? gram[t][1] : gram[t][0], c = (lane & 1) ? gram[t][3] : gram[t][2];
            float v = (lane & 2) ? c : a;
            const u32 src = ((lane & 15) + 4 * (lane & 12)) * 4;
            float& d = sq[t];
            asm volatile("ds_bpermute_b32 %0, %1, %2" : "=v"(d) : "v"(src), "v"(v) : "memory");
            mean[t] = rsum[t][0] * (1.f / DM);
            asm volatile("" : "+v"(mean[t]));
        }
        if constexpr (st == 1) { asm volatile("" : "+v"(sq[t])); sq[t] = rbf(rbf(fmaxf(fmaf(-mean[t], mean[t], sq[t] * (1.f / DM)), 0.f)) + 1e-5f); asm volatile("" : "+v"(sq[t])); }
        if constexpr (st == 2) { sq[t] = rbf(__builtin_amdgcn_sqrtf(sq[t])); ysc[t] = -1.44269504088896340736f * __builtin_amdgcn_rcpf(sq[t]); asm volatile("" : "+v"(sq[t]), "+v"(ysc[t])); }
        if constexpr (st == 3) {
            const bool lo = lane < 16;
            Xe[t] = (u32x4){lo ? pkbf(mean[t], mean[t]) : 0u, lo ? pkbf(mean[t], sq[t]) : 0u, lo ? pkbf(sq[t], sq[t]) : 0u, 0u};
            asm volatile("" : "+v"(Xe[t]));
        }
    };
    auto corr = [&](auto C) {
        f32x4 (&a)[4] = acc[C.value & 1]; u32x4 (&e)[2] = Ef; u32x4 (&x)[2] = Xe;
        asm volatile(MFMA "%0, %4, %6, %0\n\t" MFMA "%1, %4, %7, %1\n\t" MFMA "%2, %5, %6, %2\n\t" MFMA "%3, %5, %7, %3"
                     : "+v"(a[0]), "+v"(a[1]), "+v"(a[2]), "+v"(a[3]) : "v"(e[0]), "v"(e[1]), "v"(x[0]), "v"(x[1]));
    };

    f32x4 acc2[5][4];
    float yv[16], ev[16];
    u32x4 H[36][2], pollv;
    auto flag_store = [&](u32 level, u32 so) {
        asm volatile("s_mov_b64 exec, 1\n\tbuffer_store_dword %0, off, %1, %2 sc1\n\ts_mov_b64 exec, -1" :: "v"(level), "s"(srd), "s"(so) : "memory");
    };
    auto act = [&](auto C, auto HS, auto PT) {
        constexpr int t = HS.value / 2, e = C.value, pt = PT.value;
        if constexpr (HS.value % 2 == 0) {
            if constexpr (pt != 1 && t < 16) { yv[t] = acc[e & 1][2 * ((t >> 2) & 1) + (t >> 3)][t & 3] * ysc[t >> 3]; asm volatile("" :: "v"(yv[t])); }
            if constexpr (pt != 0 && t >= 2) { yv[t - 2] = yv[t - 2] * ev[t - 2]; asm volatile("" :: "v"(yv[t - 2])); }
            if constexpr (pt != 0 && t < 16) { ev[t] = __builtin_amdgcn_exp2f(yv[t]); asm volatile("" :: "v"(ev[t])); }
        } else {
            if constexpr (pt != 1 && t >= 1 && t < 17) { ev[t - 1] = 1.f + ev[t - 1]; asm volatile("" :: "v"(ev[t - 1])); }
            if constexpr (pt != 0 && t >= 3 && (t & 1)) {
                u32 pk = pkbf(yv[t - 3], yv[t - 2]);
                asm volatile("" :: "v"(pk));
                H[e][(t - 3) / 8][((t - 3) / 2) & 3] = pk;
            }
            if constexpr (pt != 0 && t >= 1 && t < 17) { ev[t - 1] = __builtin_amdgcn_rcpf(ev[t - 1]); asm volatile("" :: "v"(ev[t - 1])); }
        }
    };
    const u32 hown = hfb + 9 * s * 8192;
    const u32 hpeer[3] = {hfb + 9 * ((s + 1) & 3) * 8192, hfb + 9 * ((s + 2) & 3) * 8192, hfb + 9 * ((s + 3) & 3) * 8192};
    auto hload = [&](auto E, auto HF) {
        constexpr int e = E.value, hf = HF.value, k = e - 9;
        const u32 so = hpeer[k % 3] + (k / 3) * 8192, vo = voff; const u32x4 sr = srd;
        u32x4& d = H[e][hf];
        asm volatile("buffer_load_dwordx4 %0, %1, %2, %3 offen offset:%4 sc1" : "=a"(d) : "v"(vo), "s"(sr), "s"(so), "n"(hf * 1024) : "memory");
    };
    const u32 vzero = 0;
    const u32 fso1 = WS_FLAG + (it * 4 + kq) * 64;
    auto poll = [&]() {
        asm volatile("buffer_load_dwordx4 %0, %1, %2, %3 offen sc1" : "=v"(pollv) : "v"(vzero), "s"(srd), "s"(fso1) : "memory");
    };
    auto check = [&](u32 level) {
        asm volatile("" : "+v"(pollv));
#pragma nounroll
        for (u32 spins = 0; spins < (1u << 20); ++spins) {
            const bool bad = pollv[0] < level || pollv[1] < level || pollv[2] < level || pollv[3] < level;
            if (__builtin_amdgcn_ballot_w64(bad) == 0ull) break;
            __builtin_amdgcn_s_sleep(1);
            asm volatile("buffer_load_dwordx4 %0, %1, %2, %3 offen sc1\n\ts_waitcnt vmcnt(0)"
                         : "=v"(pollv) : "v"(vzero), "s"(srd), "s"(fso1) : "memory");
        }
    };
    const u32 pgb = WS_PART + (it * 4 + s) * 98304 + w * 2048;
    const u32 phb = WS_PH + (it * 4 + s) * 16384 + w * 4096;
    auto xfrag = [&](int ks) -> u32 { return xsb + ((ks / 3) * 12 + 3 * w + ks % 3) * 1024; };
    const u32 fso2 = WS_FLAG + 4096 + (it * 4 + s) * 64;
    u32 fv;
    const u32 vfl = (lane & 3) * 4;
    auto poll2 = [&]() {
        asm volatile("buffer_load_dword %0, %1, %2, %3 offen sc1" : "=v"(fv) : "v"(vfl), "s"(srd), "s"(fso2) : "memory");
    };
    auto check2 = [&]() {
        asm volatile("" : "+v"(fv));
#pragma nounroll
        for (u32 spins = 0; spins < (1u << 20); ++spins) {
            if (__builtin_amdgcn_ballot_w64(fv == 0u) == 0ull) break;
            __builtin_amdgcn_s_sleep(1);
            asm volatile("buffer_load_dword %0, %1, %2, %3 offen sc1\n\ts_waitcnt vmcnt(0)" : "=v"(fv) : "v"(vfl), "s"(srd), "s"(fso2) : "memory");
        }
    };
    static_for<G1, GE>([&](auto GG) {
        constexpr int G = GG.value, c = G / 36, g = G % 36, m = G - 324;
        if constexpr (g == 0 && G > 0) STAMP(2 + G / 36);
        if constexpr (PADS && pad_at(G) >= 0) asm volatile("s_branch 1f\n\t.p2align 12\nk_mlp_pad%c0:\n\ts_setpc_b64 s[30:31]\n1:" :: "n"(pad_at(G)));
        if constexpr (s_bar(G)) {
            if constexpr (G % 6 == 0) TW(asm volatile("s_waitcnt vmcnt(%0) lgkmcnt(%1)\n\ts_barrier" :: "n"(bar_wait(G)), "n"(G == G1 ? 8 : 6) : "memory"));
            else TW(asm volatile("s_waitcnt vmcnt(%0)\n\ts_barrier" :: "n"(bar_wait(G)) : "memory"));
        } else if constexpr (G % 6 == 0) asm volatile("s_waitcnt lgkmcnt(%0)" :: "n"(G == 42 ? 4 : G + 6 < GE ? 6 : 0) : "memory");
        u32x4& wf = Wf[G % 18];
        constexpr int sl = G < 324 ? 0 : m_slot(m), e = G < 324 ? 0 : m_ent(m), tl = G < 324 ? (g & 1) : m_half(m);
        constexpr bool first = G < 324 ? g < 2 : m_first(m), own = G >= 324 && ent_own(e);
        if constexpr (G >= 324) {
            if constexpr (ent_first(m) && !own) TW(asm volatile("s_waitcnt vmcnt(%0)" :: "n"(younger(SCH.hgap[e], G)) : "memory"));
        }
        auto mm = [&](auto TT) {
            constexpr int tt = TT.value;
            f32x4& ac = G < 324 ? acc[c & 1][2 * tl + tt] : acc2[sl][2 * tl + tt];
            u32x4& a = wf; u32x4& bo = G < 324 ? Xa[(g & ~1) + tt] : H[e][tt];
            if constexpr (first && own) asm volatile(MFMA "%0, %1, %2, 0" : "=&v"(ac) : "v"(a), "v"(bo));
            else if constexpr (first) asm volatile(MFMA "%0, %1, %2, 0" : "=&v"(ac) : "v"(a), "a"(bo));
            else if constexpr (own) asm volatile(MFMA "%0, %1, %2, %0" : "+v"(ac) : "v"(a), "v"(bo));
            else asm volatile(MFMA "%0, %1, %2, %0" : "+v"(ac) : "v"(a), "a"(bo));
            SB();
        };
        constexpr int hs = G >= 72 && G < 324 && g >= 1 ? (g < 12 ? g - 1 : g) : -1;
        mm(IC<0>{});
        if constexpr (G + 12 < GE) rdw(IC<G + 12>{});
        if constexpr (hs >= 0) act(IC<c - 1>{}, IC<hs>{}, IC<0>{});
        if constexpr (G >= 52 && G < 70) act(IC<0>{}, IC<2 * (G - 52)>{}, IC<2>{});
        if constexpr (G >= 326 && G < 344) act(IC<8>{}, IC<2 * (G - 326)>{}, IC<2>{});
        SB();
        mm(IC<1>{});
        if constexpr (G == 50) { corr(IC<0>{}); SB(); rde(IC<1>{}); }
        if constexpr (G < 324 && g == (c == 1 ? 30 : 11)) {
            if constexpr (c > 1) { act(IC<c - 1>{}, IC<10>{}, IC<1>{}); SB(); }
            corr(IC<c>{}); SB();
            if constexpr (c > 1) act(IC<c - 1>{}, IC<11>{}, IC<2>{});
        } else if constexpr (hs >= 0) act(IC<c - 1>{}, IC<hs>{}, IC<1>{});
        if constexpr (g == (c == 1 ? 32 : 14) && c < 8) rde(IC<c + 1>{});
        if constexpr (SCH.dn[G] >= 0) {
            dma(IC<SCH.dn[G] % 3>{}, wfb + (SCH.dn[G] / 3) * 12288);
            if constexpr (SCH.dn[G] % 3 == 2 && SCH.dn[G] < 161) set_m0(wl + ring_slot(SCH.dn[G] / 3 + 1) * 12288);
        }
        if constexpr (G == 36 || G == 37) stats(IC<G - 36>{});
        if constexpr (G >= 43 && G < 49) stats(IC<G - 41>{});
        if constexpr (G >= 52 && G < 70) act(IC<0>{}, IC<2 * (G - 52) + 1>{}, IC<2>{});
        if constexpr (G >= 326 && G < 344) act(IC<8>{}, IC<2 * (G - 326) + 1>{}, IC<2>{});
        if constexpr (s_hstore(G) >= 0) {
            constexpr int hf = s_hstore(G), cc = c - 1;
            __builtin_amdgcn_raw_buffer_store_b128(H[cc][hf], rs, voff + hf * 1024, hown + cc * 8192, 16);
        }
        if constexpr (s_pub(G) > 0) flag_store(s_pub(G), fso1 + s * 4);
        if constexpr (G == PUB2) flag_store(1u, fso2 + kq * 4);
        if constexpr (G == C1 || G == C2 || G == C3) {
            TW(asm volatile("s_waitcnt vmcnt(%0)" :: "n"(younger(G == C1 ? P1 : G == C2 ? P2 : P3, G)) : "memory"));
            check(G == C1 ? 2 : G == C2 ? 4 : 9);
        }
        if constexpr (G == P1 || G == P2 || G == P3) poll();
        if constexpr (G == P4) poll2();
        if constexpr (SCH.hl[G] >= 0) hload(IC<SCH.hl[G] / 2>{}, IC<SCH.hl[G] % 2>{});
        if constexpr (s_pstore(G) >= 0) {
            constexpr int sg = s_pstore(G) / 2, pt = s_pstore(G) % 2;
            const f32x4 a0 = acc2[sg][2 * pt], a1 = acc2[sg][2 * pt + 1];
            const u32x4 v = {pkbf(a0[0], a0[1]), pkbf(a0[2], a0[3]), pkbf(a1[0], a1[1]), pkbf(a1[2], a1[3])};
            if constexpr (sg == 0) __builtin_amdgcn_raw_buffer_store_b128(v, rs, voff, phb + kq * 1024, 16);
            else __builtin_amdgcn_raw_buffer_store_b128(v, rs, voff + pt * 1024, pgb + ((kq + sg) & 3) * 24576 + (sg - 1) * 8192, 16);
        }
        if constexpr (s_res(G)) {
            constexpr int i = (G - 620) / 2;
            set_m0(wl + RESL + i * 1024);
            const u32 so = xfrag(9 * s + (i < 2 ? 2 * kq + i : 8));
            asm volatile("s_nop 0" ::: "memory");
            dma(IC<0>{}, so);
        }
        SB();
    });
    STAMP(20);

    {
        u32x2 xr[5];
        f32x4 gb[2];
        f32x2 gh;
        const u32 ra = wl + RESL + (4 * (lane >> 4) + ((lane & 15) >> 2)) * 64 + (lane & 3) * 8;
        const u32 ga = lbase + L_EXF + 4608 + (lane >> 4) * 16;
        asm volatile("s_waitcnt vmcnt(1)" ::: "memory");
        asm volatile("ds_read_b64_tr_b16 %0, %5\n\tds_read_b64_tr_b16 %1, %5 offset:32\n\tds_read_b64_tr_b16 %2, %5 offset:1024\n\t"
                     "ds_read_b64_tr_b16 %3, %5 offset:1056\n\tds_read_b64_tr_b16 %4, %6 offset:2048"
                     : "=&v"(xr[0]), "=&v"(xr[1]), "=&v"(xr[2]), "=&v"(xr[3]), "=&v"(xr[4]) : "v"(ra), "v"(ra + (kq >> 1) * 32) : "memory");
        asm volatile("ds_read_b128 %0, %3\n\tds_read_b128 %1, %3 offset:64\n\tds_read_b64 %2, %4 offset:128\n\ts_waitcnt vmcnt(0) lgkmcnt(0)"
                     : "=&v"(gb[0]), "=&v"(gb[1]), "=&v"(gh) : "v"(ga), "v"(ga + (kq & 1) * 8) : "memory");
        asm volatile("" : "+v"(xr[0]), "+v"(xr[1]), "+v"(xr[2]), "+v"(xr[3]), "+v"(xr[4]));
        check2();
        STAMP(21);
        u32x4 P[6];
        u32 Q[4];
        const u32 pin = pgb + kq * 24576;
        asm volatile("buffer_load_dwordx4 %4, %10, %11, %14 offen sc1\n\tbuffer_load_dwordx4 %5, %10, %11, %14 offen offset:1024 sc1\n\t"
                     "buffer_load_dwordx4 %2, %10, %11, %13 offen sc1\n\tbuffer_load_dwordx4 %3, %10, %11, %13 offen offset:1024 sc1\n\t"
                     "buffer_load_dwordx4 %0, %10, %11, %12 offen sc1\n\tbuffer_load_dwordx4 %1, %10, %11, %12 offen offset:1024 sc1\n\t"
                     "buffer_load_dword %6, %15, %11, %16 offen sc1\n\tbuffer_load_dword %7, %15, %11, %16 offen offset:1024 sc1\n\t"
                     "buffer_load_dword %8, %15, %11, %16 offen offset:2048 sc1\n\tbuffer_load_dword %9, %15, %11, %16 offen offset:3072 sc1\n\t"
                     "s_waitcnt vmcnt(0)"
                     : "=&v"(P[0]), "=&v"(P[1]), "=&v"(P[2]), "=&v"(P[3]), "=&v"(P[4]), "=&v"(P[5]), "=&v"(Q[0]), "=&v"(Q[1]), "=&v"(Q[2]), "=&v"(Q[3])
                     : "v"(voff), "s"(srd), "s"(pin), "s"(pin + 8192), "s"(pin + 16384), "v"(voff + kq * 4), "s"(phb) : "memory");
        STAMP(22);
        const auto ro = __builtin_amdgcn_make_buffer_rsrc(out, 0, SEQ * DM * 2, 0x00020000);
        const u32 ov = ((it * 128 + w * 32 + (lane & 15)) * DM + s * 144 + 4 * (lane >> 4)) * 2;
        auto fin2 = [&](float a0, float a1, float g0, float g1, u32 xk) -> u32 {
            const u32 t = pkbf(a0 * -0.69314718055994530942f, a1 * -0.69314718055994530942f);
            const u32 gz = pkbf(bflo(t) * g0, bfhi(t) * g1);
            return pkbf(bflo(xk) + bflo(gz), bfhi(xk) + bfhi(gz));
        };
#pragma unroll
        for (int k = 0; k < 4; ++k) {
            u32x2 o;
#pragma unroll
            for (int j = 0; j < 2; ++j) {
                const int d = 2 * (k & 1) + j;
                const u32 p0 = P[k >> 1][d], p1 = P[2 + (k >> 1)][d], p2 = P[4 + (k >> 1)][d], xk = xr[k][j];
                o[j] = fin2(acc2[4][k][2 * j] + bflo(p0) + bflo(p1) + bflo(p2), acc2[4][k][2 * j + 1] + bfhi(p0) + bfhi(p1) + bfhi(p2),
                            gb[k >> 1][2 * j], gb[k >> 1][2 * j + 1], xk);
            }
            __builtin_amdgcn_raw_buffer_store_b64(o, ro, ov + (k >> 1) * 32, kq * 64 + (k & 1) * (16 * DM * 2), OAUX);
        }
        const u32 oh = fin2(bflo(Q[0]) + bflo(Q[1]) + bflo(Q[2]) + bflo(Q[3]), bfhi(Q[0]) + bfhi(Q[1]) + bfhi(Q[2]) + bfhi(Q[3]),
                            gh[0], gh[1], (kq & 1) ? xr[4][1] : xr[4][0]);
        __builtin_amdgcn_raw_buffer_store_b32(oh, ro, ov + 256, (kq & 1) * 4 + (kq >> 1) * (16 * DM * 2), OAUX);
    }
    asm volatile("k_mlp_end:");
}
#if defined(__HIP_DEVICE_COMPILE__)
#pragma clang attribute pop
#endif

extern "C" void kernel_launch(void* const* d_in, const int* in_sizes, int n_in, void* d_out, int out_size,
                              void* d_ws, size_t ws_size, hipStream_t stream) {
    const u16* x = (const u16*)d_in[0];
    char* ws = (char*)d_ws;
    (void)hipFuncSetAttribute((const void*)k_mlp, hipFuncAttributeMaxDynamicSharedMemorySize, MLP_LDS);
    k_prep<<<NB_PREP, 256, PREP_LDS, stream>>>(x, (const u16*)d_in[1], (const u16*)d_in[2], (const u16*)d_in[3],
                                               (const u16*)d_in[4], (const u16*)d_in[5], (const u16*)d_in[9],
                                               (const u16*)d_in[10], (const u16*)d_in[11], ws);
    k_conv<<<NB_PF + NB_CONV + NB_W1, 256, CONV_LDS, stream>>>((const u16*)d_in[6], (const u16*)d_in[8], ws);
    k_mlp<<<256, 256, MLP_LDS, stream>>>(ws, (u16*)d_out);
}
```

```cpp
#include <hip/hip_runtime.h>
#include <stdint.h>

typedef unsigned short u16;
typedef unsigned int u32;
typedef float f32x2 __attribute__((ext_vector_type(2)));
typedef float f32x4 __attribute__((ext_vector_type(4)));
typedef float f32x16 __attribute__((ext_vector_type(16)));
typedef _Float16 f16x2 __attribute__((ext_vector_type(2)));
typedef _Float16 f16x8 __attribute__((ext_vector_type(8)));
typedef __bf16 bf16x2 __attribute__((ext_vector_type(2)));
typedef __bf16 bf16x8 __attribute__((ext_vector_type(8)));
typedef short s16x4 __attribute__((ext_vector_type(4)));
typedef u32 u32x2 __attribute__((ext_vector_type(2)));
typedef u32 u32x4 __attribute__((ext_vector_type(4)));
#define LAS __attribute__((address_space(3)))
#define DI __device__ __forceinline__

constexpr int SEQ = 2048, DM = 576, NS = 128, FF = 4608;

constexpr u32 WS_MOD   = 0;
constexpr u32 WS_STAT  = 16384;
constexpr u32 WS_KSC   = 32768;
constexpr u32 WS_FLAG  = 36864;
constexpr u32 WS_EX    = 65536;
constexpr u32 WS_XT    = 1u << 20;
constexpr u32 WS_KR    = WS_XT + 2359296;
constexpr u32 WS_X1F   = WS_KR + 2359296;
constexpr u32 WS_WF    = WS_X1F + 2359296;
constexpr u32 WS_HF    = WS_WF + 10616832;
constexpr u32 WS_PART  = WS_HF + 18874368;
constexpr u32 WS_PH    = WS_PART + 6291456;
constexpr u32 WS_END   = WS_PH + 1048576;

DI float bflo(u32 u) { return __builtin_bit_cast(float, u << 16); }
DI float bfhi(u32 u) { return __builtin_bit_cast(float, u & 0xffff0000u); }
DI float bf2f(u16 v) { return __builtin_bit_cast(float, ((u32)v) << 16); }
DI u32 pkbf(float a, float b) { f32x2 v = {a, b}; return __builtin_bit_cast(u32, __builtin_convertvector(v, bf16x2)); }
DI u32 pkhf(float a, float b) { f32x2 v = {a, b}; return __builtin_bit_cast(u32, __builtin_convertvector(v, f16x2)); }
DI float rbf(float a) { return bflo(pkbf(a, a)); }
template <int CTRL, int RM = 0xf> DI float dppf(float v) {
    return __builtin_bit_cast(float, __builtin_amdgcn_update_dpp(0, __builtin_bit_cast(int, v), CTRL, RM, 0xf, true));
}
DI u32x4 trread2(const char* smem, u32 off0, u32 off1) {
    const u32x2 l = __builtin_bit_cast(u32x2, __builtin_amdgcn_ds_read_tr16_b64_v4i16((LAS s16x4*)(smem + off0)));
    const u32x2 h = __builtin_bit_cast(u32x2, __builtin_amdgcn_ds_read_tr16_b64_v4i16((LAS s16x4*)(smem + off1)));
    return (u32x4){l[0], l[1], h[0], h[1]};
}
DI void st16(char* ws, u32 off, u32x4 v) {
    __builtin_amdgcn_raw_buffer_store_b128(v, __builtin_amdgcn_make_buffer_rsrc(ws, 0, WS_END, 0x00020000), off, 0, 16);
}
DI void st4(char* ws, u32 off, u32 v) {
    __builtin_amdgcn_raw_buffer_store_b32(v, __builtin_amdgcn_make_buffer_rsrc(ws, 0, WS_END, 0x00020000), off, 0, 16);
}
DI float pow2i(int e) { e = e < -126 ? -126 : (e > 127 ? 127 : e); return __builtin_bit_cast(float, (u32)(e + 127) << 23); }
DI int expo(float x) { return (int)((__builtin_bit_cast(u32, x) >> 23) & 255u) - 127; }
DI u32 imgrow(u32 row) { return row * 1184u + (row >> 3) * 128u; }
DI f32x2 cmul(f32x2 z, f32x2 w, f32x2 wp) {
    const f32x2 zx = {z[0], z[0]}, zy = {z[1], z[1]};
    return __builtin_elementwise_fma(zy, wp, zx * w);
}
#define CODE_PREFETCH(sym, part) do { u32 lo_, hi_, len_; \
    asm volatile("s_getpc_b64 s[20:21]\n\ts_add_u32 s20, s20, " #sym "@rel32@lo+4\n\ts_addc_u32 s21, s21, " #sym "@rel32@hi+12\n\t" \
                 "s_mov_b32 %0, s20\n\ts_mov_b32 %1, s21\n\ts_mov_b32 %2, " #sym "_end-" #sym : "=s"(lo_), "=s"(hi_), "=s"(len_) :: "scc", "s20", "s21"); \
    const char* p_ = (const char*)(((unsigned long long)hi_ << 32) | lo_); \
    const u32 o_ = (threadIdx.x + (part) * 256u) * 128u; \
    if (o_ < len_) { u32 t_; asm volatile("global_load_dword %0, %1, off\n\ts_waitcnt vmcnt(0)" : "=v"(t_) : "v"(p_ + o_) : "memory"); } \
    } while (0)

#define DELAY1 20
#define NAV 26
constexpr int NA = NAV, NB = 36 - NA;
constexpr int NB_PF = 16, NB_LN = 64, NB_GEMV = 108, NB_KGEN = 576, NB_W2 = 144;
constexpr int NB_PREP = NB_PF + NB_LN + NB_GEMV + NB_KGEN + NB_W2;
constexpr int PREP_LDS = 38400;

extern "C" __global__ __launch_bounds__(256, 4) void k_prep(const u16* __restrict__ x, const u16* __restrict__ logA,
                                              const u16* __restrict__ Aim, const u16* __restrict__ Cre,
                                              const u16* __restrict__ Cim, const u16* __restrict__ logdt,
                                              const u16* __restrict__ w2, const u16* __restrict__ cvec,
                                              const u16* __restrict__ wmod, char* __restrict__ ws) {
    extern __shared__ __attribute__((aligned(16))) char smem[];
    const int tid = threadIdx.x, lane = tid & 63;
    const int wv = __builtin_amdgcn_readfirstlane(tid >> 6);
    int b = blockIdx.x;
    if (b >= NB_PF && b < NB_PF + NB_KGEN + NB_LN + NB_GEMV) b = b < NB_PF + NB_KGEN ? b + NB_LN + NB_GEMV : b - NB_KGEN;
    if (b < NB_PF) {
        if (b < 8) CODE_PREFETCH(k_prep, 0); else CODE_PREFETCH(k_conv, 0);
        if (b < 2) st16(ws, WS_FLAG + b * 4096 + tid * 16, (u32x4){0u, 0u, 0u, 0u});
    } else if ((b -= NB_PF) < NB_LN) {
        const int t0 = b * 32, tok = tid >> 3, sub = tid & 7;
        u32x4 v[9];
#pragma unroll
        for (int i = 0; i < 9; ++i) v[i] = *(const u32x4*)(x + (size_t)(t0 + tok) * DM + (sub + 8 * i) * 8);
#pragma unroll
        for (int i = 0; i < 9; ++i) *(u32x4*)(smem + imgrow(tok) + (sub + 8 * i) * 16) = v[i];
        __syncthreads();
        const int g = lane >> 4, qq = (lane & 15) >> 2, p = lane & 3;
#pragma unroll
        for (int i = 0; i < 9; ++i) {
            const int cb = wv * 9 + i;
            st16(ws, WS_XT + b * 36864 + cb * 1024 + (lane & 15) * 64 + g * 16,
                 trread2(smem, imgrow(8 * g + qq) + cb * 32 + p * 8, imgrow(8 * g + 4 + qq) + cb * 32 + p * 8));
        }
        float s = 0.f;
#pragma unroll
        for (int i = 0; i < 9; ++i)
#pragma unroll
            for (int k = 0; k < 4; ++k) s += bflo(v[i][k]) + bfhi(v[i][k]);
        s += dppf<0xB1>(s); s += dppf<0x4E>(s); s += dppf<0x141>(s);
        const float mean = s * (1.f / DM);
        float q = 0.f;
#pragma unroll
        for (int i = 0; i < 9; ++i)
#pragma unroll
            for (int k = 0; k < 4; ++k) {
                const float a = bflo(v[i][k]) - mean, c = bfhi(v[i][k]) - mean;
                q = fmaf(a, a, q); q = fmaf(c, c, q);
            }
        q += dppf<0xB1>(q); q += dppf<0x4E>(q); q += dppf<0x141>(q);
        if (sub == 0) st4(ws, WS_STAT + (t0 + tok) * 4, pkbf(mean, __builtin_amdgcn_sqrtf(rbf(rbf(q * (1.f / DM)) + 1e-5f))));
    } else if ((b -= NB_LN) < NB_GEMV) {
        const int seg = tid & 3, rg = tid >> 2;
        u32x4 v[9]; u16 cv[9];
#pragma unroll
        for (int i = 0; i < 9; ++i) {
            const int row = rg + 64 * i;
            cv[i] = cvec[row];
            v[i] = *(const u32x4*)(wmod + (size_t)row * (6 * DM) + b * 32 + seg * 8);
        }
        float acc[8];
#pragma unroll
        for (int k = 0; k < 8; ++k) acc[k] = 0.f;
#pragma unroll
        for (int i = 0; i < 9; ++i) {
            const float c = bf2f(cv[i]);
            const float sl = rbf(c * __builtin_amdgcn_rcpf(1.f + __builtin_amdgcn_exp2f(c * -1.44269504088896340736f)));
#pragma unroll
            for (int k = 0; k < 4; ++k) {
                acc[2 * k] = fmaf(sl, bflo(v[i][k]), acc[2 * k]);
                acc[2 * k + 1] = fmaf(sl, bfhi(v[i][k]), acc[2 * k + 1]);
            }
        }
#pragma unroll
        for (int k = 0; k < 8; ++k) { acc[k] += dppf<0x114>(acc[k]); acc[k] += dppf<0x118>(acc[k]); }
        float* red = (float*)smem;
        if ((lane & 15) >= 12) {
            *(f32x4*)(red + (tid >> 4) * 32 + seg * 8) = (f32x4){acc[0], acc[1], acc[2], acc[3]};
            *(f32x4*)(red + (tid >> 4) * 32 + seg * 8 + 4) = (f32x4){acc[4], acc[5], acc[6], acc[7]};
        }
        __syncthreads();
        if (tid < 32) {
            float t = 0.f;
#pragma unroll
            for (int i = 0; i < 16; ++i) t += red[i * 32 + tid];
            st4(ws, WS_MOD + (b * 32 + tid) * 4, __builtin_bit_cast(u32, rbf(t)));
        }
    } else if ((b -= NB_GEMV) < NB_KGEN) {
        const int d = b, hn = wv & 1, grp = wv >> 1;
        const int n = hn * 64 + lane, n2 = n ^ 64;
        const u16 la_ = logA[d * NS + n], ai_ = Aim[d * NS + n], cr_ = Cre[d * NS + n], ci_ = Cim[d * NS + n];
        const u16 cr2_ = Cre[d * NS + n2], ci2_ = Cim[d * NS + n2], ld_ = logdt[d];
        u32 ta[8];
#pragma unroll
        for (int j = 0; j < 8; j += 2) {
            const float l0 = (float)(2047 - tid * 8 - j);
            const u32 pk = pkbf(l0, l0 - 1.f);
            const float p0 = bflo(pk), p1 = bfhi(pk);
            ta[j] = (u32)fminf(4.f * p0, p0 + 1536.f); ta[j + 1] = (u32)fminf(4.f * p1, p1 + 1536.f);
        }
        asm volatile("" : "+v"(ta[0]), "+v"(ta[1]), "+v"(ta[2]), "+v"(ta[3]), "+v"(ta[4]), "+v"(ta[5]), "+v"(ta[6]), "+v"(ta[7]));
        const float cr = bf2f(cr_), ci = bf2f(ci_);
        const float dt = rbf(__builtin_amdgcn_exp2f(bf2f(ld_) * 1.44269504088896340736f));
        const float zr = -rbf(__builtin_amdgcn_exp2f(bf2f(la_) * 1.44269504088896340736f)) * dt, zi = bf2f(ai_) * dt;
        float bs = fabsf(cr) + fabsf(ci) + fabsf(bf2f(cr2_)) + fabsf(bf2f(ci2_));
        bs += dppf<0xB1>(bs); bs += dppf<0x4E>(bs); bs += dppf<0x141>(bs); bs += dppf<0x140>(bs);
        bs += dppf<0x142, 0xa>(bs); bs += dppf<0x143, 0xc>(bs);
        bs = __builtin_bit_cast(float, __builtin_amdgcn_readlane(__builtin_bit_cast(int, bs), 63));
        const int se = 14 - expo(2.f * dt * bs * 1.0001f);
        const float scale = pow2i(se > 100 ? 100 : se);
        const float zr2 = zr * 1.44269504088896340736f;
        auto cexp = [&](float p) -> f32x2 {
            const float mag = __builtin_amdgcn_exp2f(zr2 * p);
            const float ang = zi * p;
            const float rr = __builtin_rintf(ang * 0.15915494309189535f);
            float fr = fmaf(ang, 0.15915494309189535f, -rr);
            fr = fmaf(ang, 6.4206382e-9f, fr);
            return (f32x2){mag * __builtin_amdgcn_cosf(fr), mag * __builtin_amdgcn_sinf(fr)};
        };
        const float p1 = grp ? 4.f : 1.f;
        const f32x2 f1 = cexp(p1), c1 = cexp(16.f * p1), s0 = cexp(grp ? 512.f : 0.f), s1 = cexp(256.f * p1);
        const f32x2 fw = {f1[0], -f1[1]}, fwp = {f1[1], f1[0]};
        const f32x2 cwp = {-c1[1], c1[0]};
        const float es = 2.f * dt * scale;
        f32x2 za = cmul((f32x2){es * cr, es * ci}, s0, (f32x2){-s0[1], s0[0]});
        f32x2 zb = cmul(za, s1, (f32x2){-s1[1], s1[0]});
        f32x2 zf = {1.f, 0.f};
        char* tab = smem + wv * 8704;
        u32 keep[16];
#pragma unroll
        for (int k = 0; k < 16; ++k) {
            *(u32*)(tab + k * 272 + lane * 4) = pkhf(zf[0], zf[1]);
            *(u32*)(tab + (16 + k) * 272 + lane * 4) = pkhf(za[0], za[1]);
            keep[k] = pkhf(zb[0], zb[1]);
            zf = cmul(zf, fw, fwp); za = cmul(za, c1, cwp); zb = cmul(zb, c1, cwp);
        }
        const int r16 = lane & 15, g = lane >> 4;
        f16x8 fb[4];
        f32x4 acc0 = {0.f, 0.f, 0.f, 0.f}, acc1 = {0.f, 0.f, 0.f, 0.f};
        __builtin_amdgcn_fence(__ATOMIC_RELEASE, "wavefront");
        __builtin_amdgcn_wave_barrier();
#pragma unroll
        for (int ks = 0; ks < 4; ++ks) {
            fb[ks] = *(const f16x8*)(tab + r16 * 272 + ks * 64 + g * 16);
            const f16x8 fa = *(const f16x8*)(tab + (16 + r16) * 272 + ks * 64 + g * 16);
            acc0 = __builtin_amdgcn_mfma_f32_16x16x32_f16(fb[ks], fa, acc0, 0, 0, 0);
        }
        __builtin_amdgcn_fence(__ATOMIC_RELEASE, "wavefront");
        __builtin_amdgcn_wave_barrier();
#pragma unroll
        for (int k = 0; k < 16; ++k) *(u32*)(tab + (16 + k) * 272 + lane * 4) = keep[k];
        __builtin_amdgcn_fence(__ATOMIC_RELEASE, "wavefront");
        __builtin_amdgcn_wave_barrier();
#pragma unroll
        for (int ks = 0; ks < 4; ++ks) {
            const f16x8 fa = *(const f16x8*)(tab + (16 + r16) * 272 + ks * 64 + g * 16);
            acc1 = __builtin_amdgcn_mfma_f32_16x16x32_f16(fb[ks], fa, acc1, 0, 0, 0);
        }
        __builtin_amdgcn_fence(__ATOMIC_RELEASE, "wavefront");
        __builtin_amdgcn_wave_barrier();
        *(f32x4*)(tab + r16 * 64 + g * 16) = acc0;
        *(f32x4*)(tab + 1024 + r16 * 64 + g * 16) = acc1;
        __syncthreads();
        float kv[8];
#pragma unroll
        for (int j = 0; j < 8; ++j) {
            const u32 o = ta[j] + (ta[j] >= 2048u ? 2u * 8704u - 2048u : 0u);
            kv[j] = *(const float*)(smem + o) + *(const float*)(smem + o + 8704);
        }
        st16(ws, WS_KR + d * 4096 + tid * 16, (u32x4){pkhf(kv[0], kv[1]), pkhf(kv[2], kv[3]), pkhf(kv[4], kv[5]), pkhf(kv[6], kv[7])});
        if (tid == 0) st4(ws, WS_KSC + d * 4, __builtin_bit_cast(u32, pow2i(se > 100 ? -100 : -se)));
    } else {
        b -= NB_KGEN;
        const u16* src = w2 + (size_t)b * 32 * DM;
        if (DELAY1 > 0) __builtin_amdgcn_s_sleep(DELAY1);
        const int tok = tid >> 3, sub = tid & 7;
        u32x4 v[9];
#pragma unroll
        for (int i = 0; i < 9; ++i) v[i] = *(const u32x4*)(src + tok * DM + (sub + 8 * i) * 8);
#pragma unroll
        for (int i = 0; i < 9; ++i) *(u32x4*)(smem + imgrow(tok) + (sub + 8 * i) * 16) = v[i];
        __syncthreads();
        const int kq = b / 36, kk = b - kq * 36, sp = kk / 9, c = kk - sp * 9;
        const int rel = (sp - wv) & 3;
        const int e = rel == 0 ? c : 9 + c * 3 + (rel - 1);
        const int r0 = 4 * (lane >> 4) + ((lane & 15) >> 2), p = lane & 3;
#pragma unroll
        for (int i = 0; i < 9; ++i) {
            const int sg = (i + 1) >> 1, j = i == 0 ? 0 : (i - 1) & 1;
            const int T = 9 * wv + (sg == 0 ? 8 : 2 * ((kq + sg) & 3) + j);
            const int m = sg == 4 ? 252 + 2 * e + j
                        : e < NA ? 7 * e + i
                        : sg == 0 ? 7 * NA + 2 * NB + (e - NA) : 7 * NA + (sg == 1 ? 0 : (2 * sg - 1) * NB) + (e - NA) * 2 + j;
            const u32 col = T * 32 + p * 8;
            st16(ws, WS_WF + ((kq * 4 + wv) * 648 + 324 + m) * 1024 + lane * 16, trread2(smem, imgrow(r0) + col, imgrow(r0 + 16) + col));
        }
    }
    asm volatile("k_prep_end:");
}

constexpr int NB_CONV = 576, NB_W1 = 144;
constexpr int CONV_LDS = 36864 + 4096;
constexpr int UCP = 4640, KB_OFF = 4 * UCP;

extern "C" __global__ __launch_bounds__(256, 4) void k_conv(const u16* __restrict__ dskip, const u16* __restrict__ w1,
                                              char* __restrict__ ws) {
    extern __shared__ __attribute__((aligned(16))) char smem[];
    const int tid = threadIdx.x, lane = tid & 63;
    const int wv = __builtin_amdgcn_readfirstlane(tid >> 6);
    const float* mod = (const float*)(ws + WS_MOD);
    int b = blockIdx.x;
    if (b >= NB_PF) b = b < NB_PF + NB_CONV ? b + NB_W1 : b - NB_CONV;
    if (b < NB_PF) {
        CODE_PREFETCH(k_mlp, b >> 3);
    } else if ((b -= NB_PF) < NB_W1) {
        b = (b & 7) * 18 + (b >> 3);
        const int q = b / 9, c = b - q * 9, f0 = b * 32;
        const int seg = tid & 3, rg = tid >> 2;
        u32x4 v[9]; float sb[9], cb[9];
#pragma unroll
        for (int i = 0; i < 9; ++i) {
            const int row = rg + 64 * i;
            v[i] = *(const u32x4*)(w1 + (size_t)row * FF + f0 + seg * 8);
            sb[i] = mod[3 * DM + row]; cb[i] = mod[4 * DM + row];
        }
        float S[8], B[8];
#pragma unroll
        for (int k = 0; k < 8; ++k) { S[k] = 0.f; B[k] = 0.f; }
#pragma unroll
        for (int i = 0; i < 9; ++i) {
            const float c1 = rbf(1.f + cb[i]);
            u32x4 o;
#pragma unroll
            for (int k = 0; k < 4; ++k) {
                const float lo = bflo(v[i][k]), hi = bfhi(v[i][k]);
                o[k] = pkbf(lo * c1, hi * c1);
                S[2 * k] += bflo(o[k]); S[2 * k + 1] += bfhi(o[k]);
                B[2 * k] = fmaf(sb[i], lo, B[2 * k]); B[2 * k + 1] = fmaf(sb[i], hi, B[2 * k + 1]);
            }
            *(u32x4*)(smem + ((tid ^ ((tid >> 3) & 2)) + 256 * i) * 16) = o;
        }
#pragma unroll
        for (int k = 0; k < 8; ++k) {
            S[k] += dppf<0x114>(S[k]); S[k] += dppf<0x118>(S[k]);
            B[k] += dppf<0x114>(B[k]); B[k] += dppf<0x118>(B[k]);
        }
        float* red = (float*)(smem + 36864);
        if ((lane & 15) >= 12) {
            float* r = red + (tid >> 4) * 64 + seg * 8;
            *(f32x4*)(r) = (f32x4){S[0], S[1], S[2], S[3]}; *(f32x4*)(r + 4) = (f32x4){S[4], S[5], S[6], S[7]};
            *(f32x4*)(r + 32) = (f32x4){B[0], B[1], B[2], B[3]}; *(f32x4*)(r + 36) = (f32x4){B[4], B[5], B[6], B[7]};
        }
        __syncthreads();
        const int g = lane >> 4, qq = (lane & 15) >> 2, p = lane & 3;
#pragma unroll
        for (int i = 0; i < 9; ++i) {
            const int s = wv * 9 + i;
            const u32 a = (32 * (s >> 1) + 4 * g + qq) * 64 + (((s & 1) ^ (g & 1)) * 32) + p * 8;
            st16(ws, WS_WF + (q * 648 + c * 36 + s) * 1024 + lane * 16, trread2(smem, a, a + 1024));
        }
        if (wv == 0) {
            float t = 0.f;
#pragma unroll
            for (int i = 0; i < 16; ++i) t += red[i * 64 + lane];
            const float B_ = __builtin_bit_cast(float, __builtin_amdgcn_ds_bpermute((lane ^ 32) * 4, __builtin_bit_cast(int, t)));
            const float S_ = -t;
            const float s1 = rbf(S_), s2 = rbf(S_ - s1), s3 = S_ - s1 - s2;
            const float b1 = rbf(B_), b2 = rbf(B_ - b1), b3 = B_ - b1 - b2;
            if (lane < 32) st16(ws, WS_EX + q * 8192 + (c * 32 + lane) * 16, (u32x4){pkbf(s1, s2), pkbf(s3, b1), pkbf(b2, b3), 0u});
        } else if (wv == 1 && c == 0 && lane < 48) {
            const int d = (q & 3) * 144 + (lane < 32 ? (q >> 2) * 32 + lane : 96 + lane);
            st4(ws, WS_EX + q * 8192 + 4608 + lane * 4, __builtin_bit_cast(u32, mod[5 * DM + d]));
        }
    } else {
        const int d = b - NB_W1;
        const int tp = tid > 0 ? tid * 8 - 4 : 0;
        const char* xb = ws + WS_XT + d * 64;
        const u32x4 x0 = *(const u32x4*)(xb + (tid >> 2) * 36864 + (tid & 3) * 16);
        const u32x2 xm = *(const u32x2*)(xb + (tp >> 5) * 36864 + (tp & 31) * 2);
        u32x4 st[3];
        st[0] = *(const u32x4*)(ws + WS_STAT + tp * 4);
        st[1] = *(const u32x4*)(ws + WS_STAT + tid * 32);
        st[2] = *(const u32x4*)(ws + WS_STAT + tid * 32 + 16);
        const u32x4 kv = *(const u32x4*)(ws + WS_KR + d * 4096 + tid * 16);
        const float sa = mod[d], ca = mod[DM + d], ga = mod[2 * DM + d];
        const float dsk = bf2f(dskip[d]);
        const float kinv = ((const float*)(ws + WS_KSC))[d];
        {
            const u32x4 z = {0u, 0u, 0u, 0u};
            if (tid < 96) *(u32x4*)(smem + (tid / 24) * UCP + (tid % 24) * 16) = z;
            else if (tid < 104) *(u32x4*)(smem + KB_OFF + 4096 + (tid - 96) * 16) = z;
        }
        const float c1 = rbf(1.f + ca);
        const int he = expo(64.f * fabsf(c1) + 2.f * fabsf(sa)) - 14;
        const float hs = pow2i(-he), oscale = pow2i(he) * kinv;
        const u32 xw[6] = {xm[0], xm[1], x0[0], x0[1], x0[2], x0[3]};
        u32 P[6], hb[4];
#pragma unroll
        for (int k = 0; k < 6; ++k) {
            const u32 s0 = st[k >> 1][(k & 1) * 2], s1 = st[k >> 1][(k & 1) * 2 + 1];
            const float n0 = (bflo(xw[k]) - bflo(s0)) * __builtin_amdgcn_rcpf(bfhi(s0));
            const float n1 = (bfhi(xw[k]) - bflo(s1)) * __builtin_amdgcn_rcpf(bfhi(s1));
            const u32 t = pkbf(fmaf(n0, c1, sa), fmaf(n1, c1, sa));
            if (k >= 2) hb[k - 2] = t;
            P[k] = pkhf(bflo(t) * hs, bfhi(t) * hs);
        }
        if (tid == 0) { P[0] = 0u; P[1] = 0u; }
        u32 Q[5];
#pragma unroll
        for (int k = 0; k < 5; ++k) Q[k] = __builtin_amdgcn_alignbit(P[k + 1], P[k], 16);
        {
            char* ub = smem + 384 + tid * 16;
            *(u32x4*)(ub) = (u32x4){Q[0], Q[1], Q[2], Q[3]};
            *(u32x4*)(ub + UCP) = (u32x4){P[1], P[2], P[3], P[4]};
            *(u32x4*)(ub + 2 * UCP) = (u32x4){Q[1], Q[2], Q[3], Q[4]};
            *(u32x4*)(ub + 3 * UCP) = (u32x4){P[2], P[3], P[4], P[5]};
            *(u32x4*)(smem + KB_OFF + tid * 16) = kv;
        }
        __syncthreads();
        const int c = lane & 15, g = lane >> 4;
        f32x4 acc[8];
#pragma unroll
        for (int j = 0; j < 8; ++j) acc[j] = (f32x4){0.f, 0.f, 0.f, 0.f};
        {
            const u32 lb = (u32)(uintptr_t)(LAS char*)smem;
            const u32 ka = lb + KB_OFF + 2 * (2076 - 4 * c + 8 * g - 64 * wv) - 3584 - 64;
            const u32 ua = lb + (c & 3) * UCP + 2 * (8 * g + 64 * (c >> 2) - 64 * wv + 192);
            u32x2 kl[2][8], kh[2][8]; u32x4 uf[2][8];
            auto rd = [&](int rr, int n) {
                asm volatile("ds_read_b64 %0, %3 offset:%5\n\tds_read_b64 %1, %3 offset:%6\n\tds_read_b128 %2, %4 offset:%7"
                             : "=&v"(kl[rr][n]), "=&v"(kh[rr][n]), "=&v"(uf[rr][n]) : "v"(ka), "v"(ua),
                               "n"((7 - n) * 512 + (1 - rr) * 64), "n"((7 - n) * 512 + (1 - rr) * 64 + 8), "n"(n * 512 + (1 - rr) * 64) : "memory");
            };
            auto mm = [&](int rr, int i, int dl) {
                const u32x4 k = {kl[rr][i][0], kl[rr][i][1], kh[rr][i][0], kh[rr][i][1]};
                acc[i + dl] = __builtin_amdgcn_mfma_f32_16x16x32_f16(__builtin_bit_cast(f16x8, uf[rr][dl]), __builtin_bit_cast(f16x8, k), acc[i + dl], 0, 0, 0);
            };
            auto shell = [&](int rr, int n) {
#pragma unroll
                for (int o = 0; o <= n && o + n < 8; ++o) { mm(rr, n, o); if (o < n) mm(rr, o, n); }
            };
            auto pin = [&](int rr, int n) { asm volatile("" : "+v"(kl[rr][n]), "+v"(kh[rr][n]), "+v"(uf[rr][n])); };
            rd(0, 0); rd(0, 7); rd(0, 1); rd(0, 6); rd(0, 2); rd(0, 5); rd(0, 3); rd(0, 4);
#pragma unroll
            for (int n = 7; n >= 0; --n) {
                if (n >= 4) { asm volatile("s_waitcnt lgkmcnt(%0)" :: "n"(n == 7 ? 15 : 6 * (n - 4) + 3 * (7 - n)) : "memory"); pin(0, n); pin(0, 7 - n); }
                shell(0, n);
                __builtin_amdgcn_sched_barrier(0);
                rd(1, 7 - n);
            }
#pragma unroll
            for (int n = 0; n < 8; ++n) {
                asm volatile("s_waitcnt lgkmcnt(%0)" :: "n"(n < 3 ? 15 : 3 * (7 - n)) : "memory");
                pin(1, n);
                shell(1, n);
                __builtin_amdgcn_sched_barrier(0);
            }
        }
        __syncthreads();
#pragma unroll
        for (int j = 0; j < 8; ++j) *(f32x4*)(smem + wv * 8192 + j * 1024 + lane * 16) = acc[j];
        __syncthreads();
        f32x4 y0 = *(const f32x4*)(smem + tid * 32), y1 = *(const f32x4*)(smem + tid * 32 + 16);
#pragma unroll
        for (int w = 1; w < 4; ++w) {
            y0 += *(const f32x4*)(smem + w * 8192 + tid * 32);
            y1 += *(const f32x4*)(smem + w * 8192 + tid * 32 + 16);
        }
        const float y[8] = {y0[0], y0[1], y0[2], y0[3], y1[0], y1[1], y1[2], y1[3]};
        u32x4 o;
#pragma unroll
        for (int k = 0; k < 4; ++k) {
            const u32 yb = pkbf(y[2 * k] * oscale, y[2 * k + 1] * oscale);
            const u32 sk = pkbf(bflo(hb[k]) * dsk, bfhi(hb[k]) * dsk);
            const u32 z = pkbf(bflo(yb) + bflo(sk), bfhi(yb) + bfhi(sk));
            const u32 gz = pkbf(bflo(z) * ga, bfhi(z) * ga);
            o[k] = pkbf(bflo(x0[k]) + bflo(gz), bfhi(x0[k]) + bfhi(gz));
        }
        st16(ws, WS_X1F + (tid >> 4) * 147456 + ((d / 48) * 12 + ((tid >> 2) & 3) * 3 + (d >> 4) % 3) * 1024 + (d & 15) * 64 + (tid & 3) * 16, o);
    }
    asm volatile("k_conv_end:");
}
constexpr u32 L_EXF = 147456;
constexpr int MLP_LDS = L_EXF + 12288;
constexpr int GE = 648;

#if defined(__HIP_DEVICE_COMPILE__)
#pragma clang attribute push(__attribute__((target("no-packed-fp32-ops"))), apply_to = function)
#endif
template <int N> struct IC { static constexpr int value = N; };
template <int A, int B, class F> DI void static_for(F&& f) {
    if constexpr (B - A == 1) f(IC<A>{});
    else if constexpr (A < B) { static_for<A, (A + B) / 2>(f); static_for<(A + B) / 2, B>(f); }
}
constexpr int imin(int a, int b) { return a < b ? a : b; }
constexpr int XW = 6;
#define XSP 7
#define XIV 8
constexpr int XI = XIV;
constexpr int x_dmas(int j) { return j <= 0 ? 0 : imin((4 * j + XSP - 1) / XSP, 3 * (19 - XI)); }
constexpr int G1 = 36;
constexpr int P1 = 36 * 5 + 7, P2 = 36 * 7 + 7, P3 = 400;
#define CD1 34
#define CD3 30
#define QW3 8
constexpr int C1 = P1 + CD1, C2 = P2 + CD1, C3 = P3 + CD3;
constexpr bool s_poll(int G) { return G == P1 || G == P2 || G == P3; }
#define PB2 606
#define PL4 640
constexpr int PUB2 = PB2, P4 = PL4;
constexpr int HS0 = 22, HS1 = 35;
constexpr int s_hstore(int G) { return G < 72 ? (G == 64 ? 0 : G == 71 ? 1 : -1) : G < 324 ? (G % 36 == HS0 ? 0 : G % 36 == HS1 ? 1 : -1) : G == 338 ? 0 : G == 346 ? 1 : -1; }
constexpr int B0 = 324 + 7 * NA;
constexpr int slot_end(int sg) { return B0 - 1 + (sg == 1 ? 2 : sg == 0 ? 3 : 2 * sg + 1) * NB; }
constexpr int s_pstore(int G) {
    for (int sg = 0; sg < 4; ++sg) { if (G == slot_end(sg) + 4) return 2 * sg; if (G == slot_end(sg) + 6 && sg > 0) return 2 * sg + 1; }
    return -1;
}
constexpr bool s_res(int G) { return G == 620 || G == 622 || G == 624; }
constexpr int s_pub(int G) { return G == 36 * 3 + 24 ? 2 : G == 36 * 5 + 24 ? 4 : G == 372 ? 9 : 0; }
constexpr int m_slot(int m) { return m < 7 * NA ? (m % 7 + 1) / 2 : m < 7 * NA + 2 * NB ? 1 : m < 7 * NA + 3 * NB ? 0 : m < 252 ? 2 + (m - 7 * NA - 3 * NB) / (2 * NB) : 4; }
constexpr int m_ent(int m) { return m < 7 * NA ? m / 7 : m < 7 * NA + 2 * NB ? NA + (m - 7 * NA) / 2 : m < 7 * NA + 3 * NB ? NA + (m - 7 * NA - 2 * NB)
                                  : m < 252 ? NA + ((m - 7 * NA - 3 * NB) % (2 * NB)) / 2 : (m - 252) / 2; }
constexpr int m_half(int m) { return m < 7 * NA ? (m % 7 == 0 ? 0 : (m % 7 - 1) & 1) : m < 7 * NA + 2 * NB ? (m - 7 * NA) & 1 : m < 7 * NA + 3 * NB ? 0
                                   : m < 252 ? (m - 7 * NA - 3 * NB) & 1 : m & 1; }
constexpr bool m_first(int m) { return m < 7 || m == 252 || m == 253; }
constexpr bool ent_first(int m) { return m < 7 * NA ? m % 7 == 0 : (m < 7 * NA + 2 * NB && ((m - 7 * NA) & 1) == 0); }
constexpr bool ent_own(int e) { return e < 9; }
constexpr int first_use(int e) { return e < NA ? 324 + 7 * e : B0 + 2 * (e - NA); }
constexpr bool s_ring(int G) { return G + 12 < GE && (G < 100 ? G % 12 == 0 : G < 490 ? G % 36 == 24 : G % 18 == 6); }
constexpr int ring_slot(int grp) { const int u = (grp / 3) & 3; return (u == 0 ? 0 : u == 1 ? 6 : u == 2 ? 9 : 3) + grp % 3; }
constexpr int need_bar(int grp) { int g = 12 * grp - 12; while (g > 0 && !s_ring(g)) --g; return g; }
struct Sched {
    int dn[GE];
    int dgap[162];
    int hl[GE];
    int pre[GE + 1];
    int hgap[36];
    bool ok;
    constexpr Sched() : dn(), dgap(), hl(), pre(), hgap(), ok(true) {
        int nd = 3 * XW, nh = 0, last2 = -99, lastbar = 24;
        for (int n = 0; n < 162; ++n) dgap[n] = -1;
        for (int e = 0; e < 36; ++e) hgap[e] = -1;
        for (int G = 0; G < GE; ++G) {
            dn[G] = -1; hl[G] = -1;
            int n = 0;
            if (G >= G1) {
                if (s_ring(G)) lastbar = G;
                const bool fixed = s_hstore(G) >= 0 || s_pstore(G) >= 0 || s_res(G);
                const bool quiet = (G >= P1 && G < P1 + 17) || (G >= P2 && G < P2 + 17) || (G >= P3 && G < P3 + QW3);
                const int sp2 = G < 324 ? 4 : 5;
                if (fixed) last2 = 2 * G;
                else if (!quiet && 2 * G - last2 >= sp2) {
                    int dd = 1 << 20, dh = 1 << 20;
                    if (nd < 162 && (nd / 3 < 12 || (nd / 3 - 11) * 12 <= lastbar + 6)) dd = need_bar(nd / 3) - 40;
                    const int lim = G > C3 ? 54 : G > C2 ? 24 : G > C1 ? 12 : 0;
                    if (nh < lim) dh = first_use(9 + nh / 2) - 45;
                    if (dd < (1 << 20) || dh < (1 << 20)) {
                        if (dd <= dh) { dn[G] = nd; dgap[nd] = G; if (need_bar(nd / 3) <= G) ok = false; ++nd; }
                        else { hl[G] = 18 + nh; hgap[9 + nh / 2] = G; if (first_use(9 + nh / 2) <= G) ok = false; ++nh; }
                        last2 = last2 + sp2 > 2 * G - 1 ? last2 + sp2 : 2 * G - (G < 324 ? 0 : 1);
                        n = 1;
                    }
                }
                n += fixed + s_poll(G) + (G == P4) + (s_pub(G) > 0) + (G == PUB2);
            }
            pre[G + 1] = pre[G] + n;
        }
        if (nd != 162 || nh != 54) ok = false;
    }
};
constexpr Sched SCH{};
static_assert(SCH.ok, "schedule misses a deadline");
constexpr int younger(int a, int b) { return imin(SCH.pre[b] - SCH.pre[a + 1], 63); }
constexpr bool s_bar(int G) { return G >= G1 && (s_ring(G) || s_pub(G) > 0 || G == PUB2); }
constexpr int bar_wait(int G) {
    int n = 63;
    if (s_ring(G)) { int grp = 53; while (need_bar(grp) > G) --grp; n = G == 36 ? 3 : younger(SCH.dgap[3 * grp + 2], G); }
    if (G == PUB2) n = imin(n, younger(slot_end(3) + 6, G));
    if (s_pub(G) > 0) n = imin(n, younger(G == 372 ? 346 : 36 * s_pub(G) + HS1, G));
    return n;
}
constexpr int frag_lds(int f) { return ring_slot(f / 12) * 12288 + (f % 12) * 1024; }

constexpr u32 RESL = ring_slot(45) * 12288;
static_assert(SCH.dgap[161] < 620, "M0");
#define OAUX 0
#define PGX 55
#define PG0 95
#define PG1 192
#define PG2 285
#define PG3 388
#define PG4 517
#define PG5 641
#define PADS 1
constexpr int pad_at(int G) { return G == PG0 ? 0 : G == PG1 ? 1 : G == PG2 ? 2 : G == PG3 ? 3 : G == PG4 ? 4 : G == PG5 ? 5 : -1; }
#define SB() __builtin_amdgcn_sched_barrier(0)
#define MFMA "v_mfma_f32_16x16x32_bf16 "

extern "C" __global__ __attribute__((aligned(4096))) __launch_bounds__(256, 1) void k_mlp(char* __restrict__ ws, u16* __restrict__ out) {
    extern __shared__ __attribute__((aligned(16))) char smem[];
    const int tid = threadIdx.x, lane = tid & 63;
    const int w = __builtin_amdgcn_readfirstlane(tid >> 6);
    const int b = blockIdx.x, xcd = b & 7, rr = b >> 3;
#define MAPV 1
    const int s = rr & 3;
    const int kq = MAPV == 0 ? xcd >> 1 : MAPV == 1 ? 2 * (xcd >> 2) + ((rr >> 2) & 1) : (rr >> 2) & 3;
    const int it = MAPV == 0 ? 8 * (xcd & 1) + (rr >> 2) : MAPV == 1 ? 4 * (xcd & 3) + (rr >> 3) : 2 * xcd + (rr >> 4);
    const int q = 4 * kq + s;
    const auto rs = __builtin_amdgcn_make_buffer_rsrc(ws, 0, WS_END, 0x00020000);
    const u32x4 srd = {(u32)(uintptr_t)ws, (u32)((uintptr_t)ws >> 32) & 0xffffu, WS_END, 0x00020000u};
    const u32 voff = lane * 16;
    const u32 lbase = (u32)(uintptr_t)(LAS char*)smem;
    const u32 wl = lbase + w * 3072;
    auto set_m0 = [&](u32 v) { asm volatile("s_mov_b32 m0, %0" :: "s"(v) : "memory"); };
    auto dma = [&](auto O, u32 so) {
        const u32 vo = voff; const u32x4 sr = srd;
        asm volatile("buffer_load_dwordx4 %0, %1, %2 offen offset:%3 lds" :: "v"(vo), "s"(sr), "s"(so), "n"(O.value * 1024) : "memory");
    };
    auto dma3 = [&](u32 dst, u32 so) {
        asm volatile("s_mov_b32 m0, %3\n\ts_nop 0\n\tbuffer_load_dwordx4 %0, %1, %2 offen lds\n\t"
                     "buffer_load_dwordx4 %0, %1, %2 offen offset:1024 lds\n\tbuffer_load_dwordx4 %0, %1, %2 offen offset:2048 lds"
                     :: "v"(voff), "s"(srd), "s"(so), "s"(dst) : "memory");
    };
    const u32 xsb = WS_X1F + it * 147456;
    const u32 wfb = WS_WF + q * 663552 + w * 3072;
    auto rq_dst = [&](int p) -> u32 {
        const int x = p <= 2 ? p - 1 : p <= 6 ? p - 2 : p - 3;
        return wl + (p == 0 ? 0 : p == 3 ? 12288 : p == 7 ? 24576 : p == 15 ? L_EXF : p > 15 ? ring_slot(p - 13) * 12288 : (3 + x % 9) * 12288);
    };
    auto rq_src = [&](int p) -> u32 {
        const int x = p <= 2 ? p - 1 : p <= 6 ? p - 2 : p - 3;
        return p == 0 ? wfb : p == 3 ? wfb + 12288 : p == 7 ? wfb + 24576 : p == 15 ? WS_EX + q * 8192 + w * 3072
             : p > 15 ? wfb + (p - 13) * 12288 : xsb + w * 3072 + x * 12288;
    };
#pragma unroll
    for (int p = 0; p < XI; ++p) {
        dma3(rq_dst(p), rq_src(p));
        if (PADS && p == 2) {
            if (w == 0) asm volatile("s_call_b64 s[30:31], k_mlp_pad0\n\ts_call_b64 s[30:31], k_mlp_pad4" ::: "s30", "s31");
            if (w == 1) asm volatile("s_call_b64 s[30:31], k_mlp_pad1\n\ts_call_b64 s[30:31], k_mlp_pad5" ::: "s30", "s31");
            if (w == 2) asm volatile("s_call_b64 s[30:31], k_mlp_pad6\n\ts_call_b64 s[30:31], k_mlp_pad2" ::: "s30", "s31");
            if (w == 3) asm volatile("s_call_b64 s[30:31], k_mlp_pad3" ::: "s30", "s31");
        }
    }
    set_m0(rq_dst(XI));
    const u32 hfb = WS_HF + (it * 4 + kq) * 36 * 8192 + w * 2048;
    u32x4 Wf[18], Ef[2];
    const u32 rd0 = lbase + voff;
    const u32 rb[3] = {rd0, rd0 + 49152, rd0 + 98304};
    auto rdw = [&](auto F) {
        constexpr int lo = frag_lds(F.value);
        u32x4& d = Wf[F.value % 18]; const u32 base = rb[lo / 49152];
        asm volatile("ds_read_b128 %0, %1 offset:%2" : "=v"(d) : "v"(base), "n"(lo % 49152) : "memory");
    };
    const u32 rex = lbase + L_EXF + (lane & 15) * 16;
    auto rde = [&](auto C) {
        u32x4& d0 = Ef[0]; u32x4& d1 = Ef[1]; const u32 base = rex;
        asm volatile("ds_read_b128 %0, %2 offset:%3\n\tds_read_b128 %1, %2 offset:%4" : "=&v"(d0), "=&v"(d1) : "v"(base), "n"(C.value * 512), "n"(C.value * 512 + 256) : "memory");
    };
    f32x4 acc[2][4];

    u32x4 Xa[36];
    f32x4 gram[2], rsum[2];
    {
        const u32 a0 = wl + (4 * (lane >> 4) + ((lane & 15) >> 2)) * 64 + (lane & 3) * 8;
        u32x4 ones = {0x3f803f80u, 0x3f803f80u, 0x3f803f80u, 0x3f803f80u};
        asm volatile("" : "+v"(ones));
        u32x2 t[3][4];
        const u32 xb[3] = {a0 + 36864, a0 + 73728, a0 + 110592};
        auto xrd = [&](auto KS, auto I) {
            constexpr int hk = 2 * KS.value + (I.value >> 1), sl = (hk / 3) % 9;
            u32x2& d = t[KS.value % 3][I.value]; const u32 base = xb[sl / 3];
            asm volatile("ds_read_b64_tr_b16 %0, %1 offset:%2" : "=v"(d) : "v"(base), "n"((sl % 3) * 12288 + (hk % 3) * 1024 + (I.value & 1) * 32) : "memory");
        };
        auto admit = [&](auto KS, auto J) {
            constexpr int ks = KS.value, x = (2 * ks + 1) / 3, pos = x <= 1 ? x + 1 : x <= 4 ? x + 2 : x + 3;
            constexpr int issued = 3 * XI + x_dmas(J.value);
            static_assert(issued >= 3 * pos + 3, "X order");
            if constexpr (ks % 6 == 0) asm volatile("s_waitcnt vmcnt(%0)\n\ts_barrier" :: "n"(issued - 3 * pos - 3) : "memory");
            else if constexpr (ks % 3 != 2) asm volatile("s_waitcnt vmcnt(%0)" :: "n"(issued - 3 * pos - 3) : "memory");
        };
        static_for<0, 2>([&](auto KS) {
            admit(KS, IC<0>{});
            static_for<0, 4>([&](auto I) { xrd(KS, I); });
            rdw(IC<2 * KS.value>{}); rdw(IC<2 * KS.value + 1>{});
        });
        u32x4 xv[2];
        static_for<0, 72>([&](auto J) {
            constexpr int j = J.value, ks = j / 4, k = j % 4;
            if constexpr (PADS && j == PGX) asm volatile("s_branch 1f\n\t.p2align 12\nk_mlp_pad6:\n\ts_setpc_b64 s[30:31]\n1:");
            u32x2 (&d)[4] = t[ks % 3];
            u32x4& x0 = xv[0]; u32x4& x1 = xv[1]; u32x4& on = ones;
            if constexpr (k == 0) {
                asm volatile("s_waitcnt lgkmcnt(%4)" : "+v"(d[0]), "+v"(d[1]), "+v"(d[2]), "+v"(d[3]) : "n"(ks < 17 ? 6 : 0) : "memory");
                xv[0] = (u32x4){d[0][0], d[0][1], d[2][0], d[2][1]};
                xv[1] = (u32x4){d[1][0], d[1][1], d[3][0], d[3][1]};
                asm volatile("s_nop 1" : "+v"(x0), "+v"(x1));
            }
            f32x4& c0 = k < 2 ? acc[0][2 * (k & 1)] : k == 2 ? gram[0] : rsum[0];
            f32x4& c1 = k < 2 ? acc[0][2 * (k & 1) + 1] : k == 2 ? gram[1] : rsum[1];
            u32x4& am0 = k < 2 ? Wf[(2 * ks + k) % 18] : k == 2 ? x0 : on;
            u32x4& am1 = k < 2 ? Wf[(2 * ks + k) % 18] : k == 2 ? x1 : on;
            if constexpr (j < 4) asm volatile(MFMA "%0, %1, %2, 0" : "=&v"(c0) : "v"(am0), "v"(x0));
            else asm volatile(MFMA "%0, %1, %2, %0" : "+v"(c0) : "v"(am0), "v"(x0));
            SB();
            if constexpr (ks < 16) {
                if constexpr (k == 0) admit(IC<ks + 2>{}, J);
                if constexpr (k < 2) { xrd(IC<ks + 2>{}, IC<2 * k>{}); xrd(IC<ks + 2>{}, IC<2 * k + 1>{}); }
                else rdw(IC<2 * ks + 2 + k>{});
            }
            SB();
            if constexpr (j < 4) asm volatile(MFMA "%0, %1, %2, 0" : "=&v"(c1) : "v"(am1), "v"(x1));
            else asm volatile(MFMA "%0, %1, %2, %0" : "+v"(c1) : "v"(am1), "v"(x1));
            SB();
            if constexpr (k >= 2) { Xa[2 * ks + k - 2] = xv[k - 2]; asm volatile("" : "+a"(Xa[2 * ks + k - 2])); }
            if constexpr (x_dmas(j + 1) > x_dmas(j)) {
                constexpr int n = x_dmas(j), p = XI + n / 3;
                static_assert(p < 12 || p == 15 || (3 * (p < 15 ? p - 12 : p - 13) + 2) / 2 <= ks, "slot still holds unread x");
                dma(IC<n % 3>{}, rq_src(p));
                if constexpr (n % 3 == 2 && p < 18) set_m0(rq_dst(p + 1));
            }
            SB();
        });
    }
    float ysc[2];
    u32x4 Xe[2];
    {
        asm volatile("s_waitcnt vmcnt(6)\n\ts_barrier" ::: "memory");
        static_for<36, 48>(rdw);
        rde(IC<0>{});
        set_m0(wl + ring_slot(XW) * 12288);
        SB();
    }
    float sq[2], mean[2];
    auto stats = [&](auto ST) {
        constexpr int st = ST.value >> 1, t = ST.value & 1;
        if constexpr (st == 0) {
            const float a = (lane & 1) ? gram[t][1] : gram[t][0], c = (lane & 1) ? gram[t][3] : gram[t][2];
            float v = (lane & 2) ? c : a;
            const u32 src = ((lane & 15) + 4 * (lane & 12)) * 4;
            float& d = sq[t];
            asm volatile("ds_bpermute_b32 %0, %1, %2" : "=v"(d) : "v"(src), "v"(v) : "memory");
            mean[t] = rsum[t][0] * (1.f / DM);
            asm volatile("" : "+v"(mean[t]));
        }
        if constexpr (st == 1) { asm volatile("" : "+v"(sq[t])); sq[t] = rbf(rbf(fmaxf(fmaf(-mean[t], mean[t], sq[t] * (1.f / DM)), 0.f)) + 1e-5f); asm volatile("" : "+v"(sq[t])); }
        if constexpr (st == 2) { sq[t] = rbf(__builtin_amdgcn_sqrtf(sq[t])); ysc[t] = -1.44269504088896340736f * __builtin_amdgcn_rcpf(sq[t]); asm volatile("" : "+v"(sq[t]), "+v"(ysc[t])); }
        if constexpr (st == 3) {
            const bool lo = lane < 16;
            Xe[t] = (u32x4){lo ? pkbf(mean[t], mean[t]) : 0u, lo ? pkbf(mean[t], sq[t]) : 0u, lo ? pkbf(sq[t], sq[t]) : 0u, 0u};
            asm volatile("" : "+v"(Xe[t]));
        }
    };
    auto corr = [&](auto C) {
        f32x4 (&a)[4] = acc[C.value & 1]; u32x4 (&e)[2] = Ef; u32x4 (&x)[2] = Xe;
        asm volatile(MFMA "%0, %4, %6, %0\n\t" MFMA "%1, %4, %7, %1\n\t" MFMA "%2, %5, %6, %2\n\t" MFMA "%3, %5, %7, %3"
                     : "+v"(a[0]), "+v"(a[1]), "+v"(a[2]), "+v"(a[3]) : "v"(e[0]), "v"(e[1]), "v"(x[0]), "v"(x[1]));
    };

    f32x4 acc2[5][4];
    float yv[16], ev[16];
    u32x4 H[36][2], pollv;
    auto flag_store = [&](u32 level, u32 so) {
        asm volatile("s_mov_b64 exec, 1\n\tbuffer_store_dword %0, off, %1, %2 sc1\n\ts_mov_b64 exec, -1" :: "v"(level), "s"(srd), "s"(so) : "memory");
    };
    auto act = [&](auto C, auto HS, auto PT) {
        constexpr int t = HS.value / 2, e = C.value, pt = PT.value;
        if constexpr (HS.value % 2 == 0) {
            if constexpr (pt != 1 && t < 16) { yv[t] = acc[e & 1][2 * ((t >> 2) & 1) + (t >> 3)][t & 3] * ysc[t >> 3]; asm volatile("" :: "v"(yv[t])); }
            if constexpr (pt != 0 && t >= 2) { yv[t - 2] = yv[t - 2] * ev[t - 2]; asm volatile("" :: "v"(yv[t - 2])); }
            if constexpr (pt != 0 && t < 16) { ev[t] = __builtin_amdgcn_exp2f(yv[t]); asm volatile("" :: "v"(ev[t])); }
        } else {
            if constexpr (pt != 1 && t >= 1 && t < 17) { ev[t - 1] = 1.f + ev[t - 1]; asm volatile("" :: "v"(ev[t - 1])); }
            if constexpr (pt != 0 && t >= 3 && (t & 1)) {
                u32 pk = pkbf(yv[t - 3], yv[t - 2]);
                asm volatile("" :: "v"(pk));
                H[e][(t - 3) / 8][((t - 3) / 2) & 3] = pk;
            }
            if constexpr (pt != 0 && t >= 1 && t < 17) { ev[t - 1] = __builtin_amdgcn_rcpf(ev[t - 1]); asm volatile("" :: "v"(ev[t - 1])); }
        }
    };
    const u32 hown = hfb + 9 * s * 8192;
    const u32 hpeer[3] = {hfb + 9 * ((s + 1) & 3) * 8192, hfb + 9 * ((s + 2) & 3) * 8192, hfb + 9 * ((s + 3) & 3) * 8192};
    auto hload = [&](auto E, auto HF) {
        constexpr int e = E.value, hf = HF.value, k = e - 9;
        const u32 so = hpeer[k % 3] + (k / 3) * 8192, vo = voff; const u32x4 sr = srd;
        u32x4& d = H[e][hf];
        asm volatile("buffer_load_dwordx4 %0, %1, %2, %3 offen offset:%4 sc1" : "=a"(d) : "v"(vo), "s"(sr), "s"(so), "n"(hf * 1024) : "memory");
    };
    const u32 vzero = 0;
    const u32 fso1 = WS_FLAG + (it * 4 + kq) * 64;
    auto poll = [&]() {
        asm volatile("buffer_load_dwordx4 %0, %1, %2, %3 offen sc1" : "=v"(pollv) : "v"(vzero), "s"(srd), "s"(fso1) : "memory");
    };
    auto check = [&](u32 level) {
        asm volatile("" : "+v"(pollv));
#pragma nounroll
        for (u32 spins = 0; spins < (1u << 20); ++spins) {
            const bool bad = pollv[0] < level || pollv[1] < level || pollv[2] < level || pollv[3] < level;
            if (__builtin_amdgcn_ballot_w64(bad) == 0ull) break;
            __builtin_amdgcn_s_sleep(1);
            asm volatile("buffer_load_dwordx4 %0, %1, %2, %3 offen sc1\n\ts_waitcnt vmcnt(0)"
                         : "=v"(pollv) : "v"(vzero), "s"(srd), "s"(fso1) : "memory");
        }
    };
    const u32 pgb = WS_PART + (it * 4 + s) * 98304 + w * 2048;
    const u32 phb = WS_PH + (it * 4 + s) * 16384 + w * 4096;
    auto xfrag = [&](int ks) -> u32 { return xsb + ((ks / 3) * 12 + 3 * w + ks % 3) * 1024; };
    const u32 fso2 = WS_FLAG + 4096 + (it * 4 + s) * 64;
    u32 fv;
    const u32 vfl = (lane & 3) * 4;
    auto poll2 = [&]() {
        asm volatile("buffer_load_dword %0, %1, %2, %3 offen sc1" : "=v"(fv) : "v"(vfl), "s"(srd), "s"(fso2) : "memory");
    };
    auto check2 = [&]() {
        asm volatile("" : "+v"(fv));
#pragma nounroll
        for (u32 spins = 0; spins < (1u << 20); ++spins) {
            if (__builtin_amdgcn_ballot_w64(fv == 0u) == 0ull) break;
            __builtin_amdgcn_s_sleep(1);
            asm volatile("buffer_load_dword %0, %1, %2, %3 offen sc1\n\ts_waitcnt vmcnt(0)" : "=v"(fv) : "v"(vfl), "s"(srd), "s"(fso2) : "memory");
        }
    };
    static_for<G1, GE>([&](auto GG) {
        constexpr int G = GG.value, c = G / 36, g = G % 36, m = G - 324;
        if constexpr (PADS && pad_at(G) >= 0) asm volatile("s_branch 1f\n\t.p2align 12\nk_mlp_pad%c0:\n\ts_setpc_b64 s[30:31]\n1:" :: "n"(pad_at(G)));
        if constexpr (s_bar(G)) {
            if constexpr (G % 6 == 0) asm volatile("s_waitcnt vmcnt(%0) lgkmcnt(%1)\n\ts_barrier" :: "n"(bar_wait(G)), "n"(G == G1 ? 8 : 6) : "memory");
            else asm volatile("s_waitcnt vmcnt(%0)\n\ts_barrier" :: "n"(bar_wait(G)) : "memory");
        } else if constexpr (G % 6 == 0) asm volatile("s_waitcnt lgkmcnt(%0)" :: "n"(G == 42 ? 4 : G + 6 < GE ? 6 : 0) : "memory");
        u32x4& wf = Wf[G % 18];
        constexpr int sl = G < 324 ? 0 : m_slot(m), e = G < 324 ? 0 : m_ent(m), tl = G < 324 ? (g & 1) : m_half(m);
        constexpr bool first = G < 324 ? g < 2 : m_first(m), own = G >= 324 && ent_own(e);
        if constexpr (G >= 324) {
            if constexpr (ent_first(m) && !own) asm volatile("s_waitcnt vmcnt(%0)" :: "n"(younger(SCH.hgap[e], G)) : "memory");
        }
        auto mm = [&](auto TT) {
            constexpr int tt = TT.value;
            f32x4& ac = G < 324 ? acc[c & 1][2 * tl + tt] : acc2[sl][2 * tl + tt];
            u32x4& a = wf; u32x4& bo = G < 324 ? Xa[(g & ~1) + tt] : H[e][tt];
            if constexpr (first && own) asm volatile(MFMA "%0, %1, %2, 0" : "=&v"(ac) : "v"(a), "v"(bo));
            else if constexpr (first) asm volatile(MFMA "%0, %1, %2, 0" : "=&v"(ac) : "v"(a), "a"(bo));
            else if constexpr (own) asm volatile(MFMA "%0, %1, %2, %0" : "+v"(ac) : "v"(a), "v"(bo));
            else asm volatile(MFMA "%0, %1, %2, %0" : "+v"(ac) : "v"(a), "a"(bo));
            SB();
        };
        constexpr int hs = G >= 72 && G < 324 && g >= 1 ? (g < 12 ? g - 1 : g) : -1;
        mm(IC<0>{});
        if constexpr (G + 12 < GE) rdw(IC<G + 12>{});
        if constexpr (hs >= 0) act(IC<c - 1>{}, IC<hs>{}, IC<0>{});
        if constexpr (G >= 52 && G < 70) act(IC<0>{}, IC<2 * (G - 52)>{}, IC<2>{});
        if constexpr (G >= 326 && G < 344) act(IC<8>{}, IC<2 * (G - 326)>{}, IC<2>{});
        SB();
        mm(IC<1>{});
        if constexpr (G == 50) { corr(IC<0>{}); SB(); rde(IC<1>{}); }
        if constexpr (G < 324 && g == (c == 1 ? 30 : 11)) {
            if constexpr (c > 1) { act(IC<c - 1>{}, IC<10>{}, IC<1>{}); SB(); }
            corr(IC<c>{}); SB();
            if constexpr (c > 1) act(IC<c - 1>{}, IC<11>{}, IC<2>{});
        } else if constexpr (hs >= 0) act(IC<c - 1>{}, IC<hs>{}, IC<1>{});
        if constexpr (g == (c == 1 ? 32 : 14) && c < 8) rde(IC<c + 1>{});
        if constexpr (SCH.dn[G] >= 0) {
            dma(IC<SCH.dn[G] % 3>{}, wfb + (SCH.dn[G] / 3) * 12288);
            if constexpr (SCH.dn[G] % 3 == 2 && SCH.dn[G] < 161) set_m0(wl + ring_slot(SCH.dn[G] / 3 + 1) * 12288);
        }
        if constexpr (G == 36 || G == 37) stats(IC<G - 36>{});
        if constexpr (G >= 43 && G < 49) stats(IC<G - 41>{});
        if constexpr (G >= 52 && G < 70) act(IC<0>{}, IC<2 * (G - 52) + 1>{}, IC<2>{});
        if constexpr (G >= 326 && G < 344) act(IC<8>{}, IC<2 * (G - 326) + 1>{}, IC<2>{});
        if constexpr (s_hstore(G) >= 0) {
            constexpr int hf = s_hstore(G), cc = c - 1;
            __builtin_amdgcn_raw_buffer_store_b128(H[cc][hf], rs, voff + hf * 1024, hown + cc * 8192, 16);
        }
        if constexpr (s_pub(G) > 0) flag_store(s_pub(G), fso1 + s * 4);
        if constexpr (G == PUB2) flag_store(1u, fso2 + kq * 4);
        if constexpr (G == C1 || G == C2 || G == C3) {
            asm volatile("s_waitcnt vmcnt(%0)" :: "n"(younger(G == C1 ? P1 : G == C2 ? P2 : P3, G)) : "memory");
            check(G == C1 ? 2 : G == C2 ? 4 : 9);
        }
        if constexpr (G == P1 || G == P2 || G == P3) poll();
        if constexpr (G == P4) poll2();
        if constexpr (SCH.hl[G] >= 0) hload(IC<SCH.hl[G] / 2>{}, IC<SCH.hl[G] % 2>{});
        if constexpr (s_pstore(G) >= 0) {
            constexpr int sg = s_pstore(G) / 2, pt = s_pstore(G) % 2;
            const f32x4 a0 = acc2[sg][2 * pt], a1 = acc2[sg][2 * pt + 1];
            const u32x4 v = {pkbf(a0[0], a0[1]), pkbf(a0[2], a0[3]), pkbf(a1[0], a1[1]), pkbf(a1[2], a1[3])};
            if constexpr (sg == 0) __builtin_amdgcn_raw_buffer_store_b128(v, rs, voff, phb + kq * 1024, 16);
            else __builtin_amdgcn_raw_buffer_store_b128(v, rs, voff + pt * 1024, pgb + ((kq + sg) & 3) * 24576 + (sg - 1) * 8192, 16);
        }
        if constexpr (s_res(G)) {
            constexpr int i = (G - 620) / 2;
            set_m0(wl + RESL + i * 1024);
            const u32 so = xfrag(9 * s + (i < 2 ? 2 * kq + i : 8));
            asm volatile("s_nop 0" ::: "memory");
            dma(IC<0>{}, so);
        }
        SB();
    });

    {
        u32x2 xr[5];
        f32x4 gb[2];
        f32x2 gh;
        const u32 ra = wl + RESL + (4 * (lane >> 4) + ((lane & 15) >> 2)) * 64 + (lane & 3) * 8;
        const u32 ga = lbase + L_EXF + 4608 + (lane >> 4) * 16;
        asm volatile("s_waitcnt vmcnt(1)" ::: "memory");
        asm volatile("ds_read_b64_tr_b16 %0, %5\n\tds_read_b64_tr_b16 %1, %5 offset:32\n\tds_read_b64_tr_b16 %2, %5 offset:1024\n\t"
                     "ds_read_b64_tr_b16 %3, %5 offset:1056\n\tds_read_b64_tr_b16 %4, %6 offset:2048"
                     : "=&v"(xr[0]), "=&v"(xr[1]), "=&v"(xr[2]), "=&v"(xr[3]), "=&v"(xr[4]) : "v"(ra), "v"(ra + (kq >> 1) * 32) : "memory");
        asm volatile("ds_read_b128 %0, %3\n\tds_read_b128 %1, %3 offset:64\n\tds_read_b64 %2, %4 offset:128\n\ts_waitcnt vmcnt(0) lgkmcnt(0)"
                     : "=&v"(gb[0]), "=&v"(gb[1]), "=&v"(gh) : "v"(ga), "v"(ga + (kq & 1) * 8) : "memory");
        asm volatile("" : "+v"(xr[0]), "+v"(xr[1]), "+v"(xr[2]), "+v"(xr[3]), "+v"(xr[4]));
        check2();
        u32x4 P[6];
        u32 Q[4];
        const u32 pin = pgb + kq * 24576;
        asm volatile("buffer_load_dwordx4 %4, %10, %11, %14 offen sc1\n\tbuffer_load_dwordx4 %5, %10, %11, %14 offen offset:1024 sc1\n\t"
                     "buffer_load_dwordx4 %2, %10, %11, %13 offen sc1\n\tbuffer_load_dwordx4 %3, %10, %11, %13 offen offset:1024 sc1\n\t"
                     "buffer_load_dwordx4 %0, %10, %11, %12 offen sc1\n\tbuffer_load_dwordx4 %1, %10, %11, %12 offen offset:1024 sc1\n\t"
                     "buffer_load_dword %6, %15, %11, %16 offen sc1\n\tbuffer_load_dword %7, %15, %11, %16 offen offset:1024 sc1\n\t"
                     "buffer_load_dword %8, %15, %11, %16 offen offset:2048 sc1\n\tbuffer_load_dword %9, %15, %11, %16 offen offset:3072 sc1\n\t"
                     "s_waitcnt vmcnt(0)"
                     : "=&v"(P[0]), "=&v"(P[1]), "=&v"(P[2]), "=&v"(P[3]), "=&v"(P[4]), "=&v"(P[5]), "=&v"(Q[0]), "=&v"(Q[1]), "=&v"(Q[2]), "=&v"(Q[3])
                     : "v"(voff), "s"(srd), "s"(pin), "s"(pin + 8192), "s"(pin + 16384), "v"(voff + kq * 4), "s"(phb) : "memory");
        const auto ro = __builtin_amdgcn_make_buffer_rsrc(out, 0, SEQ * DM * 2, 0x00020000);
        const u32 ov = ((it * 128 + w * 32 + (lane & 15)) * DM + s * 144 + 4 * (lane >> 4)) * 2;
        auto fin2 = [&](float a0, float a1, float g0, float g1, u32 xk) -> u32 {
            const u32 t = pkbf(a0 * -0.69314718055994530942f, a1 * -0.69314718055994530942f);
            const u32 gz = pkbf(bflo(t) * g0, bfhi(t) * g1);
            return pkbf(bflo(xk) + bflo(gz), bfhi(xk) + bfhi(gz));
        };
#pragma unroll
        for (int k = 0; k < 4; ++k) {
            u32x2 o;
#pragma unroll
            for (int j = 0; j < 2; ++j) {
                const int d = 2 * (k & 1) + j;
                const u32 p0 = P[k >> 1][d], p1 = P[2 + (k >> 1)][d], p2 = P[4 + (k >> 1)][d], xk = xr[k][j];
                o[j] = fin2(acc2[4][k][2 * j] + bflo(p0) + bflo(p1) + bflo(p2), acc2[4][k][2 * j + 1] + bfhi(p0) + bfhi(p1) + bfhi(p2),
                            gb[k >> 1][2 * j], gb[k >> 1][2 * j + 1], xk);
            }
            __builtin_amdgcn_raw_buffer_store_b64(o, ro, ov + (k >> 1) * 32, kq * 64 + (k & 1) * (16 * DM * 2), OAUX);
        }
        const u32 oh = fin2(bflo(Q[0]) + bflo(Q[1]) + bflo(Q[2]) + bflo(Q[3]), bfhi(Q[0]) + bfhi(Q[1]) + bfhi(Q[2]) + bfhi(Q[3]),
                            gh[0], gh[1], (kq & 1) ? xr[4][1] : xr[4][0]);
        __builtin_amdgcn_raw_buffer_store_b32(oh, ro, ov + 256, (kq & 1) * 4 + (kq >> 1) * (16 * DM * 2), OAUX);
    }
    asm volatile("k_mlp_end:");
}
#if defined(__HIP_DEVICE_COMPILE__)
#pragma clang attribute pop
#endif

extern "C" void kernel_launch(void* const* d_in, const int* in_sizes, int n_in, void* d_out, int out_size,
                              void* d_ws, size_t ws_size, hipStream_t stream) {
    const u16* x = (const u16*)d_in[0];
    char* ws = (char*)d_ws;
    (void)hipFuncSetAttribute((const void*)k_mlp, hipFuncAttributeMaxDynamicSharedMemorySize, MLP_LDS);
    k_prep<<<NB_PREP, 256, PREP_LDS, stream>>>(x, (const u16*)d_in[1], (const u16*)d_in[2], (const u16*)d_in[3],
                                               (const u16*)d_in[4], (const u16*)d_in[5], (const u16*)d_in[9],
                                               (const u16*)d_in[10], (const u16*)d_in[11], ws);
    k_conv<<<NB_PF + NB_CONV + NB_W1, 256, CONV_LDS, stream>>>((const u16*)d_in[6], (const u16*)d_in[8], ws);
    k_mlp<<<256, 256, MLP_LDS, stream>>>(ws, (u16*)d_out);
}
```

```cpp
#include <hip/hip_runtime.h>
#include <stdint.h>

typedef unsigned short u16;
typedef unsigned int u32;
typedef float f32x2 __attribute__((ext_vector_type(2)));
typedef float f32x4 __attribute__((ext_vector_type(4)));
typedef float f32x16 __attribute__((ext_vector_type(16)));
typedef _Float16 f16x2 __attribute__((ext_vector_type(2)));
typedef _Float16 f16x8 __attribute__((ext_vector_type(8)));
typedef __bf16 bf16x2 __attribute__((ext_vector_type(2)));
typedef __bf16 bf16x8 __attribute__((ext_vector_type(8)));
typedef short s16x4 __attribute__((ext_vector_type(4)));
typedef u32 u32x2 __attribute__((ext_vector_type(2)));
typedef u32 u32x4 __attribute__((ext_vector_type(4)));
#define LAS __attribute__((address_space(3)))
#define DI __device__ __forceinline__

constexpr int SEQ = 2048, DM = 576, NS = 128, FF = 4608;

constexpr u32 WS_MOD   = 0;
constexpr u32 WS_STAT  = 16384;
constexpr u32 WS_KSC   = 32768;
constexpr u32 WS_FLAG  = 36864;
constexpr u32 WS_EX    = 65536;
constexpr u32 WS_XT    = 1u << 20;
constexpr u32 WS_KR    = WS_XT + 2359296;
constexpr u32 WS_X1F   = WS_KR + 2359296;
constexpr u32 WS_WF    = WS_X1F + 2359296;
constexpr u32 WS_HF    = WS_WF + 10616832;
constexpr u32 WS_PART  = WS_HF + 18874368;
constexpr u32 WS_PH    = WS_PART + 6291456;
constexpr u32 WS_END   = WS_PH + 1048576;

DI float bflo(u32 u) { return __builtin_bit_cast(float, u << 16); }
DI float bfhi(u32 u) { return __builtin_bit_cast(float, u & 0xffff0000u); }
DI float bf2f(u16 v) { return __builtin_bit_cast(float, ((u32)v) << 16); }
DI u32 pkbf(float a, float b) { f32x2 v = {a, b}; return __builtin_bit_cast(u32, __builtin_convertvector(v, bf16x2)); }
DI u32 pkhf(float a, float b) { f32x2 v = {a, b}; return __builtin_bit_cast(u32, __builtin_convertvector(v, f16x2)); }
DI float rbf(float a) { return bflo(pkbf(a, a)); }
template <int CTRL, int RM = 0xf> DI float dppf(float v) {
    return __builtin_bit_cast(float, __builtin_amdgcn_update_dpp(0, __builtin_bit_cast(int, v), CTRL, RM, 0xf, true));
}
DI u32x4 trread2(const char* smem, u32 off0, u32 off1) {
    const u32x2 l = __builtin_bit_cast(u32x2, __builtin_amdgcn_ds_read_tr16_b64_v4i16((LAS s16x4*)(smem + off0)));
    const u32x2 h = __builtin_bit_cast(u32x2, __builtin_amdgcn_ds_read_tr16_b64_v4i16((LAS s16x4*)(smem + off1)));
    return (u32x4){l[0], l[1], h[0], h[1]};
}
DI void st16(char* ws, u32 off, u32x4 v) {
    __builtin_amdgcn_raw_buffer_store_b128(v, __builtin_amdgcn_make_buffer_rsrc(ws, 0, WS_END, 0x00020000), off, 0, 16);
}
DI void st4(char* ws, u32 off, u32 v) {
    __builtin_amdgcn_raw_buffer_store_b32(v, __builtin_amdgcn_make_buffer_rsrc(ws, 0, WS_END, 0x00020000), off, 0, 16);
}
DI float pow2i(int e) { e = e < -126 ? -126 : (e > 127 ? 127 : e); return __builtin_bit_cast(float, (u32)(e + 127) << 23); }
DI int expo(float x) { return (int)((__builtin_bit_cast(u32, x) >> 23) & 255u) - 127; }
DI u32 imgrow(u32 row) { return row * 1184u + (row >> 3) * 128u; }
DI f32x2 cmul(f32x2 z, f32x2 w, f32x2 wp) {
    const f32x2 zx = {z[0], z[0]}, zy = {z[1], z[1]};
    return __builtin_elementwise_fma(zy, wp, zx * w);
}
#define CODE_PREFETCH(sym, part) do { u32 lo_, hi_, len_; \
    asm volatile("s_getpc_b64 s[20:21]\n\ts_add_u32 s20, s20, " #sym "@rel32@lo+4\n\ts_addc_u32 s21, s21, " #sym "@rel32@hi+12\n\t" \
                 "s_mov_b32 %0, s20\n\ts_mov_b32 %1, s21\n\ts_mov_b32 %2, " #sym "_end-" #sym : "=s"(lo_), "=s"(hi_), "=s"(len_) :: "scc", "s20", "s21"); \
    const char* p_ = (const char*)(((unsigned long long)hi_ << 32) | lo_); \
    const u32 o_ = (threadIdx.x + (part) * 256u) * 128u; \
    if (o_ < len_) { u32 t_; asm volatile("global_load_dword %0, %1, off\n\ts_waitcnt vmcnt(0)" : "=v"(t_) : "v"(p_ + o_) : "memory"); } \
    } while (0)

#define DELAY1 20
#define NAV 26
constexpr int NA = NAV, NB = 36 - NA;
constexpr int NB_PF = 16, NB_LN = 64, NB_GEMV = 108, NB_KGEN = 576, NB_W2 = 144;
constexpr int NB_PREP = NB_PF + NB_LN + NB_GEMV + NB_KGEN + NB_W2;
constexpr int PREP_LDS = 38400;

extern "C" __global__ __launch_bounds__(256, 4) void k_prep(const u16* __restrict__ x, const u16* __restrict__ logA,
                                              const u16* __restrict__ Aim, const u16* __restrict__ Cre,
                                              const u16* __restrict__ Cim, const u16* __restrict__ logdt,
                                              const u16* __restrict__ w2, const u16* __restrict__ cvec,
                                              const u16* __restrict__ wmod, char* __restrict__ ws) {
    extern __shared__ __attribute__((aligned(16))) char smem[];
    const int tid = threadIdx.x, lane = tid & 63;
    const int wv = __builtin_amdgcn_readfirstlane(tid >> 6);
    int b = blockIdx.x;
    if (b >= NB_PF && b < NB_PF + NB_KGEN + NB_LN + NB_GEMV) b = b < NB_PF + NB_KGEN ? b + NB_LN + NB_GEMV : b - NB_KGEN;
    if (b < NB_PF) {
        if (b < 8) CODE_PREFETCH(k_prep, 0); else CODE_PREFETCH(k_conv, 0);
        if (b < 2) st16(ws, WS_FLAG + b * 4096 + tid * 16, (u32x4){0u, 0u, 0u, 0u});
    } else if ((b -= NB_PF) < NB_LN) {
        const int t0 = b * 32, tok = tid >> 3, sub = tid & 7;
        u32x4 v[9];
#pragma unroll
        for (int i = 0; i < 9; ++i) v[i] = *(const u32x4*)(x + (size_t)(t0 + tok) * DM + (sub + 8 * i) * 8);
#pragma unroll
        for (int i = 0; i < 9; ++i) *(u32x4*)(smem + imgrow(tok) + (sub + 8 * i) * 16) = v[i];
        __syncthreads();
        const int g = lane >> 4, qq = (lane & 15) >> 2, p = lane & 3;
#pragma unroll
        for (int i = 0; i < 9; ++i) {
            const int cb = wv * 9 + i;
            st16(ws, WS_XT + b * 36864 + cb * 1024 + (lane & 15) * 64 + g * 16,
                 trread2(smem, imgrow(8 * g + qq) + cb * 32 + p * 8, imgrow(8 * g + 4 + qq) + cb * 32 + p * 8));
        }
        float s = 0.f;
#pragma unroll
        for (int i = 0; i < 9; ++i)
#pragma unroll
            for (int k = 0; k < 4; ++k) s += bflo(v[i][k]) + bfhi(v[i][k]);
        s += dppf<0xB1>(s); s += dppf<0x4E>(s); s += dppf<0x141>(s);
        const float mean = s * (1.f / DM);
        float q = 0.f;
#pragma unroll
        for (int i = 0; i < 9; ++i)
#pragma unroll
            for (int k = 0; k < 4; ++k) {
                const float a = bflo(v[i][k]) - mean, c = bfhi(v[i][k]) - mean;
                q = fmaf(a, a, q); q = fmaf(c, c, q);
            }
        q += dppf<0xB1>(q); q += dppf<0x4E>(q); q += dppf<0x141>(q);
        if (sub == 0) st4(ws, WS_STAT + (t0 + tok) * 4, pkbf(mean, __builtin_amdgcn_sqrtf(rbf(rbf(q * (1.f / DM)) + 1e-5f))));
    } else if ((b -= NB_LN) < NB_GEMV) {
        const int seg = tid & 3, rg = tid >> 2;
        u32x4 v[9]; u16 cv[9];
#pragma unroll
        for (int i = 0; i < 9; ++i) {
            const int row = rg + 64 * i;
            cv[i] = cvec[row];
            v[i] = *(const u32x4*)(wmod + (size_t)row * (6 * DM) + b * 32 + seg * 8);
        }
        float acc[8];
#pragma unroll
        for (int k = 0; k < 8; ++k) acc[k] = 0.f;
#pragma unroll
        for (int i = 0; i < 9; ++i) {
            const float c = bf2f(cv[i]);
            const float sl = rbf(c * __builtin_amdgcn_rcpf(1.f + __builtin_amdgcn_exp2f(c * -1.44269504088896340736f)));
#pragma unroll
            for (int k = 0; k < 4; ++k) {
                acc[2 * k] = fmaf(sl, bflo(v[i][k]), acc[2 * k]);
                acc[2 * k + 1] = fmaf(sl, bfhi(v[i][k]), acc[2 * k + 1]);
            }
        }
#pragma unroll
        for (int k = 0; k < 8; ++k) { acc[k] += dppf<0x114>(acc[k]); acc[k] += dppf<0x118>(acc[k]); }
        float* red = (float*)smem;
        if ((lane & 15) >= 12) {
            *(f32x4*)(red + (tid >> 4) * 32 + seg * 8) = (f32x4){acc[0], acc[1], acc[2], acc[3]};
            *(f32x4*)(red + (tid >> 4) * 32 + seg * 8 + 4) = (f32x4){acc[4], acc[5], acc[6], acc[7]};
        }
        __syncthreads();
        if (tid < 32) {
            float t = 0.f;
#pragma unroll
            for (int i = 0; i < 16; ++i) t += red[i * 32 + tid];
            st4(ws, WS_MOD + (b * 32 + tid) * 4, __builtin_bit_cast(u32, rbf(t)));
        }
    } else if ((b -= NB_GEMV) < NB_KGEN) {
        const int d = b, hn = wv & 1, grp = wv >> 1;
        const int n = hn * 64 + lane, n2 = n ^ 64;
        const u16 la_ = logA[d * NS + n], ai_ = Aim[d * NS + n], cr_ = Cre[d * NS + n], ci_ = Cim[d * NS + n];
        const u16 cr2_ = Cre[d * NS + n2], ci2_ = Cim[d * NS + n2], ld_ = logdt[d];
        u32 ta[8];
#pragma unroll
        for (int j = 0; j < 8; j += 2) {
            const float l0 = (float)(2047 - tid * 8 - j);
            const u32 pk = pkbf(l0, l0 - 1.f);
            const float p0 = bflo(pk), p1 = bfhi(pk);
            ta[j] = (u32)fminf(4.f * p0, p0 + 1536.f); ta[j + 1] = (u32)fminf(4.f * p1, p1 + 1536.f);
        }
        asm volatile("" : "+v"(ta[0]), "+v"(ta[1]), "+v"(ta[2]), "+v"(ta[3]), "+v"(ta[4]), "+v"(ta[5]), "+v"(ta[6]), "+v"(ta[7]));
        const float cr = bf2f(cr_), ci = bf2f(ci_);
        const float dt = rbf(__builtin_amdgcn_exp2f(bf2f(ld_) * 1.44269504088896340736f));
        const float zr = -rbf(__builtin_amdgcn_exp2f(bf2f(la_) * 1.44269504088896340736f)) * dt, zi = bf2f(ai_) * dt;
        float bs = fabsf(cr) + fabsf(ci) + fabsf(bf2f(cr2_)) + fabsf(bf2f(ci2_));
        bs += dppf<0xB1>(bs); bs += dppf<0x4E>(bs); bs += dppf<0x141>(bs); bs += dppf<0x140>(bs);
        bs += dppf<0x142, 0xa>(bs); bs += dppf<0x143, 0xc>(bs);
        bs = __builtin_bit_cast(float, __builtin_amdgcn_readlane(__builtin_bit_cast(int, bs), 63));
        const int se = 14 - expo(2.f * dt * bs * 1.0001f);
        const float scale = pow2i(se > 100 ? 100 : se);
        const float zr2 = zr * 1.44269504088896340736f;
        auto cexp = [&](float p) -> f32x2 {
            const float mag = __builtin_amdgcn_exp2f(zr2 * p);
            const float ang = zi * p;
            const float rr = __builtin_rintf(ang * 0.15915494309189535f);
            float fr = fmaf(ang, 0.15915494309189535f, -rr);
            fr = fmaf(ang, 6.4206382e-9f, fr);
            return (f32x2){mag * __builtin_amdgcn_cosf(fr), mag * __builtin_amdgcn_sinf(fr)};
        };
        const float p1 = grp ? 4.f : 1.f;
        const f32x2 f1 = cexp(p1), c1 = cexp(16.f * p1), s0 = cexp(grp ? 512.f : 0.f), s1 = cexp(256.f * p1);
        const f32x2 fw = {f1[0], -f1[1]}, fwp = {f1[1], f1[0]};
        const f32x2 cwp = {-c1[1], c1[0]};
        const float es = 2.f * dt * scale;
        f32x2 za = cmul((f32x2){es * cr, es * ci}, s0, (f32x2){-s0[1], s0[0]});
        f32x2 zb = cmul(za, s1, (f32x2){-s1[1], s1[0]});
        f32x2 zf = {1.f, 0.f};
        char* tab = smem + wv * 8704;
        u32 keep[16];
#pragma unroll
        for (int k = 0; k < 16; ++k) {
            *(u32*)(tab + k * 272 + lane * 4) = pkhf(zf[0], zf[1]);
            *(u32*)(tab + (16 + k) * 272 + lane * 4) = pkhf(za[0], za[1]);
            keep[k] = pkhf(zb[0], zb[1]);
            zf = cmul(zf, fw, fwp); za = cmul(za, c1, cwp); zb = cmul(zb, c1, cwp);
        }
        const int r16 = lane & 15, g = lane >> 4;
        f16x8 fb[4];
        f32x4 acc0 = {0.f, 0.f, 0.f, 0.f}, acc1 = {0.f, 0.f, 0.f, 0.f};
        __builtin_amdgcn_fence(__ATOMIC_RELEASE, "wavefront");
        __builtin_amdgcn_wave_barrier();
#pragma unroll
        for (int ks = 0; ks < 4; ++ks) {
            fb[ks] = *(const f16x8*)(tab + r16 * 272 + ks * 64 + g * 16);
            const f16x8 fa = *(const f16x8*)(tab + (16 + r16) * 272 + ks * 64 + g * 16);
            acc0 = __builtin_amdgcn_mfma_f32_16x16x32_f16(fb[ks], fa, acc0, 0, 0, 0);
        }
        __builtin_amdgcn_fence(__ATOMIC_RELEASE, "wavefront");
        __builtin_amdgcn_wave_barrier();
#pragma unroll
        for (int k = 0; k < 16; ++k) *(u32*)(tab + (16 + k) * 272 + lane * 4) = keep[k];
        __builtin_amdgcn_fence(__ATOMIC_RELEASE, "wavefront");
        __builtin_amdgcn_wave_barrier();
#pragma unroll
        for (int ks = 0; ks < 4; ++ks) {
            const f16x8 fa = *(const f16x8*)(tab + (16 + r16) * 272 + ks * 64 + g * 16);
            acc1 = __builtin_amdgcn_mfma_f32_16x16x32_f16(fb[ks], fa, acc1, 0, 0, 0);
        }
        __builtin_amdgcn_fence(__ATOMIC_RELEASE, "wavefront");
        __builtin_amdgcn_wave_barrier();
        *(f32x4*)(tab + r16 * 64 + g * 16) = acc0;
        *(f32x4*)(tab + 1024 + r16 * 64 + g * 16) = acc1;
        __syncthreads();
        float kv[8];
#pragma unroll
        for (int j = 0; j < 8; ++j) {
            const u32 o = ta[j] + (ta[j] >= 2048u ? 2u * 8704u - 2048u : 0u);
            kv[j] = *(const float*)(smem + o) + *(const float*)(smem + o + 8704);
        }
        st16(ws, WS_KR + d * 4096 + tid * 16, (u32x4){pkhf(kv[0], kv[1]), pkhf(kv[2], kv[3]), pkhf(kv[4], kv[5]), pkhf(kv[6], kv[7])});
        if (tid == 0) st4(ws, WS_KSC + d * 4, __builtin_bit_cast(u32, pow2i(se > 100 ? -100 : -se)));
    } else {
        b -= NB_KGEN;
        const u16* src = w2 + (size_t)b * 32 * DM;
        if (DELAY1 > 0) __builtin_amdgcn_s_sleep(DELAY1);
        const int tok = tid >> 3, sub = tid & 7;
        u32x4 v[9];
#pragma unroll
        for (int i = 0; i < 9; ++i) v[i] = *(const u32x4*)(src + tok * DM + (sub + 8 * i) * 8);
#pragma unroll
        for (int i = 0; i < 9; ++i) *(u32x4*)(smem + imgrow(tok) + (sub + 8 * i) * 16) = v[i];
        __syncthreads();
        const int kq = b / 36, kk = b - kq * 36, sp = kk / 9, c = kk - sp * 9;
        const int rel = (sp - wv) & 3;
        const int e = rel == 0 ? c : 9 + c * 3 + (rel - 1);
        const int r0 = 4 * (lane >> 4) + ((lane & 15) >> 2), p = lane & 3;
#pragma unroll
        for (int i = 0; i < 9; ++i) {
            const int sg = (i + 1) >> 1, j = i == 0 ? 0 : (i - 1) & 1;
            const int T = 9 * wv + (sg == 0 ? 8 : 2 * ((kq + sg) & 3) + j);
            const int m = sg == 4 ? 252 + 2 * e + j
                        : e < NA ? 7 * e + i
                        : sg == 0 ? 7 * NA + 2 * NB + (e - NA) : 7 * NA + (sg == 1 ? 0 : (2 * sg - 1) * NB) + (e - NA) * 2 + j;
            const u32 col = T * 32 + p * 8;
            st16(ws, WS_WF + ((kq * 4 + wv) * 648 + 324 + m) * 1024 + lane * 16, trread2(smem, imgrow(r0) + col, imgrow(r0 + 16) + col));
        }
    }
    asm volatile("k_prep_end:");
}

constexpr int NB_CONV = 576, NB_W1 = 144;
constexpr int CONV_LDS = 36864 + 4096;
constexpr int UCP = 4640, KB_OFF = 4 * UCP;

extern "C" __global__ __launch_bounds__(256, 4) void k_conv(const u16* __restrict__ dskip, const u16* __restrict__ w1,
                                              char* __restrict__ ws) {
    extern __shared__ __attribute__((aligned(16))) char smem[];
    const int tid = threadIdx.x, lane = tid & 63;
    const int wv = __builtin_amdgcn_readfirstlane(tid >> 6);
    const float* mod = (const float*)(ws + WS_MOD);
    int b = blockIdx.x;
    if (b >= NB_PF) b = b < NB_PF + NB_CONV ? b + NB_W1 : b - NB_CONV;
    if (b < NB_PF) {
        CODE_PREFETCH(k_mlp, b >> 3);
    } else if ((b -= NB_PF) < NB_W1) {
        b = (b & 7) * 18 + (b >> 3);
        const int q = b / 9, c = b - q * 9, f0 = b * 32;
        const int seg = tid & 3, rg = tid >> 2;
        u32x4 v[9]; float sb[9], cb[9];
#pragma unroll
        for (int i = 0; i < 9; ++i) {
            const int row = rg + 64 * i;
            v[i] = *(const u32x4*)(w1 + (size_t)row * FF + f0 + seg * 8);
            sb[i] = mod[3 * DM + row]; cb[i] = mod[4 * DM + row];
        }
        float S[8], B[8];
#pragma unroll
        for (int k = 0; k < 8; ++k) { S[k] = 0.f; B[k] = 0.f; }
#pragma unroll
        for (int i = 0; i < 9; ++i) {
            const float c1 = rbf(1.f + cb[i]);
            u32x4 o;
#pragma unroll
            for (int k = 0; k < 4; ++k) {
                const float lo = bflo(v[i][k]), hi = bfhi(v[i][k]);
                o[k] = pkbf(lo * c1, hi * c1);
                S[2 * k] += bflo(o[k]); S[2 * k + 1] += bfhi(o[k]);
                B[2 * k] = fmaf(sb[i], lo, B[2 * k]); B[2 * k + 1] = fmaf(sb[i], hi, B[2 * k + 1]);
            }
            *(u32x4*)(smem + ((tid ^ ((tid >> 3) & 2)) + 256 * i) * 16) = o;
        }
#pragma unroll
        for (int k = 0; k < 8; ++k) {
            S[k] += dppf<0x114>(S[k]); S[k] += dppf<0x118>(S[k]);
            B[k] += dppf<0x114>(B[k]); B[k] += dppf<0x118>(B[k]);
        }
        float* red = (float*)(smem + 36864);
        if ((lane & 15) >= 12) {
            float* r = red + (tid >> 4) * 64 + seg * 8;
            *(f32x4*)(r) = (f32x4){S[0], S[1], S[2], S[3]}; *(f32x4*)(r + 4) = (f32x4){S[4], S[5], S[6], S[7]};
            *(f32x4*)(r + 32) = (f32x4){B[0], B[1], B[2], B[3]}; *(f32x4*)(r + 36) = (f32x4){B[4], B[5], B[6], B[7]};
        }
        __syncthreads();
        const int g = lane >> 4, qq = (lane & 15) >> 2, p = lane & 3;
#pragma unroll
        for (int i = 0; i < 9; ++i) {
            const int s = wv * 9 + i;
            const u32 a = (32 * (s >> 1) + 4 * g + qq) * 64 + (((s & 1) ^ (g & 1)) * 32) + p * 8;
            st16(ws, WS_WF + (q * 648 + c * 36 + s) * 1024 + lane * 16, trread2(smem, a, a + 1024));
        }
        if (wv == 0) {
            float t = 0.f;
#pragma unroll
            for (int i = 0; i < 16; ++i) t += red[i * 64 + lane];
            const float B_ = __builtin_bit_cast(float, __builtin_amdgcn_ds_bpermute((lane ^ 32) * 4, __builtin_bit_cast(int, t)));
            const float S_ = -t;
            const float s1 = rbf(S_), s2 = rbf(S_ - s1), s3 = S_ - s1 - s2;
            const float b1 = rbf(B_), b2 = rbf(B_ - b1), b3 = B_ - b1 - b2;
            if (lane < 32) st16(ws, WS_EX + q * 8192 + (c * 32 + lane) * 16, (u32x4){pkbf(s1, s2), pkbf(s3, b1), pkbf(b2, b3), 0u});
        } else if (wv == 1 && c == 0 && lane < 48) {
            const int d = (q & 3) * 144 + (lane < 32 ? (q >> 2) * 32 + lane : 96 + lane);
            st4(ws, WS_EX + q * 8192 + 4608 + lane * 4, __builtin_bit_cast(u32, mod[5 * DM + d]));
        }
    } else {
        const int d = b - NB_W1;
        const int tp = tid > 0 ? tid * 8 - 4 : 0;
        const char* xb = ws + WS_XT + d * 64;
        const u32x4 x0 = *(const u32x4*)(xb + (tid >> 2) * 36864 + (tid & 3) * 16);
        const u32x2 xm = *(const u32x2*)(xb + (tp >> 5) * 36864 + (tp & 31) * 2);
        u32x4 st[3];
        st[0] = *(const u32x4*)(ws + WS_STAT + tp * 4);
        st[1] = *(const u32x4*)(ws + WS_STAT + tid * 32);
        st[2] = *(const u32x4*)(ws + WS_STAT + tid * 32 + 16);
        const u32x4 kv = *(const u32x4*)(ws + WS_KR + d * 4096 + tid * 16);
        const float sa = mod[d], ca = mod[DM + d], ga = mod[2 * DM + d];
        const float dsk = bf2f(dskip[d]);
        const float kinv = ((const float*)(ws + WS_KSC))[d];
        {
            const u32x4 z = {0u, 0u, 0u, 0u};
            if (tid < 96) *(u32x4*)(smem + (tid / 24) * UCP + (tid % 24) * 16) = z;
            else if (tid < 104) *(u32x4*)(smem + KB_OFF + 4096 + (tid - 96) * 16) = z;
        }
        const float c1 = rbf(1.f + ca);
        const int he = expo(64.f * fabsf(c1) + 2.f * fabsf(sa)) - 14;
        const float hs = pow2i(-he), oscale = pow2i(he) * kinv;
        const u32 xw[6] = {xm[0], xm[1], x0[0], x0[1], x0[2], x0[3]};
        u32 P[6], hb[4];
#pragma unroll
        for (int k = 0; k < 6; ++k) {
            const u32 s0 = st[k >> 1][(k & 1) * 2], s1 = st[k >> 1][(k & 1) * 2 + 1];
            const float n0 = (bflo(xw[k]) - bflo(s0)) * __builtin_amdgcn_rcpf(bfhi(s0));
            const float n1 = (bfhi(xw[k]) - bflo(s1)) * __builtin_amdgcn_rcpf(bfhi(s1));
            const u32 t = pkbf(fmaf(n0, c1, sa), fmaf(n1, c1, sa));
            if (k >= 2) hb[k - 2] = t;
            P[k] = pkhf(bflo(t) * hs, bfhi(t) * hs);
        }
        if (tid == 0) { P[0] = 0u; P[1] = 0u; }
        u32 Q[5];
#pragma unroll
        for (int k = 0; k < 5; ++k) Q[k] = __builtin_amdgcn_alignbit(P[k + 1], P[k], 16);
        {
            char* ub = smem + 384 + tid * 16;
            *(u32x4*)(ub) = (u32x4){Q[0], Q[1], Q[2], Q[3]};
            *(u32x4*)(ub + UCP) = (u32x4){P[1], P[2], P[3], P[4]};
            *(u32x4*)(ub + 2 * UCP) = (u32x4){Q[1], Q[2], Q[3], Q[4]};
            *(u32x4*)(ub + 3 * UCP) = (u32x4){P[2], P[3], P[4], P[5]};
            *(u32x4*)(smem + KB_OFF + tid * 16) = kv;
        }
        __syncthreads();
        const int c = lane & 15, g = lane >> 4;
        f32x4 acc[8];
#pragma unroll
        for (int j = 0; j < 8; ++j) acc[j] = (f32x4){0.f, 0.f, 0.f, 0.f};
        {
            const u32 lb = (u32)(uintptr_t)(LAS char*)smem;
            const u32 ka = lb + KB_OFF + 2 * (2076 - 4 * c + 8 * g - 64 * wv) - 3584 - 64;
            const u32 ua = lb + (c & 3) * UCP + 2 * (8 * g + 64 * (c >> 2) - 64 * wv + 192);
            u32x2 kl[2][8], kh[2][8]; u32x4 uf[2][8];
            auto rd = [&](int rr, int n) {
                asm volatile("ds_read_b64 %0, %3 offset:%5\n\tds_read_b64 %1, %3 offset:%6\n\tds_read_b128 %2, %4 offset:%7"
                             : "=&v"(kl[rr][n]), "=&v"(kh[rr][n]), "=&v"(uf[rr][n]) : "v"(ka), "v"(ua),
                               "n"((7 - n) * 512 + (1 - rr) * 64), "n"((7 - n) * 512 + (1 - rr) * 64 + 8), "n"(n * 512 + (1 - rr) * 64) : "memory");
            };
            auto mm = [&](int rr, int i, int dl) {
                const u32x4 k = {kl[rr][i][0], kl[rr][i][1], kh[rr][i][0], kh[rr][i][1]};
                acc[i + dl] = __builtin_amdgcn_mfma_f32_16x16x32_f16(__builtin_bit_cast(f16x8, uf[rr][dl]), __builtin_bit_cast(f16x8, k), acc[i + dl], 0, 0, 0);
            };
            auto shell = [&](int rr, int n) {
#pragma unroll
                for (int o = 0; o <= n && o + n < 8; ++o) { mm(rr, n, o); if (o < n) mm(rr, o, n); }
            };
            auto pin = [&](int rr, int n) { asm volatile("" : "+v"(kl[rr][n]), "+v"(kh[rr][n]), "+v"(uf[rr][n])); };
            rd(0, 0); rd(0, 7); rd(0, 1); rd(0, 6); rd(0, 2); rd(0, 5); rd(0, 3); rd(0, 4);
#pragma unroll
            for (int n = 7; n >= 0; --n) {
                if (n >= 4) { asm volatile("s_waitcnt lgkmcnt(%0)" :: "n"(n == 7 ? 15 : 6 * (n - 4) + 3 * (7 - n)) : "memory"); pin(0, n); pin(0, 7 - n); }
                shell(0, n);
                __builtin_amdgcn_sched_barrier(0);
                rd(1, 7 - n);
            }
#pragma unroll
            for (int n = 0; n < 8; ++n) {
                asm volatile("s_waitcnt lgkmcnt(%0)" :: "n"(n < 3 ? 15 : 3 * (7 - n)) : "memory");
                pin(1, n);
                shell(1, n);
                __builtin_amdgcn_sched_barrier(0);
            }
        }
        __syncthreads();
#pragma unroll
        for (int j = 0; j < 8; ++j) *(f32x4*)(smem + wv * 8192 + j * 1024 + lane * 16) = acc[j];
        __syncthreads();
        f32x4 y0 = *(const f32x4*)(smem + tid * 32), y1 = *(const f32x4*)(smem + tid * 32 + 16);
#pragma unroll
        for (int w = 1; w < 4; ++w) {
            y0 += *(const f32x4*)(smem + w * 8192 + tid * 32);
            y1 += *(const f32x4*)(smem + w * 8192 + tid * 32 + 16);
        }
        const float y[8] = {y0[0], y0[1], y0[2], y0[3], y1[0], y1[1], y1[2], y1[3]};
        u32x4 o;
#pragma unroll
        for (int k = 0; k < 4; ++k) {
            const u32 yb = pkbf(y[2 * k] * oscale, y[2 * k + 1] * oscale);
            const u32 sk = pkbf(bflo(hb[k]) * dsk, bfhi(hb[k]) * dsk);
            const u32 z = pkbf(bflo(yb) + bflo(sk), bfhi(yb) + bfhi(sk));
            const u32 gz = pkbf(bflo(z) * ga, bfhi(z) * ga);
            o[k] = pkbf(bflo(x0[k]) + bflo(gz), bfhi(x0[k]) + bfhi(gz));
        }
        st16(ws, WS_X1F + (tid >> 4) * 147456 + ((d / 48) * 12 + ((tid >> 2) & 3) * 3 + (d >> 4) % 3) * 1024 + (d & 15) * 64 + (tid & 3) * 16, o);
        if (wv == 0) asm volatile("s_call_b64 s[30:31], k_mlp_pad7\n\ts_call_b64 s[30:31], k_mlp_pad4" ::: "s30", "s31");
        if (wv == 1) asm volatile("s_call_b64 s[30:31], k_mlp_pad6\n\ts_call_b64 s[30:31], k_mlp_pad5" ::: "s30", "s31");
        if (wv == 2) asm volatile("s_call_b64 s[30:31], k_mlp_pad0\n\ts_call_b64 s[30:31], k_mlp_pad2" ::: "s30", "s31");
        if (wv == 3) asm volatile("s_call_b64 s[30:31], k_mlp_pad1\n\ts_call_b64 s[30:31], k_mlp_pad3" ::: "s30", "s31");
    }
    asm volatile("k_conv_end:");
}
constexpr u32 L_EXF = 147456;
constexpr int MLP_LDS = L_EXF + 12288;
constexpr int GE = 648;

#if defined(__HIP_DEVICE_COMPILE__)
#pragma clang attribute push(__attribute__((target("no-packed-fp32-ops"))), apply_to = function)
#endif
template <int N> struct IC { static constexpr int value = N; };
template <int A, int B, class F> DI void static_for(F&& f) {
    if constexpr (B - A == 1) f(IC<A>{});
    else if constexpr (A < B) { static_for<A, (A + B) / 2>(f); static_for<(A + B) / 2, B>(f); }
}
constexpr int imin(int a, int b) { return a < b ? a : b; }
constexpr int XW = 6;
#define XSP 7
#define XIV 8
constexpr int XI = XIV;
constexpr int x_dmas(int j) { return j <= 0 ? 0 : imin((4 * j + XSP - 1) / XSP, 3 * (19 - XI)); }
constexpr int G1 = 36;
constexpr int P1 = 36 * 5 + 7, P2 = 36 * 7 + 7, P3 = 400;
#define CD1 34
#define CD3 30
#define QW3 8
constexpr int C1 = P1 + CD1, C2 = P2 + CD1, C3 = P3 + CD3;
constexpr bool s_poll(int G) { return G == P1 || G == P2 || G == P3; }
#define PB2 606
#define PL4 640
constexpr int PUB2 = PB2, P4 = PL4;
constexpr int HS0 = 22, HS1 = 35;
constexpr int s_hstore(int G) { return G < 72 ? (G == 64 ? 0 : G == 71 ? 1 : -1) : G < 324 ? (G % 36 == HS0 ? 0 : G % 36 == HS1 ? 1 : -1) : G == 338 ? 0 : G == 346 ? 1 : -1; }
constexpr int B0 = 324 + 7 * NA;
constexpr int slot_end(int sg) { return B0 - 1 + (sg == 1 ? 2 : sg == 0 ? 3 : 2 * sg + 1) * NB; }
constexpr int s_pstore(int G) {
    for (int sg = 0; sg < 4; ++sg) { if (G == slot_end(sg) + 4) return 2 * sg; if (G == slot_end(sg) + 6 && sg > 0) return 2 * sg + 1; }
    return -1;
}
constexpr bool s_res(int G) { return G == 620 || G == 622 || G == 624; }
constexpr int s_pub(int G) { return G == 36 * 3 + 24 ? 2 : G == 36 * 5 + 24 ? 4 : G == 372 ? 9 : 0; }
constexpr int m_slot(int m) { return m < 7 * NA ? (m % 7 + 1) / 2 : m < 7 * NA + 2 * NB ? 1 : m < 7 * NA + 3 * NB ? 0 : m < 252 ? 2 + (m - 7 * NA - 3 * NB) / (2 * NB) : 4; }
constexpr int m_ent(int m) { return m < 7 * NA ? m / 7 : m < 7 * NA + 2 * NB ? NA + (m - 7 * NA) / 2 : m < 7 * NA + 3 * NB ? NA + (m - 7 * NA - 2 * NB)
                                  : m < 252 ? NA + ((m - 7 * NA - 3 * NB) % (2 * NB)) / 2 : (m - 252) / 2; }
constexpr int m_half(int m) { return m < 7 * NA ? (m % 7 == 0 ? 0 : (m % 7 - 1) & 1) : m < 7 * NA + 2 * NB ? (m - 7 * NA) & 1 : m < 7 * NA + 3 * NB ? 0
                                   : m < 252 ? (m - 7 * NA - 3 * NB) & 1 : m & 1; }
constexpr bool m_first(int m) { return m < 7 || m == 252 || m == 253; }
constexpr bool ent_first(int m) { return m < 7 * NA ? m % 7 == 0 : (m < 7 * NA + 2 * NB && ((m - 7 * NA) & 1) == 0); }
constexpr bool ent_own(int e) { return e < 9; }
constexpr int first_use(int e) { return e < NA ? 324 + 7 * e : B0 + 2 * (e - NA); }
constexpr bool s_ring(int G) { return G + 12 < GE && (G < 100 ? G % 12 == 0 : G < 490 ? G % 36 == 24 : G % 18 == 6); }
constexpr int ring_slot(int grp) { const int u = (grp / 3) & 3; return (u == 0 ? 0 : u == 1 ? 6 : u == 2 ? 9 : 3) + grp % 3; }
constexpr int need_bar(int grp) { int g = 12 * grp - 12; while (g > 0 && !s_ring(g)) --g; return g; }
struct Sched {
    int dn[GE];
    int dgap[162];
    int hl[GE];
    int pre[GE + 1];
    int hgap[36];
    bool ok;
    constexpr Sched() : dn(), dgap(), hl(), pre(), hgap(), ok(true) {
        int nd = 3 * XW, nh = 0, last2 = -99, lastbar = 24;
        for (int n = 0; n < 162; ++n) dgap[n] = -1;
        for (int e = 0; e < 36; ++e) hgap[e] = -1;
        for (int G = 0; G < GE; ++G) {
            dn[G] = -1; hl[G] = -1;
            int n = 0;
            if (G >= G1) {
                if (s_ring(G)) lastbar = G;
                const bool fixed = s_hstore(G) >= 0 || s_pstore(G) >= 0 || s_res(G);
                const bool quiet = (G >= P1 && G < P1 + 17) || (G >= P2 && G < P2 + 17) || (G >= P3 && G < P3 + QW3);
                const int sp2 = G < 324 ? 4 : 5;
                if (fixed) last2 = 2 * G;
                else if (!quiet && 2 * G - last2 >= sp2) {
                    int dd = 1 << 20, dh = 1 << 20;
                    if (nd < 162 && (nd / 3 < 12 || (nd / 3 - 11) * 12 <= lastbar + 6)) dd = need_bar(nd / 3) - 40;
                    const int lim = G > C3 ? 54 : G > C2 ? 24 : G > C1 ? 12 : 0;
                    if (nh < lim) dh = first_use(9 + nh / 2) - 45;
                    if (dd < (1 << 20) || dh < (1 << 20)) {
                        if (dd <= dh) { dn[G] = nd; dgap[nd] = G; if (need_bar(nd / 3) <= G) ok = false; ++nd; }
                        else { hl[G] = 18 + nh; hgap[9 + nh / 2] = G; if (first_use(9 + nh / 2) <= G) ok = false; ++nh; }
                        last2 = last2 + sp2 > 2 * G - 1 ? last2 + sp2 : 2 * G - (G < 324 ? 0 : 1);
                        n = 1;
                    }
                }
                n += fixed + s_poll(G) + (G == P4) + (s_pub(G) > 0) + (G == PUB2);
            }
            pre[G + 1] = pre[G] + n;
        }
        if (nd != 162 || nh != 54) ok = false;
    }
};
constexpr Sched SCH{};
static_assert(SCH.ok, "schedule misses a deadline");
constexpr int younger(int a, int b) { return imin(SCH.pre[b] - SCH.pre[a + 1], 63); }
constexpr bool s_bar(int G) { return G >= G1 && (s_ring(G) || s_pub(G) > 0 || G == PUB2); }
constexpr int bar_wait(int G) {
    int n = 63;
    if (s_ring(G)) { int grp = 53; while (need_bar(grp) > G) --grp; n = G == 36 ? 3 : younger(SCH.dgap[3 * grp + 2], G); }
    if (G == PUB2) n = imin(n, younger(slot_end(3) + 6, G));
    if (s_pub(G) > 0) n = imin(n, younger(G == 372 ? 346 : 36 * s_pub(G) + HS1, G));
    return n;
}
constexpr int frag_lds(int f) { return ring_slot(f / 12) * 12288 + (f % 12) * 1024; }

constexpr u32 RESL = ring_slot(45) * 12288;
static_assert(SCH.dgap[161] < 620, "M0");
#define OAUX 0
#define PGX 56
#define PG0 95
#define PG1 192
#define PG2 285
#define PG3 388
#define PG4 517
#define PG5 641
#define PADS 1
constexpr int pad_at(int G) { return G == PG0 ? 0 : G == PG1 ? 1 : G == PG2 ? 2 : G == PG3 ? 3 : G == PG4 ? 4 : G == PG5 ? 5 : -1; }
#define SB() __builtin_amdgcn_sched_barrier(0)
#define MFMA "v_mfma_f32_16x16x32_bf16 "

extern "C" __global__ __attribute__((aligned(4096))) __launch_bounds__(256, 1) void k_mlp(char* __restrict__ ws, u16* __restrict__ out) {
    extern __shared__ __attribute__((aligned(16))) char smem[];
    asm volatile("s_branch 1f\nk_mlp_pad7:\n\ts_setpc_b64 s[30:31]\n1:");
    const int tid = threadIdx.x, lane = tid & 63;
    const int w = __builtin_amdgcn_readfirstlane(tid >> 6);
    const int b = blockIdx.x, xcd = b & 7, rr = b >> 3;
#define MAPV 1
    const int s = rr & 3;
    const int kq = MAPV == 0 ? xcd >> 1 : MAPV == 1 ? 2 * (xcd >> 2) + ((rr >> 2) & 1) : (rr >> 2) & 3;
    const int it = MAPV == 0 ? 8 * (xcd & 1) + (rr >> 2) : MAPV == 1 ? 4 * (xcd & 3) + (rr >> 3) : 2 * xcd + (rr >> 4);
    const int q = 4 * kq + s;
    const auto rs = __builtin_amdgcn_make_buffer_rsrc(ws, 0, WS_END, 0x00020000);
    const u32x4 srd = {(u32)(uintptr_t)ws, (u32)((uintptr_t)ws >> 32) & 0xffffu, WS_END, 0x00020000u};
    const u32 voff = lane * 16;
    const u32 lbase = (u32)(uintptr_t)(LAS char*)smem;
    const u32 wl = lbase + w * 3072;
    auto set_m0 = [&](u32 v) { asm volatile("s_mov_b32 m0, %0" :: "s"(v) : "memory"); };
    auto dma = [&](auto O, u32 so) {
        const u32 vo = voff; const u32x4 sr = srd;
        asm volatile("buffer_load_dwordx4 %0, %1, %2 offen offset:%3 lds" :: "v"(vo), "s"(sr), "s"(so), "n"(O.value * 1024) : "memory");
    };
    auto dma3 = [&](u32 dst, u32 so) {
        asm volatile("s_mov_b32 m0, %3\n\ts_nop 0\n\tbuffer_load_dwordx4 %0, %1, %2 offen lds\n\t"
                     "buffer_load_dwordx4 %0, %1, %2 offen offset:1024 lds\n\tbuffer_load_dwordx4 %0, %1, %2 offen offset:2048 lds"
                     :: "v"(voff), "s"(srd), "s"(so), "s"(dst) : "memory");
    };
    const u32 xsb = WS_X1F + it * 147456;
    const u32 wfb = WS_WF + q * 663552 + w * 3072;
    auto rq_dst = [&](int p) -> u32 {
        const int x = p <= 2 ? p - 1 : p <= 6 ? p - 2 : p - 3;
        return wl + (p == 0 ? 0 : p == 3 ? 12288 : p == 7 ? 24576 : p == 15 ? L_EXF : p > 15 ? ring_slot(p - 13) * 12288 : (3 + x % 9) * 12288);
    };
    auto rq_src = [&](int p) -> u32 {
        const int x = p <= 2 ? p - 1 : p <= 6 ? p - 2 : p - 3;
        return p == 0 ? wfb : p == 3 ? wfb + 12288 : p == 7 ? wfb + 24576 : p == 15 ? WS_EX + q * 8192 + w * 3072
             : p > 15 ? wfb + (p - 13) * 12288 : xsb + w * 3072 + x * 12288;
    };
#pragma unroll
    for (int p = 0; p < XI; ++p) {
        dma3(rq_dst(p), rq_src(p));
    }
    set_m0(rq_dst(XI));
    const u32 hfb = WS_HF + (it * 4 + kq) * 36 * 8192 + w * 2048;
    u32x4 Wf[18], Ef[2];
    const u32 rd0 = lbase + voff;
    const u32 rb[3] = {rd0, rd0 + 49152, rd0 + 98304};
    auto rdw = [&](auto F) {
        constexpr int lo = frag_lds(F.value);
        u32x4& d = Wf[F.value % 18]; const u32 base = rb[lo / 49152];
        asm volatile("ds_read_b128 %0, %1 offset:%2" : "=v"(d) : "v"(base), "n"(lo % 49152) : "memory");
    };
    const u32 rex = lbase + L_EXF + (lane & 15) * 16;
    auto rde = [&](auto C) {
        u32x4& d0 = Ef[0]; u32x4& d1 = Ef[1]; const u32 base = rex;
        asm volatile("ds_read_b128 %0, %2 offset:%3\n\tds_read_b128 %1, %2 offset:%4" : "=&v"(d0), "=&v"(d1) : "v"(base), "n"(C.value * 512), "n"(C.value * 512 + 256) : "memory");
    };
    f32x4 acc[2][4];

    u32x4 Xa[36];
    f32x4 gram[2], rsum[2];
    {
        const u32 a0 = wl + (4 * (lane >> 4) + ((lane & 15) >> 2)) * 64 + (lane & 3) * 8;
        u32x4 ones = {0x3f803f80u, 0x3f803f80u, 0x3f803f80u, 0x3f803f80u};
        asm volatile("" : "+v"(ones));
        u32x2 t[3][4];
        const u32 xb[3] = {a0 + 36864, a0 + 73728, a0 + 110592};
        auto xrd = [&](auto KS, auto I) {
            constexpr int hk = 2 * KS.value + (I.value >> 1), sl = (hk / 3) % 9;
            u32x2& d = t[KS.value % 3][I.value]; const u32 base = xb[sl / 3];
            asm volatile("ds_read_b64_tr_b16 %0, %1 offset:%2" : "=v"(d) : "v"(base), "n"((sl % 3) * 12288 + (hk % 3) * 1024 + (I.value & 1) * 32) : "memory");
        };
        auto admit = [&](auto KS, auto J) {
            constexpr int ks = KS.value, x = (2 * ks + 1) / 3, pos = x <= 1 ? x + 1 : x <= 4 ? x + 2 : x + 3;
            constexpr int issued = 3 * XI + x_dmas(J.value);
            static_assert(issued >= 3 * pos + 3, "X order");
            if constexpr (ks % 6 == 0) asm volatile("s_waitcnt vmcnt(%0)\n\ts_barrier" :: "n"(issued - 3 * pos - 3) : "memory");
            else if constexpr (ks % 3 != 2) asm volatile("s_waitcnt vmcnt(%0)" :: "n"(issued - 3 * pos - 3) : "memory");
        };
        static_for<0, 2>([&](auto KS) {
            admit(KS, IC<0>{});
            static_for<0, 4>([&](auto I) { xrd(KS, I); });
            rdw(IC<2 * KS.value>{}); rdw(IC<2 * KS.value + 1>{});
        });
        u32x4 xv[2];
        static_for<0, 72>([&](auto J) {
            constexpr int j = J.value, ks = j / 4, k = j % 4;
            if constexpr (PADS && j == PGX) asm volatile("s_branch 1f\n\t.p2align 12\nk_mlp_pad6:\n\ts_setpc_b64 s[30:31]\n1:");
            u32x2 (&d)[4] = t[ks % 3];
            u32x4& x0 = xv[0]; u32x4& x1 = xv[1]; u32x4& on = ones;
            if constexpr (k == 0) {
                asm volatile("s_waitcnt lgkmcnt(%4)" : "+v"(d[0]), "+v"(d[1]), "+v"(d[2]), "+v"(d[3]) : "n"(ks < 17 ? 6 : 0) : "memory");
                xv[0] = (u32x4){d[0][0], d[0][1], d[2][0], d[2][1]};
                xv[1] = (u32x4){d[1][0], d[1][1], d[3][0], d[3][1]};
                asm volatile("s_nop 1" : "+v"(x0), "+v"(x1));
            }
            f32x4& c0 = k < 2 ? acc[0][2 * (k & 1)] : k == 2 ? gram[0] : rsum[0];
            f32x4& c1 = k < 2 ? acc[0][2 * (k & 1) + 1] : k == 2 ? gram[1] : rsum[1];
            u32x4& am0 = k < 2 ? Wf[(2 * ks + k) % 18] : k == 2 ? x0 : on;
            u32x4& am1 = k < 2 ? Wf[(2 * ks + k) % 18] : k == 2 ? x1 : on;
            if constexpr (j < 4) asm volatile(MFMA "%0, %1, %2, 0" : "=&v"(c0) : "v"(am0), "v"(x0));
            else asm volatile(MFMA "%0, %1, %2, %0" : "+v"(c0) : "v"(am0), "v"(x0));
            SB();
            if constexpr (ks < 16) {
                if constexpr (k == 0) admit(IC<ks + 2>{}, J);
                if constexpr (k < 2) { xrd(IC<ks + 2>{}, IC<2 * k>{}); xrd(IC<ks + 2>{}, IC<2 * k + 1>{}); }
                else rdw(IC<2 * ks + 2 + k>{});
            }
            SB();
            if constexpr (j < 4) asm volatile(MFMA "%0, %1, %2, 0" : "=&v"(c1) : "v"(am1), "v"(x1));
            else asm volatile(MFMA "%0, %1, %2, %0" : "+v"(c1) : "v"(am1), "v"(x1));
            SB();
            if constexpr (k >= 2) { Xa[2 * ks + k - 2] = xv[k - 2]; asm volatile("" : "+a"(Xa[2 * ks + k - 2])); }
            if constexpr (x_dmas(j + 1) > x_dmas(j)) {
                constexpr int n = x_dmas(j), p = XI + n / 3;
                static_assert(p < 12 || p == 15 || (3 * (p < 15 ? p - 12 : p - 13) + 2) / 2 <= ks, "slot still holds unread x");
                dma(IC<n % 3>{}, rq_src(p));
                if constexpr (n % 3 == 2 && p < 18) set_m0(rq_dst(p + 1));
            }
            SB();
        });
    }
    float ysc[2];
    u32x4 Xe[2];
    {
        asm volatile("s_waitcnt vmcnt(6)\n\ts_barrier" ::: "memory");
        static_for<36, 48>(rdw);
        rde(IC<0>{});
        set_m0(wl + ring_slot(XW) * 12288);
        SB();
    }
    float sq[2], mean[2];
    auto stats = [&](auto ST) {
        constexpr int st = ST.value >> 1, t = ST.value & 1;
        if constexpr (st == 0) {
            const float a = (lane & 1) ? gram[t][1] : gram[t][0], c = (lane & 1) ? gram[t][3] : gram[t][2];
            float v = (lane & 2) ? c : a;
            const u32 src = ((lane & 15) + 4 * (lane & 12)) * 4;
            float& d = sq[t];
            asm volatile("ds_bpermute_b32 %0, %1, %2" : "=v"(d) : "v"(src), "v"(v) : "memory");
            mean[t] = rsum[t][0] * (1.f / DM);
            asm volatile("" : "+v"(mean[t]));
        }
        if constexpr (st == 1) { asm volatile("" : "+v"(sq[t])); sq[t] = rbf(rbf(fmaxf(fmaf(-mean[t], mean[t], sq[t] * (1.f / DM)), 0.f)) + 1e-5f); asm volatile("" : "+v"(sq[t])); }
        if constexpr (st == 2) { sq[t] = rbf(__builtin_amdgcn_sqrtf(sq[t])); ysc[t] = -1.44269504088896340736f * __builtin_amdgcn_rcpf(sq[t]); asm volatile("" : "+v"(sq[t]), "+v"(ysc[t])); }
        if constexpr (st == 3) {
            const bool lo = lane < 16;
            Xe[t] = (u32x4){lo ? pkbf(mean[t], mean[t]) : 0u, lo ? pkbf(mean[t], sq[t]) : 0u, lo ? pkbf(sq[t], sq[t]) : 0u, 0u};
            asm volatile("" : "+v"(Xe[t]));
        }
    };
    auto corr = [&](auto C) {
        f32x4 (&a)[4] = acc[C.value & 1]; u32x4 (&e)[2] = Ef; u32x4 (&x)[2] = Xe;
        asm volatile(MFMA "%0, %4, %6, %0\n\t" MFMA "%1, %4, %7, %1\n\t" MFMA "%2, %5, %6, %2\n\t" MFMA "%3, %5, %7, %3"
                     : "+v"(a[0]), "+v"(a[1]), "+v"(a[2]), "+v"(a[3]) : "v"(e[0]), "v"(e[1]), "v"(x[0]), "v"(x[1]));
    };

    f32x4 acc2[5][4];
    float yv[16], ev[16];
    u32x4 H[36][2], pollv;
    auto flag_store = [&](u32 level, u32 so) {
        asm volatile("s_mov_b64 exec, 1\n\tbuffer_store_dword %0, off, %1, %2 sc1\n\ts_mov_b64 exec, -1" :: "v"(level), "s"(srd), "s"(so) : "memory");
    };
    auto act = [&](auto C, auto HS, auto PT) {
        constexpr int t = HS.value / 2, e = C.value, pt = PT.value;
        if constexpr (HS.value % 2 == 0) {
            if constexpr (pt != 1 && t < 16) { yv[t] = acc[e & 1][2 * ((t >> 2) & 1) + (t >> 3)][t & 3] * ysc[t >> 3]; asm volatile("" :: "v"(yv[t])); }
            if constexpr (pt != 0 && t >= 2) { yv[t - 2] = yv[t - 2] * ev[t - 2]; asm volatile("" :: "v"(yv[t - 2])); }
            if constexpr (pt != 0 && t < 16) { ev[t] = __builtin_amdgcn_exp2f(yv[t]); asm volatile("" :: "v"(ev[t])); }
        } else {
            if constexpr (pt != 1 && t >= 1 && t < 17) { ev[t - 1] = 1.f + ev[t - 1]; asm volatile("" :: "v"(ev[t - 1])); }
            if constexpr (pt != 0 && t >= 3 && (t & 1)) {
                u32 pk = pkbf(yv[t - 3], yv[t - 2]);
                asm volatile("" :: "v"(pk));
                H[e][(t - 3) / 8][((t - 3) / 2) & 3] = pk;
            }
            if constexpr (pt != 0 && t >= 1 && t < 17) { ev[t - 1] = __builtin_amdgcn_rcpf(ev[t - 1]); asm volatile("" :: "v"(ev[t - 1])); }
        }
    };
    const u32 hown = hfb + 9 * s * 8192;
    const u32 hpeer[3] = {hfb + 9 * ((s + 1) & 3) * 8192, hfb + 9 * ((s + 2) & 3) * 8192, hfb + 9 * ((s + 3) & 3) * 8192};
    auto hload = [&](auto E, auto HF) {
        constexpr int e = E.value, hf = HF.value, k = e - 9;
        const u32 so = hpeer[k % 3] + (k / 3) * 8192, vo = voff; const u32x4 sr = srd;
        u32x4& d = H[e][hf];
        asm volatile("buffer_load_dwordx4 %0, %1, %2, %3 offen offset:%4 sc1" : "=a"(d) : "v"(vo), "s"(sr), "s"(so), "n"(hf * 1024) : "memory");
    };
    const u32 vzero = 0;
    const u32 fso1 = WS_FLAG + (it * 4 + kq) * 64;
    auto poll = [&]() {
        asm volatile("buffer_load_dwordx4 %0, %1, %2, %3 offen sc1" : "=v"(pollv) : "v"(vzero), "s"(srd), "s"(fso1) : "memory");
    };
    auto check = [&](u32 level) {
        asm volatile("" : "+v"(pollv));
#pragma nounroll
        for (u32 spins = 0; spins < (1u << 20); ++spins) {
            const bool bad = pollv[0] < level || pollv[1] < level || pollv[2] < level || pollv[3] < level;
            if (__builtin_amdgcn_ballot_w64(bad) == 0ull) break;
            __builtin_amdgcn_s_sleep(1);
            asm volatile("buffer_load_dwordx4 %0, %1, %2, %3 offen sc1\n\ts_waitcnt vmcnt(0)"
                         : "=v"(pollv) : "v"(vzero), "s"(srd), "s"(fso1) : "memory");
        }
    };
    const u32 pgb = WS_PART + (it * 4 + s) * 98304 + w * 2048;
    const u32 phb = WS_PH + (it * 4 + s) * 16384 + w * 4096;
    auto xfrag = [&](int ks) -> u32 { return xsb + ((ks / 3) * 12 + 3 * w + ks % 3) * 1024; };
    const u32 fso2 = WS_FLAG + 4096 + (it * 4 + s) * 64;
    u32 fv;
    const u32 vfl = (lane & 3) * 4;
    auto poll2 = [&]() {
        asm volatile("buffer_load_dword %0, %1, %2, %3 offen sc1" : "=v"(fv) : "v"(vfl), "s"(srd), "s"(fso2) : "memory");
    };
    auto check2 = [&]() {
        asm volatile("" : "+v"(fv));
#pragma nounroll
        for (u32 spins = 0; spins < (1u << 20); ++spins) {
            if (__builtin_amdgcn_ballot_w64(fv == 0u) == 0ull) break;
            __builtin_amdgcn_s_sleep(1);
            asm volatile("buffer_load_dword %0, %1, %2, %3 offen sc1\n\ts_waitcnt vmcnt(0)" : "=v"(fv) : "v"(vfl), "s"(srd), "s"(fso2) : "memory");
        }
    };
    static_for<G1, GE>([&](auto GG) {
        constexpr int G = GG.value, c = G / 36, g = G % 36, m = G - 324;
        if constexpr (PADS && pad_at(G) >= 0) asm volatile("s_branch 1f\n\t.p2align 12\nk_mlp_pad%c0:\n\ts_setpc_b64 s[30:31]\n1:" :: "n"(pad_at(G)));
        if constexpr (s_bar(G)) {
            if constexpr (G % 6 == 0) asm volatile("s_waitcnt vmcnt(%0) lgkmcnt(%1)\n\ts_barrier" :: "n"(bar_wait(G)), "n"(G == G1 ? 8 : 6) : "memory");
            else asm volatile("s_waitcnt vmcnt(%0)\n\ts_barrier" :: "n"(bar_wait(G)) : "memory");
        } else if constexpr (G % 6 == 0) asm volatile("s_waitcnt lgkmcnt(%0)" :: "n"(G == 42 ? 4 : G + 6 < GE ? 6 : 0) : "memory");
        u32x4& wf = Wf[G % 18];
        constexpr int sl = G < 324 ? 0 : m_slot(m), e = G < 324 ? 0 : m_ent(m), tl = G < 324 ? (g & 1) : m_half(m);
        constexpr bool first = G < 324 ? g < 2 : m_first(m), own = G >= 324 && ent_own(e);
        if constexpr (G >= 324) {
            if constexpr (ent_first(m) && !own) asm volatile("s_waitcnt vmcnt(%0)" :: "n"(younger(SCH.hgap[e], G)) : "memory");
        }
        auto mm = [&](auto TT) {
            constexpr int tt = TT.value;
            f32x4& ac = G < 324 ? acc[c & 1][2 * tl + tt] : acc2[sl][2 * tl + tt];
            u32x4& a = wf; u32x4& bo = G < 324 ? Xa[(g & ~1) + tt] : H[e][tt];
            if constexpr (first && own) asm volatile(MFMA "%0, %1, %2, 0" : "=&v"(ac) : "v"(a), "v"(bo));
            else if constexpr (first) asm volatile(MFMA "%0, %1, %2, 0" : "=&v"(ac) : "v"(a), "a"(bo));
            else if constexpr (own) asm volatile(MFMA "%0, %1, %2, %0" : "+v"(ac) : "v"(a), "v"(bo));
            else asm volatile(MFMA "%0, %1, %2, %0" : "+v"(ac) : "v"(a), "a"(bo));
            SB();
        };
        constexpr int hs = G >= 72 && G < 324 && g >= 1 ? (g < 12 ? g - 1 : g) : -1;
        mm(IC<0>{});
        if constexpr (G + 12 < GE) rdw(IC<G + 12>{});
        if constexpr (hs >= 0) act(IC<c - 1>{}, IC<hs>{}, IC<0>{});
        if constexpr (G >= 52 && G < 70) act(IC<0>{}, IC<2 * (G - 52)>{}, IC<2>{});
        if constexpr (G >= 326 && G < 344) act(IC<8>{}, IC<2 * (G - 326)>{}, IC<2>{});
        SB();
        mm(IC<1>{});
        if constexpr (G == 50) { corr(IC<0>{}); SB(); rde(IC<1>{}); }
        if constexpr (G < 324 && g == (c == 1 ? 30 : 11)) {
            if constexpr (c > 1) { act(IC<c - 1>{}, IC<10>{}, IC<1>{}); SB(); }
            corr(IC<c>{}); SB();
            if constexpr (c > 1) act(IC<c - 1>{}, IC<11>{}, IC<2>{});
        } else if constexpr (hs >= 0) act(IC<c - 1>{}, IC<hs>{}, IC<1>{});
        if constexpr (g == (c == 1 ? 32 : 14) && c < 8) rde(IC<c + 1>{});
        if constexpr (SCH.dn[G] >= 0) {
            dma(IC<SCH.dn[G] % 3>{}, wfb + (SCH.dn[G] / 3) * 12288);
            if constexpr (SCH.dn[G] % 3 == 2 && SCH.dn[G] < 161) set_m0(wl + ring_slot(SCH.dn[G] / 3 + 1) * 12288);
        }
        if constexpr (G == 36 || G == 37) stats(IC<G - 36>{});
        if constexpr (G >= 43 && G < 49) stats(IC<G - 41>{});
        if constexpr (G >= 52 && G < 70) act(IC<0>{}, IC<2 * (G - 52) + 1>{}, IC<2>{});
        if constexpr (G >= 326 && G < 344) act(IC<8>{}, IC<2 * (G - 326) + 1>{}, IC<2>{});
        if constexpr (s_hstore(G) >= 0) {
            constexpr int hf = s_hstore(G), cc = c - 1;
            __builtin_amdgcn_raw_buffer_store_b128(H[cc][hf], rs, voff + hf * 1024, hown + cc * 8192, 16);
        }
        if constexpr (s_pub(G) > 0) flag_store(s_pub(G), fso1 + s * 4);
        if constexpr (G == PUB2) flag_store(1u, fso2 + kq * 4);
        if constexpr (G == C1 || G == C2 || G == C3) {
            asm volatile("s_waitcnt vmcnt(%0)" :: "n"(younger(G == C1 ? P1 : G == C2 ? P2 : P3, G)) : "memory");
            check(G == C1 ? 2 : G == C2 ? 4 : 9);
        }
        if constexpr (G == P1 || G == P2 || G == P3) poll();
        if constexpr (G == P4) poll2();
        if constexpr (SCH.hl[G] >= 0) hload(IC<SCH.hl[G] / 2>{}, IC<SCH.hl[G] % 2>{});
        if constexpr (s_pstore(G) >= 0) {
            constexpr int sg = s_pstore(G) / 2, pt = s_pstore(G) % 2;
            const f32x4 a0 = acc2[sg][2 * pt], a1 = acc2[sg][2 * pt + 1];
            const u32x4 v = {pkbf(a0[0], a0[1]), pkbf(a0[2], a0[3]), pkbf(a1[0], a1[1]), pkbf(a1[2], a1[3])};
            if constexpr (sg == 0) __builtin_amdgcn_raw_buffer_store_b128(v, rs, voff, phb + kq * 1024, 16);
            else __builtin_amdgcn_raw_buffer_store_b128(v, rs, voff + pt * 1024, pgb + ((kq + sg) & 3) * 24576 + (sg - 1) * 8192, 16);
        }
        if constexpr (s_res(G)) {
            constexpr int i = (G - 620) / 2;
            set_m0(wl + RESL + i * 1024);
            const u32 so = xfrag(9 * s + (i < 2 ? 2 * kq + i : 8));
            asm volatile("s_nop 0" ::: "memory");
            dma(IC<0>{}, so);
        }
        SB();
    });

    {
        u32x2 xr[5];
        f32x4 gb[2];
        f32x2 gh;
        const u32 ra = wl + RESL + (4 * (lane >> 4) + ((lane & 15) >> 2)) * 64 + (lane & 3) * 8;
        const u32 ga = lbase + L_EXF + 4608 + (lane >> 4) * 16;
        asm volatile("s_waitcnt vmcnt(1)" ::: "memory");
        asm volatile("ds_read_b64_tr_b16 %0, %5\n\tds_read_b64_tr_b16 %1, %5 offset:32\n\tds_read_b64_tr_b16 %2, %5 offset:1024\n\t"
                     "ds_read_b64_tr_b16 %3, %5 offset:1056\n\tds_read_b64_tr_b16 %4, %6 offset:2048"
                     : "=&v"(xr[0]), "=&v"(xr[1]), "=&v"(xr[2]), "=&v"(xr[3]), "=&v"(xr[4]) : "v"(ra), "v"(ra + (kq >> 1) * 32) : "memory");
        asm volatile("ds_read_b128 %0, %3\n\tds_read_b128 %1, %3 offset:64\n\tds_read_b64 %2, %4 offset:128\n\ts_waitcnt vmcnt(0) lgkmcnt(0)"
                     : "=&v"(gb[0]), "=&v"(gb[1]), "=&v"(gh) : "v"(ga), "v"(ga + (kq & 1) * 8) : "memory");
        asm volatile("" : "+v"(xr[0]), "+v"(xr[1]), "+v"(xr[2]), "+v"(xr[3]), "+v"(xr[4]));
        check2();
        u32x4 P[6];
        u32 Q[4];
        const u32 pin = pgb + kq * 24576;
        asm volatile("buffer_load_dwordx4 %4, %10, %11, %14 offen sc1\n\tbuffer_load_dwordx4 %5, %10, %11, %14 offen offset:1024 sc1\n\t"
                     "buffer_load_dwordx4 %2, %10, %11, %13 offen sc1\n\tbuffer_load_dwordx4 %3, %10, %11, %13 offen offset:1024 sc1\n\t"
                     "buffer_load_dwordx4 %0, %10, %11, %12 offen sc1\n\tbuffer_load_dwordx4 %1, %10, %11, %12 offen offset:1024 sc1\n\t"
                     "buffer_load_dword %6, %15, %11, %16 offen sc1\n\tbuffer_load_dword %7, %15, %11, %16 offen offset:1024 sc1\n\t"
                     "buffer_load_dword %8, %15, %11, %16 offen offset:2048 sc1\n\tbuffer_load_dword %9, %15, %11, %16 offen offset:3072 sc1\n\t"
                     "s_waitcnt vmcnt(0)"
                     : "=&v"(P[0]), "=&v"(P[1]), "=&v"(P[2]), "=&v"(P[3]), "=&v"(P[4]), "=&v"(P[5]), "=&v"(Q[0]), "=&v"(Q[1]), "=&v"(Q[2]), "=&v"(Q[3])
                     : "v"(voff), "s"(srd), "s"(pin), "s"(pin + 8192), "s"(pin + 16384), "v"(voff + kq * 4), "s"(phb) : "memory");
        const auto ro = __builtin_amdgcn_make_buffer_rsrc(out, 0, SEQ * DM * 2, 0x00020000);
        const u32 ov = ((it * 128 + w * 32 + (lane & 15)) * DM + s * 144 + 4 * (lane >> 4)) * 2;
        auto fin2 = [&](float a0, float a1, float g0, float g1, u32 xk) -> u32 {
            const u32 t = pkbf(a0 * -0.69314718055994530942f, a1 * -0.69314718055994530942f);
            const u32 gz = pkbf(bflo(t) * g0, bfhi(t) * g1);
            return pkbf(bflo(xk) + bflo(gz), bfhi(xk) + bfhi(gz));
        };
#pragma unroll
        for (int k = 0; k < 4; ++k) {
            u32x2 o;
#pragma unroll
            for (int j = 0; j < 2; ++j) {
                const int d = 2 * (k & 1) + j;
                const u32 p0 = P[k >> 1][d], p1 = P[2 + (k >> 1)][d], p2 = P[4 + (k >> 1)][d], xk = xr[k][j];
                o[j] = fin2(acc2[4][k][2 * j] + bflo(p0) + bflo(p1) + bflo(p2), acc2[4][k][2 * j + 1] + bfhi(p0) + bfhi(p1) + bfhi(p2),
                            gb[k >> 1][2 * j], gb[k >> 1][2 * j + 1], xk);
            }
            __builtin_amdgcn_raw_buffer_store_b64(o, ro, ov + (k >> 1) * 32, kq * 64 + (k & 1) * (16 * DM * 2), OAUX);
        }
        const u32 oh = fin2(bflo(Q[0]) + bflo(Q[1]) + bflo(Q[2]) + bflo(Q[3]), bfhi(Q[0]) + bfhi(Q[1]) + bfhi(Q[2]) + bfhi(Q[3]),
                            gh[0], gh[1], (kq & 1) ? xr[4][1] : xr[4][0]);
        __builtin_amdgcn_raw_buffer_store_b32(oh, ro, ov + 256, (kq & 1) * 4 + (kq >> 1) * (16 * DM * 2), OAUX);
    }
    asm volatile("k_mlp_end:");
}
#if defined(__HIP_DEVICE_COMPILE__)
#pragma clang attribute pop
#endif

extern "C" void kernel_launch(void* const* d_in, const int* in_sizes, int n_in, void* d_out, int out_size,
                              void* d_ws, size_t ws_size, hipStream_t stream) {
    const u16* x = (const u16*)d_in[0];
    char* ws = (char*)d_ws;
    (void)hipFuncSetAttribute((const void*)k_mlp, hipFuncAttributeMaxDynamicSharedMemorySize, MLP_LDS);
    k_prep<<<NB_PREP, 256, PREP_LDS, stream>>>(x, (const u16*)d_in[1], (const u16*)d_in[2], (const u16*)d_in[3],
                                               (const u16*)d_in[4], (const u16*)d_in[5], (const u16*)d_in[9],
                                               (const u16*)d_in[10], (const u16*)d_in[11], ws);
    k_conv<<<NB_PF + NB_CONV + NB_W1, 256, CONV_LDS, stream>>>((const u16*)d_in[6], (const u16*)d_in[8], ws);
    k_mlp<<<256, 256, MLP_LDS, stream>>>(ws, (u16*)d_out);
}
```

```cpp
#include <hip/hip_runtime.h>
#include <stdint.h>

typedef unsigned short u16;
typedef unsigned int u32;
typedef float f32x2 __attribute__((ext_vector_type(2)));
typedef float f32x4 __attribute__((ext_vector_type(4)));
typedef float f32x16 __attribute__((ext_vector_type(16)));
typedef _Float16 f16x2 __attribute__((ext_vector_type(2)));
typedef _Float16 f16x8 __attribute__((ext_vector_type(8)));
typedef __bf16 bf16x2 __attribute__((ext_vector_type(2)));
typedef __bf16 bf16x8 __attribute__((ext_vector_type(8)));
typedef short s16x4 __attribute__((ext_vector_type(4)));
typedef u32 u32x2 __attribute__((ext_vector_type(2)));
typedef u32 u32x4 __attribute__((ext_vector_type(4)));
#define LAS __attribute__((address_space(3)))
#define DI __device__ __forceinline__

constexpr int SEQ = 2048, DM = 576, NS = 128, FF = 4608;

constexpr u32 WS_MOD   = 0;
constexpr u32 WS_STAT  = 16384;
constexpr u32 WS_KSC   = 32768;
constexpr u32 WS_FLAG  = 36864;
constexpr u32 WS_EX    = 65536;
constexpr u32 WS_XT    = 1u << 20;
constexpr u32 WS_KR    = WS_XT + 2359296;
constexpr u32 WS_X1F   = WS_KR + 2359296;
constexpr u32 WS_WF    = WS_X1F + 2359296;
constexpr u32 WS_HF    = WS_WF + 10616832;
constexpr u32 WS_PART  = WS_HF + 18874368;
constexpr u32 WS_PH    = WS_PART + 6291456;
constexpr u32 WS_END   = WS_PH + 1048576;

DI float bflo(u32 u) { return __builtin_bit_cast(float, u << 16); }
DI float bfhi(u32 u) { return __builtin_bit_cast(float, u & 0xffff0000u); }
DI float bf2f(u16 v) { return __builtin_bit_cast(float, ((u32)v) << 16); }
DI u32 pkbf(float a, float b) { f32x2 v = {a, b}; return __builtin_bit_cast(u32, __builtin_convertvector(v, bf16x2)); }
DI u32 pkhf(float a, float b) { f32x2 v = {a, b}; return __builtin_bit_cast(u32, __builtin_convertvector(v, f16x2)); }
DI float rbf(float a) { return bflo(pkbf(a, a)); }
template <int CTRL, int RM = 0xf> DI float dppf(float v) {
    return __builtin_bit_cast(float, __builtin_amdgcn_update_dpp(0, __builtin_bit_cast(int, v), CTRL, RM, 0xf, true));
}
DI u32x4 trread2(const char* smem, u32 off0, u32 off1) {
    const u32x2 l = __builtin_bit_cast(u32x2, __builtin_amdgcn_ds_read_tr16_b64_v4i16((LAS s16x4*)(smem + off0)));
    const u32x2 h = __builtin_bit_cast(u32x2, __builtin_amdgcn_ds_read_tr16_b64_v4i16((LAS s16x4*)(smem + off1)));
    return (u32x4){l[0], l[1], h[0], h[1]};
}
DI void st16(char* ws, u32 off, u32x4 v) {
    __builtin_amdgcn_raw_buffer_store_b128(v, __builtin_amdgcn_make_buffer_rsrc(ws, 0, WS_END, 0x00020000), off, 0, 16);
}
DI void st4(char* ws, u32 off, u32 v) {
    __builtin_amdgcn_raw_buffer_store_b32(v, __builtin_amdgcn_make_buffer_rsrc(ws, 0, WS_END, 0x00020000), off, 0, 16);
}
DI float pow2i(int e) { e = e < -126 ? -126 : (e > 127 ? 127 : e); return __builtin_bit_cast(float, (u32)(e + 127) << 23); }
DI int expo(float x) { return (int)((__builtin_bit_cast(u32, x) >> 23) & 255u) - 127; }
DI u32 imgrow(u32 row) { return row * 1184u + (row >> 3) * 128u; }
DI f32x2 cmul(f32x2 z, f32x2 w, f32x2 wp) {
    const f32x2 zx = {z[0], z[0]}, zy = {z[1], z[1]};
    return __builtin_elementwise_fma(zy, wp, zx * w);
}
#define CODE_PREFETCH(sym, part) do { u32 lo_, hi_, len_; \
    asm volatile("s_getpc_b64 s[20:21]\n\ts_add_u32 s20, s20, " #sym "@rel32@lo+4\n\ts_addc_u32 s21, s21, " #sym "@rel32@hi+12\n\t" \
                 "s_mov_b32 %0, s20\n\ts_mov_b32 %1, s21\n\ts_mov_b32 %2, " #sym "_end-" #sym : "=s"(lo_), "=s"(hi_), "=s"(len_) :: "scc", "s20", "s21"); \
    const char* p_ = (const char*)(((unsigned long long)hi_ << 32) | lo_); \
    const u32 o_ = (threadIdx.x + (part) * 256u) * 128u; \
    if (o_ < len_) { u32 t_; asm volatile("global_load_dword %0, %1, off\n\ts_waitcnt vmcnt(0)" : "=v"(t_) : "v"(p_ + o_) : "memory"); } \
    } while (0)

#define DELAY1 20
#define NAV 26
constexpr int NA = NAV, NB = 36 - NA;
constexpr int NB_PF = 16, NB_LN = 64, NB_GEMV = 108, NB_KGEN = 576, NB_W2 = 144;
constexpr int NB_PREP = NB_PF + NB_LN + NB_GEMV + NB_KGEN + NB_W2;
constexpr int PREP_LDS = 38400;

extern "C" __global__ __launch_bounds__(256, 4) void k_prep(const u16* __restrict__ x, const u16* __restrict__ logA,
                                              const u16* __restrict__ Aim, const u16* __restrict__ Cre,
                                              const u16* __restrict__ Cim, const u16* __restrict__ logdt,
                                              const u16* __restrict__ w2, const u16* __restrict__ cvec,
                                              const u16* __restrict__ wmod, char* __restrict__ ws) {
    extern __shared__ __attribute__((aligned(16))) char smem[];
    const int tid = threadIdx.x, lane = tid & 63;
    const int wv = __builtin_amdgcn_readfirstlane(tid >> 6);
    int b = blockIdx.x;
    if (b >= NB_PF && b < NB_PF + NB_KGEN + NB_LN + NB_GEMV) b = b < NB_PF + NB_KGEN ? b + NB_LN + NB_GEMV : b - NB_KGEN;
    if (b < NB_PF) {
        if (b < 8) CODE_PREFETCH(k_prep, 0); else CODE_PREFETCH(k_conv, 0);
        if (b < 2) st16(ws, WS_FLAG + b * 4096 + tid * 16, (u32x4){0u, 0u, 0u, 0u});
    } else if ((b -= NB_PF) < NB_LN) {
        const int t0 = b * 32, tok = tid >> 3, sub = tid & 7;
        u32x4 v[9];
#pragma unroll
        for (int i = 0; i < 9; ++i) v[i] = *(const u32x4*)(x + (size_t)(t0 + tok) * DM + (sub + 8 * i) * 8);
#pragma unroll
        for (int i = 0; i < 9; ++i) *(u32x4*)(smem + imgrow(tok) + (sub + 8 * i) * 16) = v[i];
        __syncthreads();
        const int g = lane >> 4, qq = (lane & 15) >> 2, p = lane & 3;
#pragma unroll
        for (int i = 0; i < 9; ++i) {
            const int cb = wv * 9 + i;
            st16(ws, WS_XT + b * 36864 + cb * 1024 + (lane & 15) * 64 + g * 16,
                 trread2(smem, imgrow(8 * g + qq) + cb * 32 + p * 8, imgrow(8 * g + 4 + qq) + cb * 32 + p * 8));
        }
        float s = 0.f;
#pragma unroll
        for (int i = 0; i < 9; ++i)
#pragma unroll
            for (int k = 0; k < 4; ++k) s += bflo(v[i][k]) + bfhi(v[i][k]);
        s += dppf<0xB1>(s); s += dppf<0x4E>(s); s += dppf<0x141>(s);
        const float mean = s * (1.f / DM);
        float q = 0.f;
#pragma unroll
        for (int i = 0; i < 9; ++i)
#pragma unroll
            for (int k = 0; k < 4; ++k) {
                const float a = bflo(v[i][k]) - mean, c = bfhi(v[i][k]) - mean;
                q = fmaf(a, a, q); q = fmaf(c, c, q);
            }
        q += dppf<0xB1>(q); q += dppf<0x4E>(q); q += dppf<0x141>(q);
        if (sub == 0) st4(ws, WS_STAT + (t0 + tok) * 4, pkbf(mean, __builtin_amdgcn_sqrtf(rbf(rbf(q * (1.f / DM)) + 1e-5f))));
    } else if ((b -= NB_LN) < NB_GEMV) {
        const int seg = tid & 3, rg = tid >> 2;
        u32x4 v[9]; u16 cv[9];
#pragma unroll
        for (int i = 0; i < 9; ++i) {
            const int row = rg + 64 * i;
            cv[i] = cvec[row];
            v[i] = *(const u32x4*)(wmod + (size_t)row * (6 * DM) + b * 32 + seg * 8);
        }
        float acc[8];
#pragma unroll
        for (int k = 0; k < 8; ++k) acc[k] = 0.f;
#pragma unroll
        for (int i = 0; i < 9; ++i) {
            const float c = bf2f(cv[i]);
            const float sl = rbf(c * __builtin_amdgcn_rcpf(1.f + __builtin_amdgcn_exp2f(c * -1.44269504088896340736f)));
#pragma unroll
            for (int k = 0; k < 4; ++k) {
                acc[2 * k] = fmaf(sl, bflo(v[i][k]), acc[2 * k]);
                acc[2 * k + 1] = fmaf(sl, bfhi(v[i][k]), acc[2 * k + 1]);
            }
        }
#pragma unroll
        for (int k = 0; k < 8; ++k) { acc[k] += dppf<0x114>(acc[k]); acc[k] += dppf<0x118>(acc[k]); }
        float* red = (float*)smem;
        if ((lane & 15) >= 12) {
            *(f32x4*)(red + (tid >> 4) * 32 + seg * 8) = (f32x4){acc[0], acc[1], acc[2], acc[3]};
            *(f32x4*)(red + (tid >> 4) * 32 + seg * 8 + 4) = (f32x4){acc[4], acc[5], acc[6], acc[7]};
        }
        __syncthreads();
        if (tid < 32) {
            float t = 0.f;
#pragma unroll
            for (int i = 0; i < 16; ++i) t += red[i * 32 + tid];
            st4(ws, WS_MOD + (b * 32 + tid) * 4, __builtin_bit_cast(u32, rbf(t)));
        }
    } else if ((b -= NB_GEMV) < NB_KGEN) {
        const int d = b, hn = wv & 1, grp = wv >> 1;
        const int n = hn * 64 + lane, n2 = n ^ 64;
        const u16 la_ = logA[d * NS + n], ai_ = Aim[d * NS + n], cr_ = Cre[d * NS + n], ci_ = Cim[d * NS + n];
        const u16 cr2_ = Cre[d * NS + n2], ci2_ = Cim[d * NS + n2], ld_ = logdt[d];
        u32 ta[8];
#pragma unroll
        for (int j = 0; j < 8; j += 2) {
            const float l0 = (float)(2047 - tid * 8 - j);
            const u32 pk = pkbf(l0, l0 - 1.f);
            const float p0 = bflo(pk), p1 = bfhi(pk);
            ta[j] = (u32)fminf(4.f * p0, p0 + 1536.f); ta[j + 1] = (u32)fminf(4.f * p1, p1 + 1536.f);
        }
        asm volatile("" : "+v"(ta[0]), "+v"(ta[1]), "+v"(ta[2]), "+v"(ta[3]), "+v"(ta[4]), "+v"(ta[5]), "+v"(ta[6]), "+v"(ta[7]));
        const float cr = bf2f(cr_), ci = bf2f(ci_);
        const float dt = rbf(__builtin_amdgcn_exp2f(bf2f(ld_) * 1.44269504088896340736f));
        const float zr = -rbf(__builtin_amdgcn_exp2f(bf2f(la_) * 1.44269504088896340736f)) * dt, zi = bf2f(ai_) * dt;
        float bs = fabsf(cr) + fabsf(ci) + fabsf(bf2f(cr2_)) + fabsf(bf2f(ci2_));
        bs += dppf<0xB1>(bs); bs += dppf<0x4E>(bs); bs += dppf<0x141>(bs); bs += dppf<0x140>(bs);
        bs += dppf<0x142, 0xa>(bs); bs += dppf<0x143, 0xc>(bs);
        bs = __builtin_bit_cast(float, __builtin_amdgcn_readlane(__builtin_bit_cast(int, bs), 63));
        const int se = 14 - expo(2.f * dt * bs * 1.0001f);
        const float scale = pow2i(se > 100 ? 100 : se);
        const float zr2 = zr * 1.44269504088896340736f;
        auto cexp = [&](float p) -> f32x2 {
            const float mag = __builtin_amdgcn_exp2f(zr2 * p);
            const float ang = zi * p;
            const float rr = __builtin_rintf(ang * 0.15915494309189535f);
            float fr = fmaf(ang, 0.15915494309189535f, -rr);
            fr = fmaf(ang, 6.4206382e-9f, fr);
            return (f32x2){mag * __builtin_amdgcn_cosf(fr), mag * __builtin_amdgcn_sinf(fr)};
        };
        const float p1 = grp ? 4.f : 1.f;
        const f32x2 f1 = cexp(p1), c1 = cexp(16.f * p1), s0 = cexp(grp ? 512.f : 0.f), s1 = cexp(256.f * p1);
        const f32x2 fw = {f1[0], -f1[1]}, fwp = {f1[1], f1[0]};
        const f32x2 cwp = {-c1[1], c1[0]};
        const float es = 2.f * dt * scale;
        f32x2 za = cmul((f32x2){es * cr, es * ci}, s0, (f32x2){-s0[1], s0[0]});
        f32x2 zb = cmul(za, s1, (f32x2){-s1[1], s1[0]});
        f32x2 zf = {1.f, 0.f};
        char* tab = smem + wv * 8704;
        u32 keep[16];
#pragma unroll
        for (int k = 0; k < 16; ++k) {
            *(u32*)(tab + k * 272 + lane * 4) = pkhf(zf[0], zf[1]);
            *(u32*)(tab + (16 + k) * 272 + lane * 4) = pkhf(za[0], za[1]);
            keep[k] = pkhf(zb[0], zb[1]);
            zf = cmul(zf, fw, fwp); za = cmul(za, c1, cwp); zb = cmul(zb, c1, cwp);
        }
        const int r16 = lane & 15, g = lane >> 4;
        f16x8 fb[4];
        f32x4 acc0 = {0.f, 0.f, 0.f, 0.f}, acc1 = {0.f, 0.f, 0.f, 0.f};
        __builtin_amdgcn_fence(__ATOMIC_RELEASE, "wavefront");
        __builtin_amdgcn_wave_barrier();
#pragma unroll
        for (int ks = 0; ks < 4; ++ks) {
            fb[ks] = *(const f16x8*)(tab + r16 * 272 + ks * 64 + g * 16);
            const f16x8 fa = *(const f16x8*)(tab + (16 + r16) * 272 + ks * 64 + g * 16);
            acc0 = __builtin_amdgcn_mfma_f32_16x16x32_f16(fb[ks], fa, acc0, 0, 0, 0);
        }
        __builtin_amdgcn_fence(__ATOMIC_RELEASE, "wavefront");
        __builtin_amdgcn_wave_barrier();
#pragma unroll
        for (int k = 0; k < 16; ++k) *(u32*)(tab + (16 + k) * 272 + lane * 4) = keep[k];
        __builtin_amdgcn_fence(__ATOMIC_RELEASE, "wavefront");
        __builtin_amdgcn_wave_barrier();
#pragma unroll
        for (int ks = 0; ks < 4; ++ks) {
            const f16x8 fa = *(const f16x8*)(tab + (16 + r16) * 272 + ks * 64 + g * 16);
            acc1 = __builtin_amdgcn_mfma_f32_16x16x32_f16(fb[ks], fa, acc1, 0, 0, 0);
        }
        __builtin_amdgcn_fence(__ATOMIC_RELEASE, "wavefront");
        __builtin_amdgcn_wave_barrier();
        *(f32x4*)(tab + r16 * 64 + g * 16) = acc0;
        *(f32x4*)(tab + 1024 + r16 * 64 + g * 16) = acc1;
        __syncthreads();
        float kv[8];
#pragma unroll
        for (int j = 0; j < 8; ++j) {
            const u32 o = ta[j] + (ta[j] >= 2048u ? 2u * 8704u - 2048u : 0u);
            kv[j] = *(const float*)(smem + o) + *(const float*)(smem + o + 8704);
        }
        st16(ws, WS_KR + d * 4096 + tid * 16, (u32x4){pkhf(kv[0], kv[1]), pkhf(kv[2], kv[3]), pkhf(kv[4], kv[5]), pkhf(kv[6], kv[7])});
        if (tid == 0) st4(ws, WS_KSC + d * 4, __builtin_bit_cast(u32, pow2i(se > 100 ? -100 : -se)));
        if (wv == 0) asm volatile("s_call_b64 s[30:31], k_conv_pad0" ::: "s30", "s31");
        if (wv == 1) asm volatile("s_call_b64 s[30:31], k_conv_pad1" ::: "s30", "s31");
        if (wv == 2) asm volatile("s_call_b64 s[30:31], k_conv_pad2" ::: "s30", "s31");
        if (wv == 3) asm volatile("s_call_b64 s[30:31], k_conv_pad3" ::: "s30", "s31");
    } else {
        b -= NB_KGEN;
        const u16* src = w2 + (size_t)b * 32 * DM;
        if (DELAY1 > 0) __builtin_amdgcn_s_sleep(DELAY1);
        const int tok = tid >> 3, sub = tid & 7;
        u32x4 v[9];
#pragma unroll
        for (int i = 0; i < 9; ++i) v[i] = *(const u32x4*)(src + tok * DM + (sub + 8 * i) * 8);
#pragma unroll
        for (int i = 0; i < 9; ++i) *(u32x4*)(smem + imgrow(tok) + (sub + 8 * i) * 16) = v[i];
        __syncthreads();
        const int kq = b / 36, kk = b - kq * 36, sp = kk / 9, c = kk - sp * 9;
        const int rel = (sp - wv) & 3;
        const int e = rel == 0 ? c : 9 + c * 3 + (rel - 1);
        const int r0 = 4 * (lane >> 4) + ((lane & 15) >> 2), p = lane & 3;
#pragma unroll
        for (int i = 0; i < 9; ++i) {
            const int sg = (i + 1) >> 1, j = i == 0 ? 0 : (i - 1) & 1;
            const int T = 9 * wv + (sg == 0 ? 8 : 2 * ((kq + sg) & 3) + j);
            const int m = sg == 4 ? 252 + 2 * e + j
                        : e < NA ? 7 * e + i
                        : sg == 0 ? 7 * NA + 2 * NB + (e - NA) : 7 * NA + (sg == 1 ? 0 : (2 * sg - 1) * NB) + (e - NA) * 2 + j;
            const u32 col = T * 32 + p * 8;
            st16(ws, WS_WF + ((kq * 4 + wv) * 648 + 324 + m) * 1024 + lane * 16, trread2(smem, imgrow(r0) + col, imgrow(r0 + 16) + col));
        }
    }
    asm volatile("k_prep_end:");
}

constexpr int NB_CONV = 576, NB_W1 = 144;
constexpr int CONV_LDS = 36864 + 4096;
constexpr int UCP = 4640, KB_OFF = 4 * UCP;

extern "C" __global__ __attribute__((aligned(4096))) __launch_bounds__(256, 4) void k_conv(const u16* __restrict__ dskip, const u16* __restrict__ w1,
                                              char* __restrict__ ws) {
    extern __shared__ __attribute__((aligned(16))) char smem[];
    const int tid = threadIdx.x, lane = tid & 63;
    const int wv = __builtin_amdgcn_readfirstlane(tid >> 6);
    asm volatile("s_branch 1f\nk_conv_pad0:\n\ts_setpc_b64 s[30:31]\n1:");
    const float* mod = (const float*)(ws + WS_MOD);
    int b = blockIdx.x;
    if (b >= NB_PF) b = b < NB_PF + NB_CONV ? b + NB_W1 : b - NB_CONV;
    if (b < NB_PF) {
        CODE_PREFETCH(k_mlp, b >> 3);
    } else if ((b -= NB_PF) < NB_W1) {
        asm volatile("s_branch 1f\n\t.p2align 12\nk_conv_pad2:\n\ts_setpc_b64 s[30:31]\n1:");
        b = (b & 7) * 18 + (b >> 3);
        const int q = b / 9, c = b - q * 9, f0 = b * 32;
        const int seg = tid & 3, rg = tid >> 2;
        u32x4 v[9]; float sb[9], cb[9];
#pragma unroll
        for (int i = 0; i < 9; ++i) {
            const int row = rg + 64 * i;
            v[i] = *(const u32x4*)(w1 + (size_t)row * FF + f0 + seg * 8);
            sb[i] = mod[3 * DM + row]; cb[i] = mod[4 * DM + row];
        }
        float S[8], B[8];
#pragma unroll
        for (int k = 0; k < 8; ++k) { S[k] = 0.f; B[k] = 0.f; }
#pragma unroll
        for (int i = 0; i < 9; ++i) {
            const float c1 = rbf(1.f + cb[i]);
            u32x4 o;
#pragma unroll
            for (int k = 0; k < 4; ++k) {
                const float lo = bflo(v[i][k]), hi = bfhi(v[i][k]);
                o[k] = pkbf(lo * c1, hi * c1);
                S[2 * k] += bflo(o[k]); S[2 * k + 1] += bfhi(o[k]);
                B[2 * k] = fmaf(sb[i], lo, B[2 * k]); B[2 * k + 1] = fmaf(sb[i], hi, B[2 * k + 1]);
            }
            *(u32x4*)(smem + ((tid ^ ((tid >> 3) & 2)) + 256 * i) * 16) = o;
        }
#pragma unroll
        for (int k = 0; k < 8; ++k) {
            S[k] += dppf<0x114>(S[k]); S[k] += dppf<0x118>(S[k]);
            B[k] += dppf<0x114>(B[k]); B[k] += dppf<0x118>(B[k]);
        }
        float* red = (float*)(smem + 36864);
        if ((lane & 15) >= 12) {
            float* r = red + (tid >> 4) * 64 + seg * 8;
            *(f32x4*)(r) = (f32x4){S[0], S[1], S[2], S[3]}; *(f32x4*)(r + 4) = (f32x4){S[4], S[5], S[6], S[7]};
            *(f32x4*)(r + 32) = (f32x4){B[0], B[1], B[2], B[3]}; *(f32x4*)(r + 36) = (f32x4){B[4], B[5], B[6], B[7]};
        }
        __syncthreads();
        asm volatile("s_branch 1f\n\t.p2align 12\nk_conv_pad3:\n\ts_setpc_b64 s[30:31]\n1:");
        const int g = lane >> 4, qq = (lane & 15) >> 2, p = lane & 3;
#pragma unroll
        for (int i = 0; i < 9; ++i) {
            const int s = wv * 9 + i;
            const u32 a = (32 * (s >> 1) + 4 * g + qq) * 64 + (((s & 1) ^ (g & 1)) * 32) + p * 8;
            st16(ws, WS_WF + (q * 648 + c * 36 + s) * 1024 + lane * 16, trread2(smem, a, a + 1024));
        }
        if (wv == 0) {
            float t = 0.f;
#pragma unroll
            for (int i = 0; i < 16; ++i) t += red[i * 64 + lane];
            const float B_ = __builtin_bit_cast(float, __builtin_amdgcn_ds_bpermute((lane ^ 32) * 4, __builtin_bit_cast(int, t)));
            const float S_ = -t;
            const float s1 = rbf(S_), s2 = rbf(S_ - s1), s3 = S_ - s1 - s2;
            const float b1 = rbf(B_), b2 = rbf(B_ - b1), b3 = B_ - b1 - b2;
            if (lane < 32) st16(ws, WS_EX + q * 8192 + (c * 32 + lane) * 16, (u32x4){pkbf(s1, s2), pkbf(s3, b1), pkbf(b2, b3), 0u});
        } else if (wv == 1 && c == 0 && lane < 48) {
            const int d = (q & 3) * 144 + (lane < 32 ? (q >> 2) * 32 + lane : 96 + lane);
            st4(ws, WS_EX + q * 8192 + 4608 + lane * 4, __builtin_bit_cast(u32, mod[5 * DM + d]));
        }
    } else {
        const int d = b - NB_W1;
        asm volatile("s_branch 1f\nk_conv_pad1:\n\ts_setpc_b64 s[30:31]\n1:");
        const int tp = tid > 0 ? tid * 8 - 4 : 0;
        const char* xb = ws + WS_XT + d * 64;
        const u32x4 x0 = *(const u32x4*)(xb + (tid >> 2) * 36864 + (tid & 3) * 16);
        const u32x2 xm = *(const u32x2*)(xb + (tp >> 5) * 36864 + (tp & 31) * 2);
        u32x4 st[3];
        st[0] = *(const u32x4*)(ws + WS_STAT + tp * 4);
        st[1] = *(const u32x4*)(ws + WS_STAT + tid * 32);
        st[2] = *(const u32x4*)(ws + WS_STAT + tid * 32 + 16);
        const u32x4 kv = *(const u32x4*)(ws + WS_KR + d * 4096 + tid * 16);
        const float sa = mod[d], ca = mod[DM + d], ga = mod[2 * DM + d];
        const float dsk = bf2f(dskip[d]);
        const float kinv = ((const float*)(ws + WS_KSC))[d];
        {
            const u32x4 z = {0u, 0u, 0u, 0u};
            if (tid < 96) *(u32x4*)(smem + (tid / 24) * UCP + (tid % 24) * 16) = z;
            else if (tid < 104) *(u32x4*)(smem + KB_OFF + 4096 + (tid - 96) * 16) = z;
        }
        const float c1 = rbf(1.f + ca);
        const int he = expo(64.f * fabsf(c1) + 2.f * fabsf(sa)) - 14;
        const float hs = pow2i(-he), oscale = pow2i(he) * kinv;
        const u32 xw[6] = {xm[0], xm[1], x0[0], x0[1], x0[2], x0[3]};
        u32 P[6], hb[4];
#pragma unroll
        for (int k = 0; k < 6; ++k) {
            const u32 s0 = st[k >> 1][(k & 1) * 2], s1 = st[k >> 1][(k & 1) * 2 + 1];
            const float n0 = (bflo(xw[k]) - bflo(s0)) * __builtin_amdgcn_rcpf(bfhi(s0));
            const float n1 = (bfhi(xw[k]) - bflo(s1)) * __builtin_amdgcn_rcpf(bfhi(s1));
            const u32 t = pkbf(fmaf(n0, c1, sa), fmaf(n1, c1, sa));
            if (k >= 2) hb[k - 2] = t;
            P[k] = pkhf(bflo(t) * hs, bfhi(t) * hs);
        }
        if (tid == 0) { P[0] = 0u; P[1] = 0u; }
        u32 Q[5];
#pragma unroll
        for (int k = 0; k < 5; ++k) Q[k] = __builtin_amdgcn_alignbit(P[k + 1], P[k], 16);
        {
            char* ub = smem + 384 + tid * 16;
            *(u32x4*)(ub) = (u32x4){Q[0], Q[1], Q[2], Q[3]};
            *(u32x4*)(ub + UCP) = (u32x4){P[1], P[2], P[3], P[4]};
            *(u32x4*)(ub + 2 * UCP) = (u32x4){Q[1], Q[2], Q[3], Q[4]};
            *(u32x4*)(ub + 3 * UCP) = (u32x4){P[2], P[3], P[4], P[5]};
            *(u32x4*)(smem + KB_OFF + tid * 16) = kv;
        }
        __syncthreads();
        const int c = lane & 15, g = lane >> 4;
        f32x4 acc[8];
#pragma unroll
        for (int j = 0; j < 8; ++j) acc[j] = (f32x4){0.f, 0.f, 0.f, 0.f};
        {
            const u32 lb = (u32)(uintptr_t)(LAS char*)smem;
            const u32 ka = lb + KB_OFF + 2 * (2076 - 4 * c + 8 * g - 64 * wv) - 3584 - 64;
            const u32 ua = lb + (c & 3) * UCP + 2 * (8 * g + 64 * (c >> 2) - 64 * wv + 192);
            u32x2 kl[2][8], kh[2][8]; u32x4 uf[2][8];
            auto rd = [&](int rr, int n) {
                asm volatile("ds_read_b64 %0, %3 offset:%5\n\tds_read_b64 %1, %3 offset:%6\n\tds_read_b128 %2, %4 offset:%7"
                             : "=&v"(kl[rr][n]), "=&v"(kh[rr][n]), "=&v"(uf[rr][n]) : "v"(ka), "v"(ua),
                               "n"((7 - n) * 512 + (1 - rr) * 64), "n"((7 - n) * 512 + (1 - rr) * 64 + 8), "n"(n * 512 + (1 - rr) * 64) : "memory");
            };
            auto mm = [&](int rr, int i, int dl) {
                const u32x4 k = {kl[rr][i][0], kl[rr][i][1], kh[rr][i][0], kh[rr][i][1]};
                acc[i + dl] = __builtin_amdgcn_mfma_f32_16x16x32_f16(__builtin_bit_cast(f16x8, uf[rr][dl]), __builtin_bit_cast(f16x8, k), acc[i + dl], 0, 0, 0);
            };
            auto shell = [&](int rr, int n) {
#pragma unroll
                for (int o = 0; o <= n && o + n < 8; ++o) { mm(rr, n, o); if (o < n) mm(rr, o, n); }
            };
            auto pin = [&](int rr, int n) { asm volatile("" : "+v"(kl[rr][n]), "+v"(kh[rr][n]), "+v"(uf[rr][n])); };
            rd(0, 0); rd(0, 7); rd(0, 1); rd(0, 6); rd(0, 2); rd(0, 5); rd(0, 3); rd(0, 4);
#pragma unroll
            for (int n = 7; n >= 0; --n) {
                if (n >= 4) { asm volatile("s_waitcnt lgkmcnt(%0)" :: "n"(n == 7 ? 15 : 6 * (n - 4) + 3 * (7 - n)) : "memory"); pin(0, n); pin(0, 7 - n); }
                shell(0, n);
                __builtin_amdgcn_sched_barrier(0);
                rd(1, 7 - n);
            }
#pragma unroll
            for (int n = 0; n < 8; ++n) {
                asm volatile("s_waitcnt lgkmcnt(%0)" :: "n"(n < 3 ? 15 : 3 * (7 - n)) : "memory");
                pin(1, n);
                shell(1, n);
                __builtin_amdgcn_sched_barrier(0);
            }
        }
        __syncthreads();
#pragma unroll
        for (int j = 0; j < 8; ++j) *(f32x4*)(smem + wv * 8192 + j * 1024 + lane * 16) = acc[j];
        __syncthreads();
        f32x4 y0 = *(const f32x4*)(smem + tid * 32), y1 = *(const f32x4*)(smem + tid * 32 + 16);
#pragma unroll
        for (int w = 1; w < 4; ++w) {
            y0 += *(const f32x4*)(smem + w * 8192 + tid * 32);
            y1 += *(const f32x4*)(smem + w * 8192 + tid * 32 + 16);
        }
        const float y[8] = {y0[0], y0[1], y0[2], y0[3], y1[0], y1[1], y1[2], y1[3]};
        u32x4 o;
#pragma unroll
        for (int k = 0; k < 4; ++k) {
            const u32 yb = pkbf(y[2 * k] * oscale, y[2 * k + 1] * oscale);
            const u32 sk = pkbf(bflo(hb[k]) * dsk, bfhi(hb[k]) * dsk);
            const u32 z = pkbf(bflo(yb) + bflo(sk), bfhi(yb) + bfhi(sk));
            const u32 gz = pkbf(bflo(z) * ga, bfhi(z) * ga);
            o[k] = pkbf(bflo(x0[k]) + bflo(gz), bfhi(x0[k]) + bfhi(gz));
        }
        st16(ws, WS_X1F + (tid >> 4) * 147456 + ((d / 48) * 12 + ((tid >> 2) & 3) * 3 + (d >> 4) % 3) * 1024 + (d & 15) * 64 + (tid & 3) * 16, o);
        if (wv == 0) asm volatile("s_call_b64 s[30:31], k_mlp_pad7\n\ts_call_b64 s[30:31], k_mlp_pad4" ::: "s30", "s31");
        if (wv == 1) asm volatile("s_call_b64 s[30:31], k_mlp_pad6\n\ts_call_b64 s[30:31], k_mlp_pad5" ::: "s30", "s31");
        if (wv == 2) asm volatile("s_call_b64 s[30:31], k_mlp_pad0\n\ts_call_b64 s[30:31], k_mlp_pad2" ::: "s30", "s31");
        if (wv == 3) asm volatile("s_call_b64 s[30:31], k_mlp_pad1\n\ts_call_b64 s[30:31], k_mlp_pad3" ::: "s30", "s31");
    }
    asm volatile("k_conv_end:");
}
constexpr u32 L_EXF = 147456;
constexpr int MLP_LDS = L_EXF + 12288;
constexpr int GE = 648;

#if defined(__HIP_DEVICE_COMPILE__)
#pragma clang attribute push(__attribute__((target("no-packed-fp32-ops"))), apply_to = function)
#endif
template <int N> struct IC { static constexpr int value = N; };
template <int A, int B, class F> DI void static_for(F&& f) {
    if constexpr (B - A == 1) f(IC<A>{});
    else if constexpr (A < B) { static_for<A, (A + B) / 2>(f); static_for<(A + B) / 2, B>(f); }
}
constexpr int imin(int a, int b) { return a < b ? a : b; }
constexpr int XW = 6;
#define XSP 7
#define XIV 8
constexpr int XI = XIV;
constexpr int x_dmas(int j) { return j <= 0 ? 0 : imin((4 * j + XSP - 1) / XSP, 3 * (19 - XI)); }
constexpr int G1 = 36;
constexpr int P1 = 36 * 5 + 7, P2 = 36 * 7 + 7, P3 = 400;
#define CD1 34
#define CD3 30
#define QW3 8
constexpr int C1 = P1 + CD1, C2 = P2 + CD1, C3 = P3 + CD3;
constexpr bool s_poll(int G) { return G == P1 || G == P2 || G == P3; }
#define PB2 606
#define PL4 640
constexpr int PUB2 = PB2, P4 = PL4;
constexpr int HS0 = 22, HS1 = 35;
constexpr int s_hstore(int G) { return G < 72 ? (G == 64 ? 0 : G == 71 ? 1 : -1) : G < 324 ? (G % 36 == HS0 ? 0 : G % 36 == HS1 ? 1 : -1) : G == 338 ? 0 : G == 346 ? 1 : -1; }
constexpr int B0 = 324 + 7 * NA;
constexpr int slot_end(int sg) { return B0 - 1 + (sg == 1 ? 2 : sg == 0 ? 3 : 2 * sg + 1) * NB; }
constexpr int s_pstore(int G) {
    for (int sg = 0; sg < 4; ++sg) { if (G == slot_end(sg) + 4) return 2 * sg; if (G == slot_end(sg) + 6 && sg > 0) return 2 * sg + 1; }
    return -1;
}
constexpr bool s_res(int G) { return G == 620 || G == 622 || G == 624; }
constexpr int s_pub(int G) { return G == 36 * 3 + 24 ? 2 : G == 36 * 5 + 24 ? 4 : G == 372 ? 9 : 0; }
constexpr int m_slot(int m) { return m < 7 * NA ? (m % 7 + 1) / 2 : m < 7 * NA + 2 * NB ? 1 : m < 7 * NA + 3 * NB ? 0 : m < 252 ? 2 + (m - 7 * NA - 3 * NB) / (2 * NB) : 4; }
constexpr int m_ent(int m) { return m < 7 * NA ? m / 7 : m < 7 * NA + 2 * NB ? NA + (m - 7 * NA) / 2 : m < 7 * NA + 3 * NB ? NA + (m - 7 * NA - 2 * NB)
                                  : m < 252 ? NA + ((m - 7 * NA - 3 * NB) % (2 * NB)) / 2 : (m - 252) / 2; }
constexpr int m_half(int m) { return m < 7 * NA ? (m % 7 == 0 ? 0 : (m % 7 - 1) & 1) : m < 7 * NA + 2 * NB ? (m - 7 * NA) & 1 : m < 7 * NA + 3 * NB ? 0
                                   : m < 252 ? (m - 7 * NA - 3 * NB) & 1 : m & 1; }
constexpr bool m_first(int m) { return m < 7 || m == 252 || m == 253; }
constexpr bool ent_first(int m) { return m < 7 * NA ? m % 7 == 0 : (m < 7 * NA + 2 * NB && ((m - 7 * NA) & 1) == 0); }
constexpr bool ent_own(int e) { return e < 9; }
constexpr int first_use(int e) { return e < NA ? 324 + 7 * e : B0 + 2 * (e - NA); }
constexpr bool s_ring(int G) { return G + 12 < GE && (G < 100 ? G % 12 == 0 : G < 490 ? G % 36 == 24 : G % 18 == 6); }
constexpr int ring_slot(int grp) { const int u = (grp / 3) & 3; return (u == 0 ? 0 : u == 1 ? 6 : u == 2 ? 9 : 3) + grp % 3; }
constexpr int need_bar(int grp) { int g = 12 * grp - 12; while (g > 0 && !s_ring(g)) --g; return g; }
struct Sched {
    int dn[GE];
    int dgap[162];
    int hl[GE];
    int pre[GE + 1];
    int hgap[36];
    bool ok;
    constexpr Sched() : dn(), dgap(), hl(), pre(), hgap(), ok(true) {
        int nd = 3 * XW, nh = 0, last2 = -99, lastbar = 24;
        for (int n = 0; n < 162; ++n) dgap[n] = -1;
        for (int e = 0; e < 36; ++e) hgap[e] = -1;
        for (int G = 0; G < GE; ++G) {
            dn[G] = -1; hl[G] = -1;
            int n = 0;
            if (G >= G1) {
                if (s_ring(G)) lastbar = G;
                const bool fixed = s_hstore(G) >= 0 || s_pstore(G) >= 0 || s_res(G);
                const bool quiet = (G >= P1 && G < P1 + 17) || (G >= P2 && G < P2 + 17) || (G >= P3 && G < P3 + QW3);
                const int sp2 = G < 324 ? 4 : 5;
                if (fixed) last2 = 2 * G;
                else if (!quiet && 2 * G - last2 >= sp2) {
                    int dd = 1 << 20, dh = 1 << 20;
                    if (nd < 162 && (nd / 3 < 12 || (nd / 3 - 11) * 12 <= lastbar + 6)) dd = need_bar(nd / 3) - 40;
                    const int lim = G > C3 ? 54 : G > C2 ? 24 : G > C1 ? 12 : 0;
                    if (nh < lim) dh = first_use(9 + nh / 2) - 45;
                    if (dd < (1 << 20) || dh < (1 << 20)) {
                        if (dd <= dh) { dn[G] = nd; dgap[nd] = G; if (need_bar(nd / 3) <= G) ok = false; ++nd; }
                        else { hl[G] = 18 + nh; hgap[9 + nh / 2] = G; if (first_use(9 + nh / 2) <= G) ok = false; ++nh; }
                        last2 = last2 + sp2 > 2 * G - 1 ? last2 + sp2 : 2 * G - (G < 324 ? 0 : 1);
                        n = 1;
                    }
                }
                n += fixed + s_poll(G) + (G == P4) + (s_pub(G) > 0) + (G == PUB2);
            }
            pre[G + 1] = pre[G] + n;
        }
        if (nd != 162 || nh != 54) ok = false;
    }
};
constexpr Sched SCH{};
static_assert(SCH.ok, "schedule misses a deadline");
constexpr int younger(int a, int b) { return imin(SCH.pre[b] - SCH.pre[a + 1], 63); }
constexpr bool s_bar(int G) { return G >= G1 && (s_ring(G) || s_pub(G) > 0 || G == PUB2); }
constexpr int bar_wait(int G) {
    int n = 63;
    if (s_ring(G)) { int grp = 53; while (need_bar(grp) > G) --grp; n = G == 36 ? 3 : younger(SCH.dgap[3 * grp + 2], G); }
    if (G == PUB2) n = imin(n, younger(slot_end(3) + 6, G));
    if (s_pub(G) > 0) n = imin(n, younger(G == 372 ? 346 : 36 * s_pub(G) + HS1, G));
    return n;
}
constexpr int frag_lds(int f) { return ring_slot(f / 12) * 12288 + (f % 12) * 1024; }

constexpr u32 RESL = ring_slot(45) * 12288;
static_assert(SCH.dgap[161] < 620, "M0");
#define OAUX 0
#define PGX 56
#define PG0 95
#define PG1 192
#define PG2 285
#define PG3 388
#define PG4 517
#define PG5 641
#define PADS 1
constexpr int pad_at(int G) { return G == PG0 ? 0 : G == PG1 ? 1 : G == PG2 ? 2 : G == PG3 ? 3 : G == PG4 ? 4 : G == PG5 ? 5 : -1; }
#define SB() __builtin_amdgcn_sched_barrier(0)
#define MFMA "v_mfma_f32_16x16x32_bf16 "

extern "C" __global__ __attribute__((aligned(4096))) __launch_bounds__(256, 1) void k_mlp(char* __restrict__ ws, u16* __restrict__ out) {
    extern __shared__ __attribute__((aligned(16))) char smem[];
    asm volatile("s_branch 1f\nk_mlp_pad7:\n\ts_setpc_b64 s[30:31]\n1:");
    const int tid = threadIdx.x, lane = tid & 63;
    const int w = __builtin_amdgcn_readfirstlane(tid >> 6);
    const int b = blockIdx.x, xcd = b & 7, rr = b >> 3;
#define MAPV 1
    const int s = rr & 3;
    const int kq = MAPV == 0 ? xcd >> 1 : MAPV == 1 ? 2 * (xcd >> 2) + ((rr >> 2) & 1) : (rr >> 2) & 3;
    const int it = MAPV == 0 ? 8 * (xcd & 1) + (rr >> 2) : MAPV == 1 ? 4 * (xcd & 3) + (rr >> 3) : 2 * xcd + (rr >> 4);
    const int q = 4 * kq + s;
    const auto rs = __builtin_amdgcn_make_buffer_rsrc(ws, 0, WS_END, 0x00020000);
    const u32x4 srd = {(u32)(uintptr_t)ws, (u32)((uintptr_t)ws >> 32) & 0xffffu, WS_END, 0x00020000u};
    const u32 voff = lane * 16;
    const u32 lbase = (u32)(uintptr_t)(LAS char*)smem;
    const u32 wl = lbase + w * 3072;
    auto set_m0 = [&](u32 v) { asm volatile("s_mov_b32 m0, %0" :: "s"(v) : "memory"); };
    auto dma = [&](auto O, u32 so) {
        const u32 vo = voff; const u32x4 sr = srd;
        asm volatile("buffer_load_dwordx4 %0, %1, %2 offen offset:%3 lds" :: "v"(vo), "s"(sr), "s"(so), "n"(O.value * 1024) : "memory");
    };
    auto dma3 = [&](u32 dst, u32 so) {
        asm volatile("s_mov_b32 m0, %3\n\ts_nop 0\n\tbuffer_load_dwordx4 %0, %1, %2 offen lds\n\t"
                     "buffer_load_dwordx4 %0, %1, %2 offen offset:1024 lds\n\tbuffer_load_dwordx4 %0, %1, %2 offen offset:2048 lds"
                     :: "v"(voff), "s"(srd), "s"(so), "s"(dst) : "memory");
    };
    const u32 xsb = WS_X1F + it * 147456;
    const u32 wfb = WS_WF + q * 663552 + w * 3072;
    auto rq_dst = [&](int p) -> u32 {
        const int x = p <= 2 ? p - 1 : p <= 6 ? p - 2 : p - 3;
        return wl + (p == 0 ? 0 : p == 3 ? 12288 : p == 7 ? 24576 : p == 15 ? L_EXF : p > 15 ? ring_slot(p - 13) * 12288 : (3 + x % 9) * 12288);
    };
    auto rq_src = [&](int p) -> u32 {
        const int x = p <= 2 ? p - 1 : p <= 6 ? p - 2 : p - 3;
        return p == 0 ? wfb : p == 3 ? wfb + 12288 : p == 7 ? wfb + 24576 : p == 15 ? WS_EX + q * 8192 + w * 3072
             : p > 15 ? wfb + (p - 13) * 12288 : xsb + w * 3072 + x * 12288;
    };
#pragma unroll
    for (int p = 0; p < XI; ++p) {
        dma3(rq_dst(p), rq_src(p));
    }
    set_m0(rq_dst(XI));
    const u32 hfb = WS_HF + (it * 4 + kq) * 36 * 8192 + w * 2048;
    u32x4 Wf[18], Ef[2];
    const u32 rd0 = lbase + voff;
    const u32 rb[3] = {rd0, rd0 + 49152, rd0 + 98304};
    auto rdw = [&](auto F) {
        constexpr int lo = frag_lds(F.value);
        u32x4& d = Wf[F.value % 18]; const u32 base = rb[lo / 49152];
        asm volatile("ds_read_b128 %0, %1 offset:%2" : "=v"(d) : "v"(base), "n"(lo % 49152) : "memory");
    };
    const u32 rex = lbase + L_EXF + (lane & 15) * 16;
    auto rde = [&](auto C) {
        u32x4& d0 = Ef[0]; u32x4& d1 = Ef[1]; const u32 base = rex;
        asm volatile("ds_read_b128 %0, %2 offset:%3\n\tds_read_b128 %1, %2 offset:%4" : "=&v"(d0), "=&v"(d1) : "v"(base), "n"(C.value * 512), "n"(C.value * 512 + 256) : "memory");
    };
    f32x4 acc[2][4];

    u32x4 Xa[36];
    f32x4 gram[2], rsum[2];
    {
        const u32 a0 = wl + (4 * (lane >> 4) + ((lane & 15) >> 2)) * 64 + (lane & 3) * 8;
        u32x4 ones = {0x3f803f80u, 0x3f803f80u, 0x3f803f80u, 0x3f803f80u};
        asm volatile("" : "+v"(ones));
        u32x2 t[3][4];
        const u32 xb[3] = {a0 + 36864, a0 + 73728, a0 + 110592};
        auto xrd = [&](auto KS, auto I) {
            constexpr int hk = 2 * KS.value + (I.value >> 1), sl = (hk / 3) % 9;
            u32x2& d = t[KS.value % 3][I.value]; const u32 base = xb[sl / 3];
            asm volatile("ds_read_b64_tr_b16 %0, %1 offset:%2" : "=v"(d) : "v"(base), "n"((sl % 3) * 12288 + (hk % 3) * 1024 + (I.value & 1) * 32) : "memory");
        };
        auto admit = [&](auto KS, auto J) {
            constexpr int ks = KS.value, x = (2 * ks + 1) / 3, pos = x <= 1 ? x + 1 : x <= 4 ? x + 2 : x + 3;
            constexpr int issued = 3 * XI + x_dmas(J.value);
            static_assert(issued >= 3 * pos + 3, "X order");
            if constexpr (ks % 6 == 0) asm volatile("s_waitcnt vmcnt(%0)\n\ts_barrier" :: "n"(issued - 3 * pos - 3) : "memory");
            else if constexpr (ks % 3 != 2) asm volatile("s_waitcnt vmcnt(%0)" :: "n"(issued - 3 * pos - 3) : "memory");
        };
        static_for<0, 2>([&](auto KS) {
            admit(KS, IC<0>{});
            static_for<0, 4>([&](auto I) { xrd(KS, I); });
            rdw(IC<2 * KS.value>{}); rdw(IC<2 * KS.value + 1>{});
        });
        u32x4 xv[2];
        static_for<0, 72>([&](auto J) {
            constexpr int j = J.value, ks = j / 4, k = j % 4;
            if constexpr (PADS && j == PGX) asm volatile("s_branch 1f\n\t.p2align 12\nk_mlp_pad6:\n\ts_setpc_b64 s[30:31]\n1:");
            u32x2 (&d)[4] = t[ks % 3];
            u32x4& x0 = xv[0]; u32x4& x1 = xv[1]; u32x4& on = ones;
            if constexpr (k == 0) {
                asm volatile("s_waitcnt lgkmcnt(%4)" : "+v"(d[0]), "+v"(d[1]), "+v"(d[2]), "+v"(d[3]) : "n"(ks < 17 ? 6 : 0) : "memory");
                xv[0] = (u32x4){d[0][0], d[0][1], d[2][0], d[2][1]};
                xv[1] = (u32x4){d[1][0], d[1][1], d[3][0], d[3][1]};
                asm volatile("s_nop 1" : "+v"(x0), "+v"(x1));
            }
            f32x4& c0 = k < 2 ? acc[0][2 * (k & 1)] : k == 2 ? gram[0] : rsum[0];
            f32x4& c1 = k < 2 ? acc[0][2 * (k & 1) + 1] : k == 2 ? gram[1] : rsum[1];
            u32x4& am0 = k < 2 ? Wf[(2 * ks + k) % 18] : k == 2 ? x0 : on;
            u32x4& am1 = k < 2 ? Wf[(2 * ks + k) % 18] : k == 2 ? x1 : on;
            if constexpr (j < 4) asm volatile(MFMA "%0, %1, %2, 0" : "=&v"(c0) : "v"(am0), "v"(x0));
            else asm volatile(MFMA "%0, %1, %2, %0" : "+v"(c0) : "v"(am0), "v"(x0));
            SB();
            if constexpr (ks < 16) {
                if constexpr (k == 0) admit(IC<ks + 2>{}, J);
                if constexpr (k < 2) { xrd(IC<ks + 2>{}, IC<2 * k>{}); xrd(IC<ks + 2>{}, IC<2 * k + 1>{}); }
                else rdw(IC<2 * ks + 2 + k>{});
            }
            SB();
            if constexpr (j < 4) asm volatile(MFMA "%0, %1, %2, 0" : "=&v"(c1) : "v"(am1), "v"(x1));
            else asm volatile(MFMA "%0, %1, %2, %0" : "+v"(c1) : "v"(am1), "v"(x1));
            SB();
            if constexpr (k >= 2) { Xa[2 * ks + k - 2] = xv[k - 2]; asm volatile("" : "+a"(Xa[2 * ks + k - 2])); }
            if constexpr (x_dmas(j + 1) > x_dmas(j)) {
                constexpr int n = x_dmas(j), p = XI + n / 3;
                static_assert(p < 12 || p == 15 || (3 * (p < 15 ? p - 12 : p - 13) + 2) / 2 <= ks, "slot still holds unread x");
                dma(IC<n % 3>{}, rq_src(p));
                if constexpr (n % 3 == 2 && p < 18) set_m0(rq_dst(p + 1));
            }
            SB();
        });
    }
    float ysc[2];
    u32x4 Xe[2];
    {
        asm volatile("s_waitcnt vmcnt(6)\n\ts_barrier" ::: "memory");
        static_for<36, 48>(rdw);
        rde(IC<0>{});
        set_m0(wl + ring_slot(XW) * 12288);
        SB();
    }
    float sq[2], mean[2];
    auto stats = [&](auto ST) {
        constexpr int st = ST.value >> 1, t = ST.value & 1;
        if constexpr (st == 0) {
            const float a = (lane & 1) ? gram[t][1] : gram[t][0], c = (lane & 1) ? gram[t][3] : gram[t][2];
            float v = (lane & 2) ? c : a;
            const u32 src = ((lane & 15) + 4 * (lane & 12)) * 4;
            float& d = sq[t];
            asm volatile("ds_bpermute_b32 %0, %1, %2" : "=v"(d) : "v"(src), "v"(v) : "memory");
            mean[t] = rsum[t][0] * (1.f / DM);
            asm volatile("" : "+v"(mean[t]));
        }
        if constexpr (st == 1) { asm volatile("" : "+v"(sq[t])); sq[t] = rbf(rbf(fmaxf(fmaf(-mean[t], mean[t], sq[t] * (1.f / DM)), 0.f)) + 1e-5f); asm volatile("" : "+v"(sq[t])); }
        if constexpr (st == 2) { sq[t] = rbf(__builtin_amdgcn_sqrtf(sq[t])); ysc[t] = -1.44269504088896340736f * __builtin_amdgcn_rcpf(sq[t]); asm volatile("" : "+v"(sq[t]), "+v"(ysc[t])); }
        if constexpr (st == 3) {
            const bool lo = lane < 16;
            Xe[t] = (u32x4){lo ? pkbf(mean[t], mean[t]) : 0u, lo ? pkbf(mean[t], sq[t]) : 0u, lo ? pkbf(sq[t], sq[t]) : 0u, 0u};
            asm volatile("" : "+v"(Xe[t]));
        }
    };
    auto corr = [&](auto C) {
        f32x4 (&a)[4] = acc[C.value & 1]; u32x4 (&e)[2] = Ef; u32x4 (&x)[2] = Xe;
        asm volatile(MFMA "%0, %4, %6, %0\n\t" MFMA "%1, %4, %7, %1\n\t" MFMA "%2, %5, %6, %2\n\t" MFMA "%3, %5, %7, %3"
                     : "+v"(a[0]), "+v"(a[1]), "+v"(a[2]), "+v"(a[3]) : "v"(e[0]), "v"(e[1]), "v"(x[0]), "v"(x[1]));
    };

    f32x4 acc2[5][4];
    float yv[16], ev[16];
    u32x4 H[36][2], pollv;
    auto flag_store = [&](u32 level, u32 so) {
        asm volatile("s_mov_b64 exec, 1\n\tbuffer_store_dword %0, off, %1, %2 sc1\n\ts_mov_b64 exec, -1" :: "v"(level), "s"(srd), "s"(so) : "memory");
    };
    auto act = [&](auto C, auto HS, auto PT) {
        constexpr int t = HS.value / 2, e = C.value, pt = PT.value;
        if constexpr (HS.value % 2 == 0) {
            if constexpr (pt != 1 && t < 16) { yv[t] = acc[e & 1][2 * ((t >> 2) & 1) + (t >> 3)][t & 3] * ysc[t >> 3]; asm volatile("" :: "v"(yv[t])); }
            if constexpr (pt != 0 && t >= 2) { yv[t - 2] = yv[t - 2] * ev[t - 2]; asm volatile("" :: "v"(yv[t - 2])); }
            if constexpr (pt != 0 && t < 16) { ev[t] = __builtin_amdgcn_exp2f(yv[t]); asm volatile("" :: "v"(ev[t])); }
        } else {
            if constexpr (pt != 1 && t >= 1 && t < 17) { ev[t - 1] = 1.f + ev[t - 1]; asm volatile("" :: "v"(ev[t - 1])); }
            if constexpr (pt != 0 && t >= 3 && (t & 1)) {
                u32 pk = pkbf(yv[t - 3], yv[t - 2]);
                asm volatile("" :: "v"(pk));
                H[e][(t - 3) / 8][((t - 3) / 2) & 3] = pk;
            }
            if constexpr (pt != 0 && t >= 1 && t < 17) { ev[t - 1] = __builtin_amdgcn_rcpf(ev[t - 1]); asm volatile("" :: "v"(ev[t - 1])); }
        }
    };
    const u32 hown = hfb + 9 * s * 8192;
    const u32 hpeer[3] = {hfb + 9 * ((s + 1) & 3) * 8192, hfb + 9 * ((s + 2) & 3) * 8192, hfb + 9 * ((s + 3) & 3) * 8192};
    auto hload = [&](auto E, auto HF) {
        constexpr int e = E.value, hf = HF.value, k = e - 9;
        const u32 so = hpeer[k % 3] + (k / 3) * 8192, vo = voff; const u32x4 sr = srd;
        u32x4& d = H[e][hf];
        asm volatile("buffer_load_dwordx4 %0, %1, %2, %3 offen offset:%4 sc1" : "=a"(d) : "v"(vo), "s"(sr), "s"(so), "n"(hf * 1024) : "memory");
    };
    const u32 vzero = 0;
    const u32 fso1 = WS_FLAG + (it * 4 + kq) * 64;
    auto poll = [&]() {
        asm volatile("buffer_load_dwordx4 %0, %1, %2, %3 offen sc1" : "=v"(pollv) : "v"(vzero), "s"(srd), "s"(fso1) : "memory");
    };
    auto check = [&](u32 level) {
        asm volatile("" : "+v"(pollv));
#pragma nounroll
        for (u32 spins = 0; spins < (1u << 20); ++spins) {
            const bool bad = pollv[0] < level || pollv[1] < level || pollv[2] < level || pollv[3] < level;
            if (__builtin_amdgcn_ballot_w64(bad) == 0ull) break;
            __builtin_amdgcn_s_sleep(1);
            asm volatile("buffer_load_dwordx4 %0, %1, %2, %3 offen sc1\n\ts_waitcnt vmcnt(0)"
                         : "=v"(pollv) : "v"(vzero), "s"(srd), "s"(fso1) : "memory");
        }
    };
    const u32 pgb = WS_PART + (it * 4 + s) * 98304 + w * 2048;
    const u32 phb = WS_PH + (it * 4 + s) * 16384 + w * 4096;
    auto xfrag = [&](int ks) -> u32 { return xsb + ((ks / 3) * 12 + 3 * w + ks % 3) * 1024; };
    const u32 fso2 = WS_FLAG + 4096 + (it * 4 + s) * 64;
    u32 fv;
    const u32 vfl = (lane & 3) * 4;
    auto poll2 = [&]() {
        asm volatile("buffer_load_dword %0, %1, %2, %3 offen sc1" : "=v"(fv) : "v"(vfl), "s"(srd), "s"(fso2) : "memory");
    };
    auto check2 = [&]() {
        asm volatile("" : "+v"(fv));
#pragma nounroll
        for (u32 spins = 0; spins < (1u << 20); ++spins) {
            if (__builtin_amdgcn_ballot_w64(fv == 0u) == 0ull) break;
            __builtin_amdgcn_s_sleep(1);
            asm volatile("buffer_load_dword %0, %1, %2, %3 offen sc1\n\ts_waitcnt vmcnt(0)" : "=v"(fv) : "v"(vfl), "s"(srd), "s"(fso2) : "memory");
        }
    };
    static_for<G1, GE>([&](auto GG) {
        constexpr int G = GG.value, c = G / 36, g = G % 36, m = G - 324;
        if constexpr (PADS && pad_at(G) >= 0) asm volatile("s_branch 1f\n\t.p2align 12\nk_mlp_pad%c0:\n\ts_setpc_b64 s[30:31]\n1:" :: "n"(pad_at(G)));
        if constexpr (s_bar(G)) {
            if constexpr (G % 6 == 0) asm volatile("s_waitcnt vmcnt(%0) lgkmcnt(%1)\n\ts_barrier" :: "n"(bar_wait(G)), "n"(G == G1 ? 8 : 6) : "memory");
            else asm volatile("s_waitcnt vmcnt(%0)\n\ts_barrier" :: "n"(bar_wait(G)) : "memory");
        } else if constexpr (G % 6 == 0) asm volatile("s_waitcnt lgkmcnt(%0)" :: "n"(G == 42 ? 4 : G + 6 < GE ? 6 : 0) : "memory");
        u32x4& wf = Wf[G % 18];
        constexpr int sl = G < 324 ? 0 : m_slot(m), e = G < 324 ? 0 : m_ent(m), tl = G < 324 ? (g & 1) : m_half(m);
        constexpr bool first = G < 324 ? g < 2 : m_first(m), own = G >= 324 && ent_own(e);
        if constexpr (G >= 324) {
            if constexpr (ent_first(m) && !own) asm volatile("s_waitcnt vmcnt(%0)" :: "n"(younger(SCH.hgap[e], G)) : "memory");
        }
        auto mm = [&](auto TT) {
            constexpr int tt = TT.value;
            f32x4& ac = G < 324 ? acc[c & 1][2 * tl + tt] : acc2[sl][2 * tl + tt];
            u32x4& a = wf; u32x4& bo = G < 324 ? Xa[(g & ~1) + tt] : H[e][tt];
            if constexpr (first && own) asm volatile(MFMA "%0, %1, %2, 0" : "=&v"(ac) : "v"(a), "v"(bo));
            else if constexpr (first) asm volatile(MFMA "%0, %1, %2, 0" : "=&v"(ac) : "v"(a), "a"(bo));
            else if constexpr (own) asm volatile(MFMA "%0, %1, %2, %0" : "+v"(ac) : "v"(a), "v"(bo));
            else asm volatile(MFMA "%0, %1, %2, %0" : "+v"(ac) : "v"(a), "a"(bo));
            SB();
        };
        constexpr int hs = G >= 72 && G < 324 && g >= 1 ? (g < 12 ? g - 1 : g) : -1;
        mm(IC<0>{});
        if constexpr (G + 12 < GE) rdw(IC<G + 12>{});
        if constexpr (hs >= 0) act(IC<c - 1>{}, IC<hs>{}, IC<0>{});
        if constexpr (G >= 52 && G < 70) act(IC<0>{}, IC<2 * (G - 52)>{}, IC<2>{});
        if constexpr (G >= 326 && G < 344) act(IC<8>{}, IC<2 * (G - 326)>{}, IC<2>{});
        SB();
        mm(IC<1>{});
        if constexpr (G == 50) { corr(IC<0>{}); SB(); rde(IC<1>{}); }
        if constexpr (G < 324 && g == (c == 1 ? 30 : 11)) {
            if constexpr (c > 1) { act(IC<c - 1>{}, IC<10>{}, IC<1>{}); SB(); }
            corr(IC<c>{}); SB();
            if constexpr (c > 1) act(IC<c - 1>{}, IC<11>{}, IC<2>{});
        } else if constexpr (hs >= 0) act(IC<c - 1>{}, IC<hs>{}, IC<1>{});
        if constexpr (g == (c == 1 ? 32 : 14) && c < 8) rde(IC<c + 1>{});
        if constexpr (SCH.dn[G] >= 0) {
            dma(IC<SCH.dn[G] % 3>{}, wfb + (SCH.dn[G] / 3) * 12288);
            if constexpr (SCH.dn[G] % 3 == 2 && SCH.dn[G] < 161) set_m0(wl + ring_slot(SCH.dn[G] / 3 + 1) * 12288);
        }
        if constexpr (G == 36 || G == 37) stats(IC<G - 36>{});
        if constexpr (G >= 43 && G < 49) stats(IC<G - 41>{});
        if constexpr (G >= 52 && G < 70) act(IC<0>{}, IC<2 * (G - 52) + 1>{}, IC<2>{});
        if constexpr (G >= 326 && G < 344) act(IC<8>{}, IC<2 * (G - 326) + 1>{}, IC<2>{});
        if constexpr (s_hstore(G) >= 0) {
            constexpr int hf = s_hstore(G), cc = c - 1;
            __builtin_amdgcn_raw_buffer_store_b128(H[cc][hf], rs, voff + hf * 1024, hown + cc * 8192, 16);
        }
        if constexpr (s_pub(G) > 0) flag_store(s_pub(G), fso1 + s * 4);
        if constexpr (G == PUB2) flag_store(1u, fso2 + kq * 4);
        if constexpr (G == C1 || G == C2 || G == C3) {
            asm volatile("s_waitcnt vmcnt(%0)" :: "n"(younger(G == C1 ? P1 : G == C2 ? P2 : P3, G)) : "memory");
            check(G == C1 ? 2 : G == C2 ? 4 : 9);
        }
        if constexpr (G == P1 || G == P2 || G == P3) poll();
        if constexpr (G == P4) poll2();
        if constexpr (SCH.hl[G] >= 0) hload(IC<SCH.hl[G] / 2>{}, IC<SCH.hl[G] % 2>{});
        if constexpr (s_pstore(G) >= 0) {
            constexpr int sg = s_pstore(G) / 2, pt = s_pstore(G) % 2;
            const f32x4 a0 = acc2[sg][2 * pt], a1 = acc2[sg][2 * pt + 1];
            const u32x4 v = {pkbf(a0[0], a0[1]), pkbf(a0[2], a0[3]), pkbf(a1[0], a1[1]), pkbf(a1[2], a1[3])};
            if constexpr (sg == 0) __builtin_amdgcn_raw_buffer_store_b128(v, rs, voff, phb + kq * 1024, 16);
            else __builtin_amdgcn_raw_buffer_store_b128(v, rs, voff + pt * 1024, pgb + ((kq + sg) & 3) * 24576 + (sg - 1) * 8192, 16);
        }
        if constexpr (s_res(G)) {
            constexpr int i = (G - 620) / 2;
            set_m0(wl + RESL + i * 1024);
            const u32 so = xfrag(9 * s + (i < 2 ? 2 * kq + i : 8));
            asm volatile("s_nop 0" ::: "memory");
            dma(IC<0>{}, so);
        }
        SB();
    });

    {
        u32x2 xr[5];
        f32x4 gb[2];
        f32x2 gh;
        const u32 ra = wl + RESL + (4 * (lane >> 4) + ((lane & 15) >> 2)) * 64 + (lane & 3) * 8;
        const u32 ga = lbase + L_EXF + 4608 + (lane >> 4) * 16;
        asm volatile("s_waitcnt vmcnt(1)" ::: "memory");
        asm volatile("ds_read_b64_tr_b16 %0, %5\n\tds_read_b64_tr_b16 %1, %5 offset:32\n\tds_read_b64_tr_b16 %2, %5 offset:1024\n\t"
                     "ds_read_b64_tr_b16 %3, %5 offset:1056\n\tds_read_b64_tr_b16 %4, %6 offset:2048"
                     : "=&v"(xr[0]), "=&v"(xr[1]), "=&v"(xr[2]), "=&v"(xr[3]), "=&v"(xr[4]) : "v"(ra), "v"(ra + (kq >> 1) * 32) : "memory");
        asm volatile("ds_read_b128 %0, %3\n\tds_read_b128 %1, %3 offset:64\n\tds_read_b64 %2, %4 offset:128\n\ts_waitcnt vmcnt(0) lgkmcnt(0)"
                     : "=&v"(gb[0]), "=&v"(gb[1]), "=&v"(gh) : "v"(ga), "v"(ga + (kq & 1) * 8) : "memory");
        asm volatile("" : "+v"(xr[0]), "+v"(xr[1]), "+v"(xr[2]), "+v"(xr[3]), "+v"(xr[4]));
        check2();
        u32x4 P[6];
        u32 Q[4];
        const u32 pin = pgb + kq * 24576;
        asm volatile("buffer_load_dwordx4 %4, %10, %11, %14 offen sc1\n\tbuffer_load_dwordx4 %5, %10, %11, %14 offen offset:1024 sc1\n\t"
                     "buffer_load_dwordx4 %2, %10, %11, %13 offen sc1\n\tbuffer_load_dwordx4 %3, %10, %11, %13 offen offset:1024 sc1\n\t"
                     "buffer_load_dwordx4 %0, %10, %11, %12 offen sc1\n\tbuffer_load_dwordx4 %1, %10, %11, %12 offen offset:1024 sc1\n\t"
                     "buffer_load_dword %6, %15, %11, %16 offen sc1\n\tbuffer_load_dword %7, %15, %11, %16 offen offset:1024 sc1\n\t"
                     "buffer_load_dword %8, %15, %11, %16 offen offset:2048 sc1\n\tbuffer_load_dword %9, %15, %11, %16 offen offset:3072 sc1\n\t"
                     "s_waitcnt vmcnt(0)"
                     : "=&v"(P[0]), "=&v"(P[1]), "=&v"(P[2]), "=&v"(P[3]), "=&v"(P[4]), "=&v"(P[5]), "=&v"(Q[0]), "=&v"(Q[1]), "=&v"(Q[2]), "=&v"(Q[3])
                     : "v"(voff), "s"(srd), "s"(pin), "s"(pin + 8192), "s"(pin + 16384), "v"(voff + kq * 4), "s"(phb) : "memory");
        const auto ro = __builtin_amdgcn_make_buffer_rsrc(out, 0, SEQ * DM * 2, 0x00020000);
        const u32 ov = ((it * 128 + w * 32 + (lane & 15)) * DM + s * 144 + 4 * (lane >> 4)) * 2;
        auto fin2 = [&](float a0, float a1, float g0, float g1, u32 xk) -> u32 {
            const u32 t = pkbf(a0 * -0.69314718055994530942f, a1 * -0.69314718055994530942f);
            const u32 gz = pkbf(bflo(t) * g0, bfhi(t) * g1);
            return pkbf(bflo(xk) + bflo(gz), bfhi(xk) + bfhi(gz));
        };
#pragma unroll
        for (int k = 0; k < 4; ++k) {
            u32x2 o;
#pragma unroll
            for (int j = 0; j < 2; ++j) {
                const int d = 2 * (k & 1) + j;
                const u32 p0 = P[k >> 1][d], p1 = P[2 + (k >> 1)][d], p2 = P[4 + (k >> 1)][d], xk = xr[k][j];
                o[j] = fin2(acc2[4][k][2 * j] + bflo(p0) + bflo(p1) + bflo(p2), acc2[4][k][2 * j + 1] + bfhi(p0) + bfhi(p1) + bfhi(p2),
                            gb[k >> 1][2 * j], gb[k >> 1][2 * j + 1], xk);
            }
            __builtin_amdgcn_raw_buffer_store_b64(o, ro, ov + (k >> 1) * 32, kq * 64 + (k & 1) * (16 * DM * 2), OAUX);
        }
        const u32 oh = fin2(bflo(Q[0]) + bflo(Q[1]) + bflo(Q[2]) + bflo(Q[3]), bfhi(Q[0]) + bfhi(Q[1]) + bfhi(Q[2]) + bfhi(Q[3]),
                            gh[0], gh[1], (kq & 1) ? xr[4][1] : xr[4][0]);
        __builtin_amdgcn_raw_buffer_store_b32(oh, ro, ov + 256, (kq & 1) * 4 + (kq >> 1) * (16 * DM * 2), OAUX);
    }
    asm volatile("k_mlp_end:");
}
#if defined(__HIP_DEVICE_COMPILE__)
#pragma clang attribute pop
#endif

extern "C" void kernel_launch(void* const* d_in, const int* in_sizes, int n_in, void* d_out, int out_size,
                              void* d_ws, size_t ws_size, hipStream_t stream) {
    const u16* x = (const u16*)d_in[0];
    char* ws = (char*)d_ws;
    (void)hipFuncSetAttribute((const void*)k_mlp, hipFuncAttributeMaxDynamicSharedMemorySize, MLP_LDS);
    k_prep<<<NB_PREP, 256, PREP_LDS, stream>>>(x, (const u16*)d_in[1], (const u16*)d_in[2], (const u16*)d_in[3],
                                               (const u16*)d_in[4], (const u16*)d_in[5], (const u16*)d_in[9],
                                               (const u16*)d_in[10], (const u16*)d_in[11], ws);
    k_conv<<<NB_PF + NB_CONV + NB_W1, 256, CONV_LDS, stream>>>((const u16*)d_in[6], (const u16*)d_in[8], ws);
    k_mlp<<<256, 256, MLP_LDS, stream>>>(ws, (u16*)d_out);
}
```
